```python
import torch

def _softmax(x, axis=-1):
    return torch.softmax(x, dim=axis)
def _gelu(x):
    return torch.nn.functional.gelu(x, approximate="tanh")
def _silu(x):
    return torch.nn.functional.silu(x)
def _sigmoid(x):
    return torch.sigmoid(x)
def _layernorm(x, axis=-1, eps=1e-5):
    m = x.mean(dim=axis, keepdim=True)
    v = x.var(dim=axis, keepdim=True, unbiased=False)
    return (x - m) / torch.sqrt(v + eps)
def _scatter_add(tgt, idx, src, axis=0):
    return tgt.clone().index_add_(axis, idx, src)
def _segment_sum(src, seg, n):
    out = torch.zeros((n,) + src.shape[1:], dtype=src.dtype)
    return out.index_add_(0, seg, src)
def _pad(x, pw):
    flat = tuple(v for pair in reversed(pw) for v in pair)
    return torch.nn.functional.pad(x, flat)

kernel_name = 'sequential_preLN__mha_block__mixer_channel_mlp__prompt'

def setup_inputs():

    _w = (lambda a: (lambda t: t.to(torch.bfloat16) if t.is_floating_point() else t)(torch.as_tensor(a)))
    def _A_setup():
        B, S, D, H = 1, 2048, 640, 32
        return {
            "x": _w((lambda *s: torch.randn(*s, dtype=torch.bfloat16) * 0.05)(B, S, D)),
            "wqkv": _w((lambda *s: torch.randn(*s, dtype=torch.bfloat16) * 0.05)(D, 3 * D)),
            "wo": _w((lambda *s: torch.randn(*s, dtype=torch.bfloat16) * 0.05)(D, D)),
        }
    _A_in = _A_setup()

    def _B_setup():
        B, S, D, F = 1, 2048, 640, 1728
        return {
            "x": _w((lambda *s: torch.randn(*s, dtype=torch.bfloat16) * 0.05)(B, S, D)),
            "w1": _w((lambda *s: torch.randn(*s, dtype=torch.bfloat16) * 3.2)(D, F) * (D ** -0.5)),
            "b1": _w((lambda *s: torch.randn(*s, dtype=torch.bfloat16) * 1.6)(F)),
            "w2": _w((lambda *s: torch.randn(*s, dtype=torch.bfloat16) * 0.2)(F, D) * (F ** -0.5)),
            "b2": _w((lambda *s: torch.randn(*s, dtype=torch.bfloat16) * 0.4)(D)),
        }
    _B_in = _B_setup()

    return {
        "A_x": _A_in["x"],
        "A_wqkv": _A_in["wqkv"],
        "A_wo": _A_in["wo"],
        "B_x": _B_in["x"],
        "B_w1": _B_in["w1"],
        "B_b1": _B_in["b1"],
        "B_w2": _B_in["w2"],
        "B_b2": _B_in["b2"],
    }

def reference(A_x, A_wqkv, A_wo, B_x, B_w1, B_b1, B_w2, B_b2):
    def _A_ref(x, wqkv, wo):
        B, S, D = x.shape
        H = 32
        Dh = D // H
        qkv = (lambda x, *d: x.permute(*d))((x @ wqkv).reshape(B, S, 3, H, Dh), 2, 0, 3, 1, 4)
        q, k, v = qkv[0], qkv[1], qkv[2]
        scores = torch.einsum("bhqd,bhkd->bhqk", q, k) * (Dh ** -0.5)
        p = _softmax(scores, axis=-1)
        o = torch.einsum("bhqk,bhkd->bhqd", p, v)
        return (lambda x, *d: x.permute(*d))(o, 0, 2, 1, 3).reshape(B, S, D) @ wo

    def _B_ref(x, w1, b1, w2, b2):
        y = _layernorm(x, axis=-1)
        h = _gelu(torch.einsum("bsd,df->bsf", y, w1) + b1)
        return torch.einsum("bsf,fd->bsd", h, w2) + b2

    x = A_x
    h = x + _A_ref(_layernorm(x, axis=-1), A_wqkv, A_wo)
    h2 = h + _B_ref(_layernorm(h, axis=-1), B_w1, B_b1, B_w2, B_b2)
    return h2
```

```cpp
#include <hip/hip_runtime.h>
#include <hip/hip_bf16.h>
typedef unsigned short u16;
constexpr int S = 2048, D = 640, H = 32, DH = 20, F = 1728;
__device__ __forceinline__ float bf2f(u16 v) { return __uint_as_float((unsigned)v << 16); }
__device__ __forceinline__ u16 f2bf(float f) { unsigned u = __float_as_uint(f); u += 0x7fffu + ((u >> 16) & 1u); return (u16)(u >> 16); }

__global__ void ln_k(const u16* x, u16* y, int twice) {
  int row = blockIdx.x, t = threadIdx.x; __shared__ float red[2][64];
  float v[10]; float s = 0;
  for (int i = 0; i < 10; ++i) { v[i] = bf2f(x[row * D + t + 64 * i]); s += v[i]; }
  for (int p = 0; p <= twice; ++p) {
    s = 0; for (int i = 0; i < 10; ++i) s += v[i];
    for (int o = 32; o; o >>= 1) s += __shfl_xor(s, o);
    float m = s / D, q = 0; for (int i = 0; i < 10; ++i) q += (v[i] - m) * (v[i] - m);
    for (int o = 32; o; o >>= 1) q += __shfl_xor(q, o);
    float r = rsqrtf(q / D + 1e-5f);
    for (int i = 0; i < 10; ++i) v[i] = bf2f(f2bf((v[i] - m) * r));
  }
  for (int i = 0; i < 10; ++i) y[row * D + t + 64 * i] = f2bf(v[i]);
}
__global__ void gemm_k(const u16* A, const u16* W, const u16* bias, const u16* res, u16* C, int N, int K, int act) {
  int n = blockIdx.x * 64 + threadIdx.x, m0 = blockIdx.y * 8;
  float acc[8] = {0};
  for (int k = 0; k < K; ++k) { float w = bf2f(W[k * N + n]); for (int i = 0; i < 8; ++i) acc[i] += bf2f(A[(m0 + i) * K + k]) * w; }
  for (int i = 0; i < 8; ++i) { float v = acc[i]; if (bias) v += bf2f(bias[n]);
    if (act) { float u = 0.7978845608f * (v + 0.044715f * v * v * v); v = 0.5f * v * (1.f + tanhf(u)); }
    if (res) v = bf2f(f2bf(v)) + bf2f(res[(m0 + i) * N + n]);
    C[(m0 + i) * N + n] = f2bf(v); }
}
__global__ void attn_k(const u16* qkv, u16* o) {
  int q = blockIdx.x * 64 + threadIdx.x, h = blockIdx.y;
  float qv[DH], acc[DH]; for (int d = 0; d < DH; ++d) { qv[d] = bf2f(qkv[q * 3 * D + h * DH + d]) * 0.22360679775f; acc[d] = 0; }
  float m = -1e30f, l = 0;
  for (int k = 0; k < S; ++k) { float s = 0; for (int d = 0; d < DH; ++d) s += qv[d] * bf2f(qkv[k * 3 * D + D + h * DH + d]);
    float mn = fmaxf(m, s), a = __expf(m - mn), p = __expf(s - mn); l = l * a + p; m = mn;
    for (int d = 0; d < DH; ++d) acc[d] = acc[d] * a + p * bf2f(qkv[k * 3 * D + 2 * D + h * DH + d]); }
  for (int d = 0; d < DH; ++d) o[q * D + h * DH + d] = f2bf(acc[d] / l);
}
extern "C" void kernel_launch(void* const* d_in, const int* in_sizes, int n_in, void* d_out, int out_size, void* d_ws, size_t ws_size, hipStream_t stream) {
  const u16* x = (const u16*)d_in[0]; const u16* wqkv = (const u16*)d_in[1]; const u16* wo = (const u16*)d_in[2];
  const u16* w1 = (const u16*)d_in[4]; const u16* b1 = (const u16*)d_in[5]; const u16* w2 = (const u16*)d_in[6]; const u16* b2 = (const u16*)d_in[7];
  u16* ws = (u16*)d_ws; u16* xn = ws; u16* qkv = xn + S * D; u16* o = qkv + S * 3 * D; u16* h = o + S * D; u16* y = h + S * D; u16* h1 = y + S * D;
  ln_k<<<S, 64, 0, stream>>>(x, xn, 0);
  gemm_k<<<dim3(3 * D / 64, S / 8), 64, 0, stream>>>(xn, wqkv, nullptr, nullptr, qkv, 3 * D, D, 0);
  attn_k<<<dim3(S / 64, H), 64, 0, stream>>>(qkv, o);
  gemm_k<<<dim3(D / 64, S / 8), 64, 0, stream>>>(o, wo, nullptr, x, h, D, D, 0);
  ln_k<<<S, 64, 0, stream>>>(h, y, 1);
  gemm_k<<<dim3(F / 64, S / 8), 64, 0, stream>>>(y, w1, b1, nullptr, h1, F, D, 1);
  gemm_k<<<dim3(D / 64, S / 8), 64, 0, stream>>>(h1, w2, b2, h, (u16*)d_out, D, F, 0);
}
```

```cpp
#include <hip/hip_runtime.h>
#include <hip/hip_bf16.h>
typedef unsigned short u16;
typedef __attribute__((ext_vector_type(8))) short bf16x8;
typedef __attribute__((ext_vector_type(8))) __bf16 bf8;
typedef __attribute__((ext_vector_type(4))) short s16x4;
typedef __attribute__((ext_vector_type(16))) float f32x16;
typedef __attribute__((ext_vector_type(4))) float f32x4;
typedef __attribute__((ext_vector_type(2))) float f32x2;
typedef __attribute__((ext_vector_type(4))) unsigned u32x4;
typedef __attribute__((ext_vector_type(2))) unsigned u32x2;
typedef __attribute__((ext_vector_type(2))) __bf16 bf2;
#define LAS __attribute__((address_space(3)))
#define DI __device__ __forceinline__
#define SBAR() __builtin_amdgcn_sched_barrier(0)
#define LGKM0() __builtin_amdgcn_s_waitcnt(0xC07F)
#define BAR() asm volatile("s_barrier" ::: "memory")
#define WAITV(n) asm volatile("s_waitcnt vmcnt(%0)" :: "i"(n) : "memory")

constexpr int S = 2048, D = 640, H = 32, DH = 20, F = 1728;
constexpr int QT = 1280, KVT = 2688;
constexpr size_t WS_Q = 0, WS_KV = WS_Q + (size_t)H * 64 * QT, WS_O = WS_KV + (size_t)H * 64 * KVT,
                 WS_H = WS_O + (size_t)S * D * 2, WS_H1 = WS_H + (size_t)S * D * 2,
                 WS_WO = WS_H1 + (size_t)S * F * 2, WS_W1 = WS_WO + (size_t)D * D * 2, WS_W2 = WS_W1 + (size_t)D * F * 2 + 20480,
                 WS_CS1 = WS_W2 + (size_t)D * F * 2, WS_ST = WS_CS1 + 4 * 1792 * 4, WS_END = WS_ST + (size_t)S * 8 * 8;
#define STAMPT(k, s) do {} while (0)
constexpr int KSKEW = 1;
constexpr float QSCALE = 0.22360679774997896f * 1.4426950408889634f;

DI float bf2f(u16 v) { return __uint_as_float((unsigned)v << 16); }
DI float bflo(unsigned u) { return __uint_as_float(u << 16); }
DI float bfhi(unsigned u) { return __uint_as_float(u & 0xffff0000u); }
DI unsigned pk2(float lo, float hi) { f32x2 v = {lo, hi}; return __builtin_bit_cast(unsigned, __builtin_convertvector(v, bf2)); }
DI void glds16(unsigned voff, const void* sbase, unsigned lds) {
  asm volatile("s_mov_b32 m0, %2\n\ts_nop 0\n\tglobal_load_lds_dwordx4 %0, %1" :: "v"(voff), "s"(sbase), "s"(__builtin_amdgcn_readfirstlane(lds)) : "memory"); }
DI s16x4 trread(const LAS char* p) { return __builtin_amdgcn_ds_read_tr16_b64_v4i16((LAS s16x4*)p); }
DI bf16x8 cat8(s16x4 a, s16x4 b) { return __builtin_shufflevector(a, b, 0, 1, 2, 3, 4, 5, 6, 7); }
#define MFMA32(a, b, c) __builtin_amdgcn_mfma_f32_32x32x16_bf16(__builtin_bit_cast(bf8, a), __builtin_bit_cast(bf8, b), c, 0, 0, 0)
#define MFMA32K8(a, b, c) __builtin_amdgcn_mfma_f32_32x32x8bf16_1k(a, b, c, 0, 0, 0)
#define MFMA16(a, b, c) __builtin_amdgcn_mfma_f32_16x16x32_bf16(__builtin_bit_cast(bf8, a), __builtin_bit_cast(bf8, b), c, 0, 0, 0)
DI float xhalf_sum(float v) { auto r = __builtin_amdgcn_permlane32_swap(__float_as_uint(v), __float_as_uint(v), false, false); return __uint_as_float(r[0]) + __uint_as_float(r[1]); }
DI float xhalf_max(float v) { auto r = __builtin_amdgcn_permlane32_swap(__float_as_uint(v), __float_as_uint(v), false, false); return fmaxf(__uint_as_float(r[0]), __uint_as_float(r[1])); }
DI float xq_sum(float v) { auto r = __builtin_amdgcn_permlane16_swap(__float_as_uint(v), __float_as_uint(v), false, false); return xhalf_sum(__uint_as_float(r[0]) + __uint_as_float(r[1])); }
typedef __amdgpu_buffer_rsrc_t rsrc_t;
DI rsrc_t mkrsrc(const void* p, unsigned bytes) { return __builtin_amdgcn_make_buffer_rsrc((void*)p, 0, bytes, 0x00020000); }
constexpr int XAUX = 0;
DI bf16x8 ldfrag(rsrc_t r, unsigned voff, unsigned soff) { return __builtin_bit_cast(bf16x8, __builtin_amdgcn_raw_buffer_load_b128(r, voff, soff, XAUX)); }
DI void st16(rsrc_t r, unsigned voff, u32x4 v) { __builtin_amdgcn_raw_buffer_store_b128(v, r, voff, 0, XAUX); }
DI void st8(rsrc_t r, unsigned voff, u32x2 v) { __builtin_amdgcn_raw_buffer_store_b64(v, r, voff, 0, XAUX); }

#define K1_NSLOT 4
constexpr int K1_STAGE = 32768, K1_LDS = K1_NSLOT * K1_STAGE + 2048;
#define X_NOSUM 0
struct F4 { bf16x8 a0, a1, b0, b1; };
template <bool isV> DI void k1_compute(char* __restrict__ ws, char* smem, int bm, int bn, int wid) {
  const int lane = threadIdx.x & 63, r = lane & 31, hh = lane >> 5;
  const int wm = wid >> 1, wn = wid & 1;
  f32x16 c00 = f32x16{}, c01 = f32x16{}, c10 = f32x16{}, c11 = f32x16{};
  f32x4 rs4 = f32x4{}, cs4 = f32x4{};
  const int fA = (r >> 1) & 7;
  const int gi = lane >> 4, q = (lane & 15) >> 2, p = lane & 3;
  const LAS char* sm = (const LAS char*)smem;
  const int aoff = (wm * 64 + wn * 32 + r) * 128;
  const int boff = 16384 + (8 * (gi >> 1) + q) * 256 + 32 * (gi & 1) + 8 * p;
  const int b0off = boff + (((2 * wn + wm) ^ q) << 6), b1off = boff + (((2 * wn + (1 ^ wm)) ^ q) << 6);
  const unsigned sel1 = ((lane & 15) == (gi & 1)) ? 0x3F803F80u : 0u;
  const bf16x8 sel = __builtin_bit_cast(bf16x8, u32x4{sel1, sel1, sel1, sel1});
#define K1_LDA0(X, sb, s) X.a0 = *(const LAS bf16x8*)(sb + aoff + (((2 * (s) + hh) ^ fA) << 4))
#define K1_LDA1(X, sb, s) X.a1 = *(const LAS bf16x8*)(sb + (aoff ^ 4096) + (((2 * (s) + hh) ^ fA) << 4))
#define K1_LDB0(X, sb, s) X.b0 = cat8(trread(sb + b0off + (s) * 4096), trread(sb + b0off + (s) * 4096 + 1024))
#define K1_LDB1(X, sb, s) X.b1 = cat8(trread(sb + b1off + (s) * 4096), trread(sb + b1off + (s) * 4096 + 1024))
#define K1_MM(c, a, b) c = isV ? MFMA32(a, b, c) : MFMA32(b, a, c)
#define K1_STEP(X, Y, sb, s, LD) do { \
    K1_MM(c00, X.a0, X.b0); SBAR(); if (LD) { K1_LDA0(Y, sb, s); K1_LDB0(Y, sb, s); } SBAR(); \
    K1_MM(c01, X.a0, X.b1); SBAR(); if (LD) { K1_LDB1(Y, sb, s); } SBAR(); \
    K1_MM(c10, X.a1, X.b0); SBAR(); if (LD) { K1_LDA1(Y, sb, s); } SBAR(); \
    K1_MM(c11, X.a1, X.b1); SBAR(); \
    if (!X_NOSUM) { rs4 = MFMA16(sel, X.a0, rs4); cs4 = MFMA16(sel, X.b0, cs4); } SBAR(); } while (0)
  F4 x, y;
  BAR();
  STAMPT(0, 1);
  K1_LDA0(x, sm, 0); K1_LDB0(x, sm, 0); K1_LDB1(x, sm, 0); K1_LDA1(x, sm, 0);
  int slot = 0;
#pragma unroll 1
  for (int st = 0; st < 10; ++st) {
    const LAS char* sb = sm + slot * K1_STAGE;
    slot = slot + 1 == K1_NSLOT ? 0 : slot + 1;
    const LAS char* sn = sm + slot * K1_STAGE;
    K1_STEP(x, y, sb, 1, true);
    K1_STEP(y, x, sb, 2, true);
    K1_STEP(x, y, sb, 3, true);
    LGKM0(); BAR(); SBAR();
    K1_STEP(y, x, sn, 0, true);
  }
  STAMPT(0, 2);
  LAS float* sSum = (LAS float*)(smem + K1_NSLOT * K1_STAGE); LAS float* sSq = sSum + 128; LAS float* sCs = sSum + 256;
  if (lane < 16) {
    sSum[wm * 64 + wn * 32 + lane] = rs4[0]; sSum[wm * 64 + wn * 32 + 16 + lane] = rs4[1];
    sCs[wn * 64 + wm * 32 + lane] = cs4[0]; sCs[wn * 64 + wm * 32 + 16 + lane] = cs4[1];
  }
  LGKM0(); BAR();
  if (!isV) {
    const bool isQ = bn < 5;
    char* img = ws + (isQ ? WS_Q : WS_KV); const int tstride = isQ ? QT : KVT;
#pragma unroll
    for (int i = 0; i < 2; ++i) {
      const int tl = wm * 64 + (i ^ wn) * 32;
      const float m = sSum[tl + r] * (1.f / D), rstd = rsqrtf(fmaxf(sSq[tl + r] * (1.f / D) - m * m, 0.f) + 1e-5f) * (isQ ? QSCALE : 1.f);
      int tile = bm * 4 + wm * 2 + (i ^ wn); if (!isQ) tile = (tile + 64 - KSKEW) & 63;
#pragma unroll
      for (int jn = 0; jn < 2; ++jn) {
        const f32x16& c = i ? (jn ? c11 : c10) : (jn ? c01 : c00);
#pragma unroll
        for (int g = 0; g < 4; ++g) {
          const int nl = wn * 64 + (jn ^ wm) * 32 + 8 * g + 4 * hh;
          const f32x4 c4 = *(const LAS f32x4*)(sCs + nl);
          const int n = (bn % 5) * 128 + nl, head = n / 20, d = n - head * 20;
          u32x2 v;
          v[0] = pk2((c[4 * g] - m * c4[0]) * rstd, (c[4 * g + 1] - m * c4[1]) * rstd);
          v[1] = pk2((c[4 * g + 2] - m * c4[2]) * rstd, (c[4 * g + 3] - m * c4[3]) * rstd);
          const int off = d < 16 ? (d >> 3) * 512 + r * 16 + (d & 7) * 2 : 1024 + r * 8;
          *(u32x2*)(img + (unsigned)((head * 64 + tile) * tstride + off)) = v;
        }
      }
    }
  } else {
#pragma unroll
    for (int i = 0; i < 2; ++i) {
      f32x4 m4[4], r4[4];
#pragma unroll
      for (int g = 0; g < 4; ++g) {
        const int tl = wm * 64 + (i ^ wn) * 32 + 8 * g + 4 * hh;
        m4[g] = *(const LAS f32x4*)(sSum + tl) * (1.f / D); const f32x4 sq = *(const LAS f32x4*)(sSq + tl);
#pragma unroll
        for (int e = 0; e < 4; ++e) r4[g][e] = rsqrtf(fmaxf(sq[e] * (1.f / D) - m4[g][e] * m4[g][e], 0.f) + 1e-5f);
      }
      const int tile = bm * 4 + wm * 2 + (i ^ wn);
#pragma unroll
      for (int jn = 0; jn < 2; ++jn) {
        const f32x16& c = i ? (jn ? c11 : c10) : (jn ? c01 : c00);
        const int nl = wn * 64 + (jn ^ wm) * 32 + r, n = (bn - 10) * 128 + nl, head = n / 20, d = n - head * 20;
        const float cs = sCs[nl];
        char* dst = ws + WS_KV + (unsigned)((head * 64 + tile) * KVT + 1280 + hh * 352 + d * 16);
#pragma unroll
        for (int s = 0; s < 2; ++s) {
          u32x4 v;
#pragma unroll
          for (int g = 0; g < 2; ++g) {
            const int e = 8 * s + 4 * g; const f32x4 m = m4[2 * s + g], rr = r4[2 * s + g];
            v[2 * g] = pk2((c[e] - m[0] * cs) * rr[0], (c[e + 1] - m[1] * cs) * rr[1]);
            v[2 * g + 1] = pk2((c[e + 2] - m[2] * cs) * rr[2], (c[e + 3] - m[3] * cs) * rr[3]);
          }
          *(u32x4*)(dst + s * 704) = v;
          if (d >= 18) { const unsigned c1 = d == 18 ? 0x3F803F80u : 0u; *(u32x4*)(dst + s * 704 + 32) = u32x4{c1, c1, c1, c1}; }
        }
      }
    }
  }
}
DI void k1_helper(const u16* __restrict__ x, const u16* __restrict__ w, char* smem, int bm, int bn, int rot, int hw) {
  const int lane = threadIdx.x & 63, r = lane & 31, hh = lane >> 5;
  const unsigned lds0 = (unsigned)(uintptr_t)smem;
  const unsigned voffA0 = (lane >> 3) * 1280 + (((lane & 7) ^ (lane >> 4)) << 4);
  const unsigned voffA1 = (lane >> 3) * 1280 + (((lane & 7) ^ (4 + (lane >> 4))) << 4);
  const unsigned voffB = (lane >> 4) * 3840 + (((((lane & 15) >> 2) ^ (lane >> 4)) * 4 + (lane & 3)) << 4);
  const char* xa = (const char*)x + (size_t)(bm * 128 + hw * 32) * 1280;
  const char* wb = (const char*)w + (size_t)(hw * 16) * 3840 + bn * 256;
  auto piece = [&](int st, int i) {
    int ks = st + rot; if (ks >= 10) ks -= 10;
    const unsigned dst = lds0 + (st % K1_NSLOT) * K1_STAGE + hw * 4096;
    if (i < 4) glds16((i & 1) ? voffA1 : voffA0, xa + i * 8 * 1280 + ks * 128, dst + i * 1024);
    else glds16(voffB, wb + (size_t)(ks * 64 + (i - 4) * 4) * 3840, dst + 16384 + (i - 4) * 1024);
  };
#pragma unroll 1
  for (int st = 0; st < K1_NSLOT; ++st)
#pragma unroll
    for (int i = 0; i < 8; ++i) piece(st, i);
  WAITV(8 * (K1_NSLOT - 1)); BAR();
  float q0 = 0.f, q1 = 0.f;
  const LAS char* sa = (const LAS char*)smem + (hw * 32 + r) * 128 + hh * 64;
#pragma unroll
  for (int st = 0; st < 10; ++st) {
    u32x4 v[4];
#pragma unroll
    for (int i = 0; i < 4; ++i) v[i] = *(const LAS u32x4*)(sa + (st % K1_NSLOT) * K1_STAGE + ((i ^ (r >> 1)) & 3) * 16);
#pragma unroll
    for (int i = 0; i < 8; ++i) {
      if (st >= 1 && st - 1 + K1_NSLOT < 10) piece(st - 1 + K1_NSLOT, i);
#pragma unroll
      for (int e = 0; e < 2; ++e) { const unsigned u = v[i >> 1][2 * (i & 1) + e]; const float lo = __uint_as_float(u << 16), hi = __uint_as_float(u & 0xffff0000u);
        asm volatile("v_fmac_f32 %0, %1, %1" : "+v"(q0) : "v"(lo)); asm volatile("v_fmac_f32 %0, %1, %1" : "+v"(q1) : "v"(hi)); }
      SBAR();
    }
    const int fl = (st + K1_NSLOT - 1 < 9 ? st + K1_NSLOT - 1 : 9) - (st + 1);
    if (fl >= 3) WAITV(24); else if (fl == 2) WAITV(16); else if (fl == 1) WAITV(8); else WAITV(0);
    LGKM0(); BAR();
  }
  LAS float* sSq = (LAS float*)(smem + K1_NSLOT * K1_STAGE) + 128;
  const float qq = xhalf_sum(q0 + q1);
  if (hh == 0) sSq[hw * 32 + r] = qq;
  LGKM0(); BAR();
}
__global__ __launch_bounds__(512, 2) void k1_qkv(const u16* __restrict__ x, const u16* __restrict__ w, char* __restrict__ ws) {
  extern __shared__ __attribute__((aligned(16))) char smem[];
  const int b = blockIdx.x, xg = b & 7, j = b >> 3;
  const int bm = (xg >> 1) * 4 + (j & 3), bn = (xg & 1) * 8 + (j >> 2);
  if (bn >= 15) return;
  STAMPT(0, 0);
  const int wid = __builtin_amdgcn_readfirstlane(threadIdx.x >> 6);
#define K1_ROT (j % 10)
  if (wid >= 4) k1_helper(x, w, smem, bm, bn, K1_ROT, wid - 4);
  else if (bn >= 10) k1_compute<true>(ws, smem, bm, bn, wid); else k1_compute<false>(ws, smem, bm, bn, wid);
  STAMPT(0, 3);
}

constexpr int CH_TILES = 8, CH_BYTES = CH_TILES * KVT, NCH = 64 / CH_TILES, CH_PIECES = CH_BYTES / 1024;
#define EX(v) __builtin_amdgcn_exp2f(v)
#define PF(p) __builtin_bit_cast(bf16x8, p)
struct Frag { bf16x8 k1, v0, v1; s16x4 k2; };
#define HSTEP(SC, SN, PC0, PC1, PP0, PP1, O, X, Q1, Q2, L1, L2, L3, L4) do { \
    O = MFMA32(X.v0, PF(PP0), O); SBAR(); L1; SBAR(); \
    PP1[2] = pk2(SN[12], SN[13]); PP1[3] = pk2(SN[14], SN[15]); SBAR(); \
    SC[0] = EX(SC[0]); SC[1] = EX(SC[1]); SC[2] = EX(SC[2]); SC[3] = EX(SC[3]); SBAR(); \
    SN = MFMA32(X.k1, Q1, f32x16{}); SBAR(); L2; SBAR(); \
    SC[4] = EX(SC[4]); SC[5] = EX(SC[5]); PC0[0] = pk2(SC[0], SC[1]); SBAR(); SC[6] = EX(SC[6]); SC[7] = EX(SC[7]); PC0[1] = pk2(SC[2], SC[3]); SBAR(); \
    SN = MFMA32K8(X.k2, Q2, SN); SBAR(); L3; SBAR(); \
    SC[8] = EX(SC[8]); SC[9] = EX(SC[9]); PC0[2] = pk2(SC[4], SC[5]); SBAR(); SC[10] = EX(SC[10]); SC[11] = EX(SC[11]); PC0[3] = pk2(SC[6], SC[7]); SBAR(); \
    O = MFMA32(X.v1, PF(PP1), O); SBAR(); L4; SBAR(); \
    SC[12] = EX(SC[12]); SC[13] = EX(SC[13]); PC1[0] = pk2(SC[8], SC[9]); SBAR(); SC[14] = EX(SC[14]); SC[15] = EX(SC[15]); PC1[1] = pk2(SC[10], SC[11]); SBAR(); \
  } while (0)
#define LDV0(Y, base) Y.v0 = *(const LAS bf16x8*)((base) + voff)
#define LDV1(Y, base) Y.v1 = *(const LAS bf16x8*)((base) + voff + 704)
#define LDK1(Y, base) Y.k1 = *(const LAS bf16x8*)((base) + lane * 16)
#define LDK2(Y, base) Y.k2 = *(const LAS s16x4*)((base) + k2off)
#define NOP_ do {} while (0)
DI float rowmax16(const f32x16& s) {
  float m = s[0];
#pragma unroll
  for (int e = 1; e < 16; ++e) m = fmaxf(m, s[e]);
  return xhalf_max(m);
}
DI s16x4 shift_from(const f32x16& s, s16x4 q2, int hh) {
  const float m = -rowmax16(s);
  const unsigned mh = pk2(m, 0.f) & 0xffffu, ml = pk2(m - bflo(mh), 0.f) & 0xffffu;
  return hh ? s16x4{(short)mh, (short)ml, 0, 0} : q2;
}
DI bool attn_unsafe(const f32x16& o, int hh) {
  const float l = hh ? o[8] : o[12];
  bool bad = !(l >= 1e-20f && l <= 1e30f);
#pragma unroll
  for (int e = 0; e < 12; ++e) bad |= !(fabsf(o[e]) <= 3e38f);
  return __any(bad);
}
DI f32x16 attn_online(const char* kv, bf16x8 q1, s16x4 q2, int lane, int k2off, int voff) {
  if (lane >= 32) q2 = s16x4{0, 0, 0, 0};
  float m = -INFINITY; f32x16 o = f32x16{};
#pragma unroll 1
  for (int t = 0; t < 64; ++t) {
    const char* kt = kv + ((t + 64 - KSKEW) & 63) * KVT; const char* vt = kv + t * KVT;
    f32x16 s = MFMA32(*(const bf16x8*)(kt + lane * 16), q1, f32x16{}); s = MFMA32K8(*(const s16x4*)(kt + k2off), q2, s);
    const float mn = fmaxf(m, rowmax16(s)), al = EX(m - mn); m = mn;
#pragma unroll
    for (int e = 0; e < 16; ++e) { o[e] *= al; s[e] = EX(s[e] - mn); }
    const u32x4 p0 = {pk2(s[0], s[1]), pk2(s[2], s[3]), pk2(s[4], s[5]), pk2(s[6], s[7])};
    const u32x4 p1 = {pk2(s[8], s[9]), pk2(s[10], s[11]), pk2(s[12], s[13]), pk2(s[14], s[15])};
    o = MFMA32(*(const bf16x8*)(vt + voff), PF(p0), o); o = MFMA32(*(const bf16x8*)(vt + voff + 704), PF(p1), o);
  }
  return o;
}
DI void attn_compute(char* __restrict__ ws, char* smem, int head, int qc, int wid) {
  const int lane = threadIdx.x & 63, r = lane & 31, hh = lane >> 5;
  const char* kv = ws + WS_KV + (size_t)head * 64 * KVT;
  const int qt = qc * 8 + wid * 2;
  const char* qimg = ws + WS_Q + (size_t)(head * 64 + qt) * QT;
  const int k2off = hh ? 1600 : 1024 + r * 8;
  const int voff = 1280 + hh * 352 + (r < 21 ? r : (r == 24 ? 20 : 21)) * 16;
  const bf16x8 qa1 = *(const bf16x8*)(qimg + lane * 16), qb1 = *(const bf16x8*)(qimg + QT + lane * 16);
  s16x4 qa2 = *(const s16x4*)(qimg + 1024 + r * 8), qb2 = *(const s16x4*)(qimg + QT + 1024 + r * 8);
  if (hh) { qa2 = s16x4{0, 0, 0, 0}; qb2 = s16x4{0, 0, 0, 0}; }
  Frag x, y;
  x.k1 = *(const bf16x8*)(kv + 63 * KVT + lane * 16); x.k2 = *(const s16x4*)(kv + 63 * KVT + k2off);
  x.v0 = bf16x8{}; x.v1 = bf16x8{};
  const LAS char* sm = (const LAS char*)smem;
  f32x16 sa, sb, oa = f32x16{}, ob = f32x16{};
  u32x4 pa0 = {0, 0, 0, 0}, pa1 = {0, 0, 0, 0}, pb0 = {0, 0, 0, 0}, pb1 = {0, 0, 0, 0};
  sa = MFMA32(x.k1, qa1, f32x16{}); sa = MFMA32K8(x.k2, qa2, sa); qa2 = shift_from(sa, qa2, hh);
  sb = MFMA32(x.k1, qb1, f32x16{}); sb = MFMA32K8(x.k2, qb2, sb); qb2 = shift_from(sb, qb2, hh);
  sa = f32x16{}; sb = f32x16{};
#define ITER(X, Y, base) do { \
    HSTEP(sb, sa, pb0, pb1, pa0, pa1, oa, X, qa1, qa2, LDV0(Y, base), LDK1(Y, base), LDK2(Y, base), LDV1(Y, base)); \
    HSTEP(sa, sb, pa0, pa1, pb0, pb1, ob, X, qb1, qb2, NOP_, NOP_, NOP_, LGKM0()); } while (0)
#pragma unroll 1
  for (int c = 0; c < NCH; c += 2) {
#pragma unroll
    for (int t = 0; t < 2 * CH_TILES; t += 2) {
      if (t % CH_TILES == 0) { BAR(); SBAR(); if (c == 0 && t == 0) STAMPT(1, 1); }
      ITER(x, y, sm + t * KVT); ITER(y, x, sm + (t + 1) * KVT);
    }
  }
  HSTEP(sb, sa, pb0, pb1, pa0, pa1, oa, x, qa1, qa2, NOP_, NOP_, NOP_, NOP_);
  pb1[2] = pk2(sb[12], sb[13]); pb1[3] = pk2(sb[14], sb[15]);
  ob = MFMA32(x.v0, PF(pb0), ob); ob = MFMA32(x.v1, PF(pb1), ob);
  STAMPT(1, 2);
  if (attn_unsafe(oa, hh)) oa = attn_online(kv, qa1, qa2, lane, k2off, voff);
  if (attn_unsafe(ob, hh)) ob = attn_online(kv, qb1, qb2, lane, k2off, voff);
  const rsrc_t ro = mkrsrc(ws + WS_O, S * D * 2);
#pragma unroll
  for (int u = 0; u < 2; ++u) {
    const f32x16& o = u ? ob : oa;
    const float il = 1.f / (hh ? o[8] : o[12]);
    const int rt = (qt + u) * 2 + (r >> 4);
#pragma unroll
    for (int g = 0; g < 3; ++g) {
      const int k = head * 20 + 8 * g + 4 * hh;
      if (g < 2 || hh == 0) st8(ro, (rt * 20 + (k >> 5)) * 1024 + (((k >> 2) & 3) * 16 + (r & 15)) * 16 + ((k >> 4) & 1) * 8,
                                u32x2{pk2(o[4 * g] * il, o[4 * g + 1] * il), pk2(o[4 * g + 2] * il, o[4 * g + 3] * il)});
    }
  }
}
DI void attn_helper(const u16* __restrict__ wo, const u16* __restrict__ w1, const u16* __restrict__ w2, char* __restrict__ ws, char* smem, int head, int hw) {
  const int lane = threadIdx.x & 63, b = blockIdx.x;
  const char* kv = ws + WS_KV + (size_t)head * 64 * KVT;
  const unsigned lds0 = (unsigned)(uintptr_t)smem;
  auto issue = [&](int c) {
#pragma unroll
    for (int i = 0; i < 6; ++i) { const int pc = hw + 4 * i; if (pc < CH_PIECES) glds16(lane * 16, kv + (size_t)c * CH_BYTES + pc * 1024, lds0 + (c & 1) * CH_BYTES + pc * 1024); }
  };
  issue(0);
  const u16* src; int ld, f0; size_t dbase;
  if (b < 40) { src = wo; ld = D; f0 = b * 20; dbase = WS_WO; } else if (b < 148) { src = w1; ld = F; f0 = (b - 40) * 20; dbase = WS_W1; } else { src = w2; ld = D; f0 = (b - 148) * 20; dbase = WS_W2; }
  const int nks = b < 148 ? 20 : 54;
  const rsrc_t rw = mkrsrc(ws + dbase, D * F * 2);
  float cs = 0.f;
#pragma unroll 1
  for (int c = 0; c < NCH; ++c) {
    WAITV(0); BAR();
    if (c + 1 < NCH) issue(c + 1);
    if (c < 5) {
      const int f = f0 + hw * 5 + c, gct = f / nks, ks = f - gct * nks;
      const u16* p = src + (size_t)(32 * ks + 4 * (lane >> 4)) * ld + 16 * gct + (lane & 15);
      u16 e[8];
#pragma unroll
      for (int j = 0; j < 8; ++j) e[j] = p[(16 * (j >> 2) + (j & 3)) * ld];
      u32x4 v;
#pragma unroll
      for (int j = 0; j < 4; ++j) { v[j] = e[2 * j] | ((unsigned)e[2 * j + 1] << 16); cs += bf2f(e[2 * j]) + bf2f(e[2 * j + 1]); }
      const int di = (b >= 40 && b < 148) ? f : ((gct / 5) * nks + ks) * 5 + gct % 5;
      st16(rw, di * 1024 + lane * 16, v);
      if (c == 4 && b >= 40 && b < 148) { cs = xq_sum(cs); if (lane < 16) ((float*)(ws + WS_CS1))[hw * 1792 + 16 * gct + lane] = cs; }
    }
  }
}
__global__ __launch_bounds__(512, 2) void k2_attn(const u16* __restrict__ wo, const u16* __restrict__ w1, const u16* __restrict__ w2, char* __restrict__ ws) {
  extern __shared__ __attribute__((aligned(16))) char smem[];
  const int wid = __builtin_amdgcn_readfirstlane(threadIdx.x >> 6);
  const int b = blockIdx.x, head = (b & 7) * 4 + ((b >> 3) & 3), qc = b >> 5;
  STAMPT(1, 0);
  if (wid >= 4) attn_helper(wo, w1, w2, ws, smem, head, wid - 4); else attn_compute(ws, smem, head, qc, wid);
  STAMPT(1, 3);
}

template <int NC> struct GS { f32x4 acc[2][NC]; bf16x8 a[4][2]; bf16x8 b[2][NC]; };
template <int NC, int s, bool LDB> DI void gstep(GS<NC>& g, const LAS char* nb, int cstride, rsrc_t ra, unsigned va0, unsigned va1, unsigned soff) {
#pragma unroll
  for (int m = 0; m < 2 * NC; ++m) {
    g.acc[m & 1][m >> 1] = MFMA16(g.b[s & 1][m >> 1], g.a[s][m & 1], g.acc[m & 1][m >> 1]); SBAR();
    if (LDB && m < NC) { g.b[(s + 1) & 1][m] = *(const LAS bf16x8*)(nb + m * cstride); SBAR(); }
  }
  g.a[s][0] = ldfrag(ra, va0, soff); g.a[s][1] = ldfrag(ra, va1, soff); SBAR();
}
template <int NC, int NKS, int NSLOT, unsigned OOB  > DI void gemm_core(GS<NC>& g, const LAS char* wl, int sstride, int cstride, rsrc_t ra, unsigned va0, unsigned va1, unsigned astep) {
  constexpr int NIT = (NKS - 1) / 4, TAIL = NKS - 4 * NIT;
#pragma unroll
  for (int i = 0; i < 2; ++i)
#pragma unroll
    for (int j = 0; j < NC; ++j) g.acc[i][j] = f32x4{};
  BAR(); SBAR();
#pragma unroll
  for (int j = 0; j < NC; ++j) g.b[0][j] = *(const LAS bf16x8*)(wl + j * cstride);
  int slot = 0;
#pragma unroll 1
  for (int it = 0; it < NIT; ++it) {
    const LAS char* cb = wl + slot * 4 * sstride;
    slot = slot + 1 == NSLOT ? 0 : slot + 1;
    const LAS char* nb = wl + slot * 4 * sstride;
    const int i4 = 4 * it + 4;
    LGKM0(); gstep<NC, 0, true>(g, cb + sstride, cstride, ra, va0, va1, i4 * astep);
    LGKM0(); gstep<NC, 1, true>(g, cb + 2 * sstride, cstride, ra, va0, va1, i4 + 1 < NKS ? (i4 + 1) * astep : OOB);
    LGKM0(); gstep<NC, 2, true>(g, cb + 3 * sstride, cstride, ra, va0, va1, i4 + 2 < NKS ? (i4 + 2) * astep : OOB);
    LGKM0(); BAR(); SBAR();
    gstep<NC, 3, true>(g, nb, cstride, ra, va0, va1, i4 + 3 < NKS ? (i4 + 3) * astep : OOB);
  }
  const LAS char* cb = wl + slot * 4 * sstride;
#pragma unroll
  for (int s = 0; s < TAIL; ++s) {
    LGKM0();
#pragma unroll
    for (int m = 0; m < 2 * NC; ++m) {
      g.acc[m & 1][m >> 1] = MFMA16(g.b[s & 1][m >> 1], g.a[s][m & 1], g.acc[m & 1][m >> 1]); SBAR();
      if (s + 1 < TAIL && m < NC) { g.b[(s + 1) & 1][m] = *(const LAS bf16x8*)(cb + (s + 1) * sstride + m * cstride); SBAR(); }
    }
  }
}
template <int NC> DI void gemm_first(GS<NC>& g, rsrc_t ra, unsigned va0, unsigned va1, unsigned astep) {
#pragma unroll
  for (int s = 0; s < 4; ++s) { g.a[s][0] = ldfrag(ra, va0, s * astep); g.a[s][1] = ldfrag(ra, va1, s * astep); }
}
template <int NCHUNK, int NP, int NPL, int NSLOT, class SrcF> DI void gemm_helper(SrcF srcf, unsigned lds0, int hw) {
  const unsigned v = (threadIdx.x & 63) * 16;
  constexpr int PW = NP / 4;
  static_assert(NP % 4 == 0 || true, "");
  auto issue = [&](int c) {
    const int np = c == NCHUNK - 1 ? NPL : NP;
#pragma unroll
    for (int i = 0; i < (NP + 3) / 4; ++i) { int pc = hw + 4 * i; if (pc >= np) pc = hw; glds16(v, srcf(c, pc), lds0 + ((c % NSLOT) * NP + pc) * 1024); }
  };
  constexpr int PERW = (NP + 3) / 4; (void)PW;
  issue(0); issue(1);
#pragma unroll
  for (int c = 0; c < NCHUNK; ++c) {
    const int n_issued = c == 0 ? 2 : (c - 1 + NSLOT < NCHUNK ? (c == 1 ? NSLOT : c - 1 + NSLOT) : NCHUNK);
    const int fl = (n_issued < NCHUNK ? n_issued : NCHUNK) - 1 - c;
    if (fl >= 3) WAITV(3 * PERW); else if (fl == 2) WAITV(2 * PERW); else if (fl == 1) WAITV(PERW); else WAITV(0);
    BAR();
    if (c == 0) { for (int k = 2; k < NSLOT && k < NCHUNK; ++k) issue(k); }
    else if (c - 1 + NSLOT < NCHUNK) issue(c - 1 + NSLOT);
  }
}
template <int NC> DI void pair_reduce(GS<NC>& g, char* smem, int wid, f32x4 (&out)[NC]) {
  const int lane = threadIdx.x & 63, kh = wid >> 1;
  LAS f32x4* red = (LAS f32x4*)smem + lane;
  BAR();
#pragma unroll
  for (int j = 0; j < NC; ++j) red[(wid * NC + j) * 64] = kh ? g.acc[0][j] : g.acc[1][j];
  LGKM0(); BAR();
#pragma unroll
  for (int j = 0; j < NC; ++j) out[j] = (kh ? g.acc[1][j] : g.acc[0][j]) + red[((wid ^ 2) * NC + j) * 64];
}
DI unsigned piece_off(int rt, int gct, int nks, int lane) { return (rt * nks + (gct >> 1)) * 1024 + lane * 16 + (gct & 1) * 8; }
template <int NC> DI void store_tiles(rsrc_t r, int rt, int gct0, int nct, int nks, int lane, const u32x2 (&v)[NC]) {
  if (gct0 & 1) {
    st8(r, piece_off(rt, gct0, nks, lane), v[0]);
#pragma unroll
    for (int j = 1; j + 1 < NC; j += 2) {
      if (j + 1 < nct) st16(r, piece_off(rt, gct0 + j, nks, lane), u32x4{v[j][0], v[j][1], v[j + 1][0], v[j + 1][1]});
      else st8(r, piece_off(rt, gct0 + j, nks, lane), v[j]);
    }
  } else {
#pragma unroll
    for (int j = 0; j + 1 < NC; j += 2) st16(r, piece_off(rt, gct0 + j, nks, lane), u32x4{v[j][0], v[j][1], v[j + 1][0], v[j + 1][1]});
    if (nct == NC) st8(r, piece_off(rt, gct0 + NC - 1, nks, lane), v[NC - 1]);
  }
}

__global__ __launch_bounds__(512, 2) void k3_wo(const u16* __restrict__ x, char* __restrict__ ws) {
  extern __shared__ __attribute__((aligned(16))) char smem[];
  const int wid = __builtin_amdgcn_readfirstlane(threadIdx.x >> 6), lane = threadIdx.x & 63;
  const int b = blockIdx.x, grp = (b & 7) * 4 + ((b >> 3) & 3), c = b >> 5;
  STAMPT(2, 0);
  if (wid >= 4) {
    const char* w = ws + WS_WO + (size_t)c * 102400;
    gemm_helper<3, 40, 20, 4>([&](int ch, int pc) { return w + (ch * 40 + pc) * 1024; }, (unsigned)(uintptr_t)smem, wid - 4);
    BAR(); BAR();
    return;
  }
  const int rh = wid & 1, kh = wid >> 1;
  GS<5> g;
  const rsrc_t ra = mkrsrc(ws + WS_O, S * D * 2);
  const unsigned va0 = ((grp * 4 + rh * 2) * 20 + kh) * 1024 + lane * 16;
  gemm_first<5>(g, ra, va0, va0 + 20480, 2048);
  const int rt = grp * 4 + rh * 2 + kh, tok = rt * 16 + (lane & 15);
  u32x2 xr[5];
#pragma unroll
  for (int j = 0; j < 5; ++j) xr[j] = *(const u32x2*)(x + (size_t)tok * D + c * 80 + j * 16 + (lane >> 4) * 4);
  gemm_core<5, 10, 4, S * D * 2>(g, (const LAS char*)smem + kh * 5120 + lane * 16, 10240, 1024, ra, va0, va0 + 20480, 2048);
  STAMPT(2, 2);
  f32x4 o[5]; pair_reduce<5>(g, smem, wid, o);
  u32x2 hv[5]; float s1 = 0.f, s2 = 0.f;
#pragma unroll
  for (int j = 0; j < 5; ++j) {
    hv[j] = u32x2{pk2(o[j][0] + bflo(xr[j][0]), o[j][1] + bfhi(xr[j][0])), pk2(o[j][2] + bflo(xr[j][1]), o[j][3] + bfhi(xr[j][1]))};
#pragma unroll
    for (int e = 0; e < 2; ++e) { const float lo = bflo(hv[j][e]), hi = bfhi(hv[j][e]); s1 += lo + hi; s2 += lo * lo + hi * hi; }
  }
  store_tiles<5>(mkrsrc(ws + WS_H, S * D * 2), rt, c * 5, 5, 20, lane, hv);
  s1 = xq_sum(s1); s2 = xq_sum(s2);
  if (lane < 16) st8(mkrsrc(ws + WS_ST, S * 64), tok * 64 + c * 8, u32x2{__float_as_uint(s1), __float_as_uint(s2)});
  STAMPT(2, 3);
}

DI float gelu_tanh(float v) {
  const float t = v * (-2.302208198f + -0.1029432397f * v * v);
  return v * __builtin_amdgcn_rcpf(1.f + EX(t));
}
__global__ __launch_bounds__(512, 2) void k4_w1(const u16* __restrict__ b1, char* __restrict__ ws) {
  extern __shared__ __attribute__((aligned(16))) char smem[];
  const int wid = __builtin_amdgcn_readfirstlane(threadIdx.x >> 6), lane = threadIdx.x & 63;
  const int b = blockIdx.x, grp = (b & 7) * 2 + ((b >> 4) & 1), c = ((b >> 5) << 1) | ((b >> 3) & 1);
  const int gct0 = (c * 27) >> 2, nct = (((c + 1) * 27) >> 2) - gct0;
  STAMPT(3, 0);
  if (wid >= 4) {
    const char* w = ws + WS_W1 + (size_t)gct0 * 20480;
    gemm_helper<5, 28, 28, 5>([&](int ch, int pc) { return w + ((pc % 7) * 20 + ch * 4 + pc / 7) * 1024; }, (unsigned)(uintptr_t)smem, wid - 4);
    return;
  }
  GS<7> g;
  const rsrc_t ra = mkrsrc(ws + WS_H, S * D * 2);
  const int rt0 = grp * 8 + wid * 2;
  const unsigned va0 = rt0 * 20480 + lane * 16;
  gemm_first<7>(g, ra, va0, va0 + 20480, 1024);
  float mean[2], rstd[2];
#pragma unroll
  for (int i = 0; i < 2; ++i) {
    const u32x4 p = __builtin_amdgcn_raw_buffer_load_b128(mkrsrc(ws + WS_ST, S * 64), ((rt0 + i) * 16 + (lane & 15)) * 64 + (lane >> 4) * 16, 0, XAUX);
    const float m = xq_sum(__uint_as_float(p[0]) + __uint_as_float(p[2])) * (1.f / D);
    const float var = fmaxf(xq_sum(__uint_as_float(p[1]) + __uint_as_float(p[3])) * (1.f / D) - m * m, 0.f);
    const float r1 = rsqrtf(var + 1e-5f);
    mean[i] = m; rstd[i] = r1 * rsqrtf(var * r1 * r1 + 1e-5f);
  }
  gemm_core<7, 20, 5, S * D * 2>(g, (const LAS char*)smem + lane * 16, 7168, 1024, ra, va0, va0 + 20480, 1024);
  STAMPT(3, 2);
  const rsrc_t ro = mkrsrc(ws + WS_H1, S * F * 2);
  u32x2 hv[2][7];
#pragma unroll
  for (int j = 0; j < 7; ++j) {
    const int n = min((gct0 + j) * 16 + (lane >> 4) * 4, F - 4);
    const float* csp = (const float*)(ws + WS_CS1) + n;
    const f32x4 cs = (*(const f32x4*)csp + *(const f32x4*)(csp + 1792)) + (*(const f32x4*)(csp + 2 * 1792) + *(const f32x4*)(csp + 3 * 1792));
    const u32x2 bb = *(const u32x2*)(b1 + n);
    const f32x4 bv = {bflo(bb[0]), bfhi(bb[0]), bflo(bb[1]), bfhi(bb[1])};
#pragma unroll
    for (int i = 0; i < 2; ++i) {
      const f32x4 pre = (g.acc[i][j] - mean[i] * cs) * rstd[i] + bv;
      hv[i][j] = u32x2{pk2(gelu_tanh(pre[0]), gelu_tanh(pre[1])), pk2(gelu_tanh(pre[2]), gelu_tanh(pre[3]))};
    }
  }
  store_tiles<7>(ro, rt0, gct0, nct, 54, lane, hv[0]); store_tiles<7>(ro, rt0 + 1, gct0, nct, 54, lane, hv[1]);
  STAMPT(3, 3);
}

__global__ __launch_bounds__(512, 2) void k5_w2(const u16* __restrict__ b2, u16* __restrict__ out, char* __restrict__ ws) {
  extern __shared__ __attribute__((aligned(16))) char smem[];
  const int wid = __builtin_amdgcn_readfirstlane(threadIdx.x >> 6), lane = threadIdx.x & 63;
  const int b = blockIdx.x, grp = (b & 7) * 4 + ((b >> 3) & 3), c = b >> 5;
  STAMPT(4, 0);
  if (wid >= 4) {
    const char* w = ws + WS_W2 + (size_t)c * 276480;
    gemm_helper<7, 40, 30, 4>([&](int ch, int pc) { return w + (ch * 40 + pc) * 1024; }, (unsigned)(uintptr_t)smem, wid - 4);
    BAR(); BAR();
    return;
  }
  const int rh = wid & 1, kh = wid >> 1;
  GS<5> g;
  const rsrc_t ra = mkrsrc(ws + WS_H1, S * F * 2);
  const unsigned va0 = ((grp * 4 + rh * 2) * 54 + kh) * 1024 + lane * 16;
  gemm_first<5>(g, ra, va0, va0 + 55296, 2048);
  const int rt = grp * 4 + rh * 2 + kh, tok = rt * 16 + (lane & 15);
  u32x2 hr[5], bb[5];
#pragma unroll
  for (int j = 0; j < 5; ++j) { hr[j] = *(const u32x2*)(ws + WS_H + piece_off(rt, c * 5 + j, 20, lane)); bb[j] = *(const u32x2*)(b2 + c * 80 + j * 16 + (lane >> 4) * 4); }
  gemm_core<5, 27, 4, S * F * 2>(g, (const LAS char*)smem + kh * 5120 + lane * 16, 10240, 1024, ra, va0, va0 + 55296, 2048);
  STAMPT(4, 2);
  f32x4 o[5]; pair_reduce<5>(g, smem, wid, o);
#pragma unroll
  for (int j = 0; j < 5; ++j)
    *(u32x2*)(out + (size_t)tok * D + c * 80 + j * 16 + (lane >> 4) * 4) =
        u32x2{pk2(o[j][0] + bflo(bb[j][0]) + bflo(hr[j][0]), o[j][1] + bfhi(bb[j][0]) + bfhi(hr[j][0])), pk2(o[j][2] + bflo(bb[j][1]) + bflo(hr[j][1]), o[j][3] + bfhi(bb[j][1]) + bfhi(hr[j][1]))};
  STAMPT(4, 3);
}

extern "C" void kernel_launch(void* const* d_in, const int* in_sizes, int n_in, void* d_out, int out_size, void* d_ws, size_t ws_size, hipStream_t stream) {
  const u16* x = (const u16*)d_in[0]; const u16* wqkv = (const u16*)d_in[1]; const u16* wo = (const u16*)d_in[2];
  const u16* w1 = (const u16*)d_in[4]; const u16* b1 = (const u16*)d_in[5]; const u16* w2 = (const u16*)d_in[6]; const u16* b2 = (const u16*)d_in[7];
  char* ws = (char*)d_ws;
  (void)hipFuncSetAttribute((const void*)k1_qkv, hipFuncAttributeMaxDynamicSharedMemorySize, K1_LDS);
  (void)hipFuncSetAttribute((const void*)k3_wo, hipFuncAttributeMaxDynamicSharedMemorySize, 163840);
  (void)hipFuncSetAttribute((const void*)k4_w1, hipFuncAttributeMaxDynamicSharedMemorySize, 163840);
  (void)hipFuncSetAttribute((const void*)k5_w2, hipFuncAttributeMaxDynamicSharedMemorySize, 163840);
  k1_qkv<<<256, 512, K1_LDS, stream>>>(x, wqkv, ws);
  k2_attn<<<256, 512, 2 * CH_BYTES, stream>>>(wo, w1, w2, ws);
  k3_wo<<<256, 512, 122880, stream>>>(x, ws);
  k4_w1<<<256, 512, 143360, stream>>>(b1, ws);
  k5_w2<<<256, 512, 163840, stream>>>(b2, (u16*)d_out, ws);
}
```

```cpp
#include <hip/hip_runtime.h>
#include <hip/hip_bf16.h>
typedef unsigned short u16;
typedef __attribute__((ext_vector_type(8))) short bf16x8;
typedef __attribute__((ext_vector_type(8))) __bf16 bf8;
typedef __attribute__((ext_vector_type(4))) short s16x4;
typedef __attribute__((ext_vector_type(16))) float f32x16;
typedef __attribute__((ext_vector_type(4))) float f32x4;
typedef __attribute__((ext_vector_type(2))) float f32x2;
typedef __attribute__((ext_vector_type(4))) unsigned u32x4;
typedef __attribute__((ext_vector_type(2))) unsigned u32x2;
typedef __attribute__((ext_vector_type(2))) __bf16 bf2;
#define LAS __attribute__((address_space(3)))
#define DI __device__ __forceinline__
#define SBAR() __builtin_amdgcn_sched_barrier(0)
#define LGKM0() __builtin_amdgcn_s_waitcnt(0xC07F)
#define BAR() asm volatile("s_barrier" ::: "memory")
#define WAITV(n) asm volatile("s_waitcnt vmcnt(%0)" :: "i"(n) : "memory")

constexpr int S = 2048, D = 640, H = 32, DH = 20, F = 1728;
constexpr int QT = 1280, KVT = 2688;
constexpr size_t WS_Q = 0, WS_KV = WS_Q + (size_t)H * 64 * QT, WS_O = WS_KV + (size_t)H * 64 * KVT,
                 WS_H = WS_O + (size_t)S * D * 2, WS_H1 = WS_H + (size_t)S * D * 2,
                 WS_WO = WS_H1 + (size_t)S * F * 2, WS_W1 = WS_WO + (size_t)D * D * 2, WS_W2 = WS_W1 + (size_t)D * F * 2 + 20480,
                 WS_CS1 = WS_W2 + (size_t)D * F * 2, WS_ST = WS_CS1 + 4 * 1792 * 4, WS_XST = WS_ST + (size_t)S * 8 * 8, WS_CSQ = WS_XST + (size_t)S * 16, WS_FL = WS_CSQ + 16384, WS_END = WS_FL + 65536;
#define STAMPT(k, s) do {} while (0)
constexpr int KSKEW = 1;
constexpr float QSCALE = 0.22360679774997896f * 1.4426950408889634f;

DI float bf2f(u16 v) { return __uint_as_float((unsigned)v << 16); }
DI float bflo(unsigned u) { return __uint_as_float(u << 16); }
DI float bfhi(unsigned u) { return __uint_as_float(u & 0xffff0000u); }
DI unsigned pk2(float lo, float hi) { f32x2 v = {lo, hi}; return __builtin_bit_cast(unsigned, __builtin_convertvector(v, bf2)); }
DI void glds16(unsigned voff, const void* sbase, unsigned lds) {
  asm volatile("s_mov_b32 m0, %2\n\ts_nop 0\n\tglobal_load_lds_dwordx4 %0, %1" :: "v"(voff), "s"(sbase), "s"(__builtin_amdgcn_readfirstlane(lds)) : "memory"); }
DI void glds16x(unsigned voff, const void* sbase, unsigned lds) {
  asm volatile("s_mov_b32 m0, %2\n\ts_nop 0\n\tglobal_load_lds_dwordx4 %0, %1 sc1" :: "v"(voff), "s"(sbase), "s"(__builtin_amdgcn_readfirstlane(lds)) : "memory"); }
DI s16x4 trread(const LAS char* p) { return __builtin_amdgcn_ds_read_tr16_b64_v4i16((LAS s16x4*)p); }
DI bf16x8 cat8(s16x4 a, s16x4 b) { return __builtin_shufflevector(a, b, 0, 1, 2, 3, 4, 5, 6, 7); }
#define MFMA32(a, b, c) __builtin_amdgcn_mfma_f32_32x32x16_bf16(__builtin_bit_cast(bf8, a), __builtin_bit_cast(bf8, b), c, 0, 0, 0)
#define MFMA32K8(a, b, c) __builtin_amdgcn_mfma_f32_32x32x8bf16_1k(a, b, c, 0, 0, 0)
#define MFMA16(a, b, c) __builtin_amdgcn_mfma_f32_16x16x32_bf16(__builtin_bit_cast(bf8, a), __builtin_bit_cast(bf8, b), c, 0, 0, 0)
DI float xhalf_sum(float v) { auto r = __builtin_amdgcn_permlane32_swap(__float_as_uint(v), __float_as_uint(v), false, false); return __uint_as_float(r[0]) + __uint_as_float(r[1]); }
DI float xhalf_max(float v) { auto r = __builtin_amdgcn_permlane32_swap(__float_as_uint(v), __float_as_uint(v), false, false); return fmaxf(__uint_as_float(r[0]), __uint_as_float(r[1])); }
DI float xq_sum(float v) { auto r = __builtin_amdgcn_permlane16_swap(__float_as_uint(v), __float_as_uint(v), false, false); return xhalf_sum(__uint_as_float(r[0]) + __uint_as_float(r[1])); }
typedef __amdgpu_buffer_rsrc_t rsrc_t;
DI rsrc_t mkrsrc(const void* p, unsigned bytes) { return __builtin_amdgcn_make_buffer_rsrc((void*)p, 0, bytes, 0x00020000); }
constexpr int XAUX = 16;
DI bf16x8 ldfrag(rsrc_t r, unsigned voff, unsigned soff) { return __builtin_bit_cast(bf16x8, __builtin_amdgcn_raw_buffer_load_b128(r, voff, soff, XAUX)); }
DI void st16(rsrc_t r, unsigned voff, u32x4 v) { __builtin_amdgcn_raw_buffer_store_b128(v, r, voff, 0, XAUX); }
DI void st8(rsrc_t r, unsigned voff, u32x2 v) { __builtin_amdgcn_raw_buffer_store_b64(v, r, voff, 0, XAUX); }

DI s16x4 ldhalf(rsrc_t r, unsigned voff, unsigned soff) { return __builtin_bit_cast(s16x4, __builtin_amdgcn_raw_buffer_load_b64(r, voff, soff, XAUX)); }
DI void st16s(rsrc_t r, unsigned voff, unsigned soff, u32x4 v) { __builtin_amdgcn_raw_buffer_store_b128(v, r, voff + soff, 0, XAUX); }
DI void st8s(rsrc_t r, unsigned voff, unsigned soff, u32x2 v) { __builtin_amdgcn_raw_buffer_store_b64(v, r, voff, soff, XAUX); }
typedef unsigned long long u64;
typedef __attribute__((address_space(1))) u64 gu64;
#define RLX_AGENT __ATOMIC_RELAXED, __HIP_MEMORY_SCOPE_AGENT
constexpr int FL_EPOCH = 0, FL_QKV = 64  , FL_W = FL_QKV + 256  , FL_O = FL_W + 256  ,
              FL_H = FL_O + 1024  , FL_H1 = FL_H + 1024  ;
constexpr unsigned SPIN_CAP = 1u << 17;
DI gu64* flagp(char* ws, int i) { return (gu64*)(ws + WS_FL) + i; }
DI void signal(char* ws, int i, u64 tag) { __hip_atomic_store(flagp(ws, i), tag, RLX_AGENT); }
DI void await(char* ws, int i  , u64 tag) {
  gu64* p = flagp(ws, i);
#pragma unroll 1
  for (unsigned sp = 0; sp < SPIN_CAP; ++sp) {
    if (__all(__hip_atomic_load(p, RLX_AGENT) == tag)) break;
    __builtin_amdgcn_s_sleep(1);
  }
  asm volatile("" ::: "memory");
}

constexpr int K1_NSLOT = 4, K1_STAGE = 32768;
#define K1_PRE 2
struct F4 { bf16x8 a0, a1, b0, b1; };
template <bool isV> DI void k1_compute(char* ws, char* smem, int bm, int bn, int wid) {
  const int lane = threadIdx.x & 63, r = lane & 31, hh = lane >> 5;
  const int wm = wid >> 1, wn = wid & 1;
  f32x16 c00 = f32x16{}, c01 = f32x16{}, c10 = f32x16{}, c11 = f32x16{};
  const int fA = (r >> 1) & 7;
  const int gi = lane >> 4, q = (lane & 15) >> 2, p = lane & 3;
  const LAS char* sm = (const LAS char*)smem;
  const int aoff = (wm * 64 + wn * 32 + r) * 128;
  const int boff = 16384 + (8 * (gi >> 1) + q) * 256 + 32 * (gi & 1) + 8 * p;
  const int b0off = boff + (((2 * wn + wm) ^ q) << 6), b1off = boff + (((2 * wn + (1 ^ wm)) ^ q) << 6);
#define K1_LDA0(X, sb, s) X.a0 = *(const LAS bf16x8*)(sb + aoff + (((2 * (s) + hh) ^ fA) << 4))
#define K1_LDA1(X, sb, s) X.a1 = *(const LAS bf16x8*)(sb + (aoff ^ 4096) + (((2 * (s) + hh) ^ fA) << 4))
#define K1_LDB0(X, sb, s) X.b0 = cat8(trread(sb + b0off + (s) * 4096), trread(sb + b0off + (s) * 4096 + 1024))
#define K1_LDB1(X, sb, s) X.b1 = cat8(trread(sb + b1off + (s) * 4096), trread(sb + b1off + (s) * 4096 + 1024))
#define K1_MM(c, a, b) c = isV ? MFMA32(a, b, c) : MFMA32(b, a, c)
#define K1_STEP(X, Y, sb, s, LD) do { \
    K1_MM(c00, X.a0, X.b0); SBAR(); if (LD) { K1_LDA0(Y, sb, s); K1_LDB0(Y, sb, s); } SBAR(); \
    K1_MM(c01, X.a0, X.b1); SBAR(); if (LD) { K1_LDB1(Y, sb, s); } SBAR(); \
    K1_MM(c10, X.a1, X.b0); SBAR(); if (LD) { K1_LDA1(Y, sb, s); } SBAR(); \
    K1_MM(c11, X.a1, X.b1); SBAR(); } while (0)
  const bool isQ = bn < 5; const int tstride = isQ ? QT : KVT;
  const rsrc_t rimg = mkrsrc(ws + (isQ ? WS_Q : WS_KV), H * 64 * KVT);
  unsigned soff[8];
  if (!isV) {
#pragma unroll
    for (int u = 0; u < 8; ++u) {
      const int n = (bn % 5) * 128 + wn * 64 + ((u >> 2) ^ wm) * 32 + 8 * (u & 3) + 4 * hh, head = n / 20, d = n - head * 20;
      soff[u] = head * 64 * tstride + (d < 16 ? (d >> 3) * 512 + r * 16 + (d & 7) * 2 : 1024 + r * 8);
      asm volatile("" : "+v"(soff[u]));
    }
  } else {
#pragma unroll
    for (int jn = 0; jn < 2; ++jn) {
      const int n = (bn - 10) * 128 + wn * 64 + (jn ^ wm) * 32 + r, head = n / 20, d = n - head * 20;
      soff[jn] = head * 64 * KVT + 1280 + hh * 352 + d * 16;
      if (d >= 18) {
        const unsigned c1 = d == 18 ? 0x3F803F80u : 0u;
#pragma unroll
        for (int u = 0; u < 4; ++u) st16s(rimg, soff[jn] + (u & 1) * 704 + 32, (bm * 4 + wm * 2 + (u >> 1)) * KVT, u32x4{c1, c1, c1, c1});
      }
      asm volatile("" : "+v"(soff[jn]));
    }
  }
  F4 x, y;
  BAR();
  STAMPT(0, 1);
  K1_LDA0(x, sm, 0); K1_LDB0(x, sm, 0); K1_LDB1(x, sm, 0); K1_LDA1(x, sm, 0);
  int slot = 0;
#pragma unroll 1
  for (int st = 0; st < 10; ++st) {
    const LAS char* sb = sm + slot * K1_STAGE;
    slot = slot + 1 == K1_NSLOT ? 0 : slot + 1;
    const LAS char* sn = sm + slot * K1_STAGE;
    K1_STEP(x, y, sb, 1, true);
    K1_STEP(y, x, sb, 2, true);
    K1_STEP(x, y, sb, 3, true);
    LGKM0(); BAR(); SBAR();
    K1_STEP(y, x, sn, 0, true);
  }
  STAMPT(0, 2);
  LGKM0(); BAR();
  const LAS float* sMean = (const LAS float*)(smem + K1_NSLOT * K1_STAGE); const LAS float* sRstd = sMean + 128; const LAS float* sCs = sMean + 256;
  if (!isV) {
#pragma unroll
    for (int i = 0; i < 2; ++i) {
      const int tl = wm * 64 + (i ^ wn) * 32;
      const float m = sMean[tl + r], rstd = sRstd[tl + r] * (isQ ? QSCALE : 1.f);
      int tile = bm * 4 + wm * 2 + (i ^ wn); if (!isQ) tile = (tile + 64 - KSKEW) & 63;
#pragma unroll
      for (int jn = 0; jn < 2; ++jn) {
        const f32x16& c = i ? (jn ? c11 : c10) : (jn ? c01 : c00);
#pragma unroll
        for (int g = 0; g < 4; ++g) {
          const f32x4 c4 = *(const LAS f32x4*)(sCs + wn * 64 + (jn ^ wm) * 32 + 8 * g + 4 * hh);
          u32x2 v;
          v[0] = pk2((c[4 * g] - m * c4[0]) * rstd, (c[4 * g + 1] - m * c4[1]) * rstd);
          v[1] = pk2((c[4 * g + 2] - m * c4[2]) * rstd, (c[4 * g + 3] - m * c4[3]) * rstd);
          st8s(rimg, soff[jn * 4 + g], tile * tstride, v);
        }
      }
    }
  } else {
#pragma unroll
    for (int i = 0; i < 2; ++i) {
      const int tile = bm * 4 + wm * 2 + (i ^ wn);
#pragma unroll
      for (int s = 0; s < 2; ++s) {
        f32x4 m4[2], r4[2];
#pragma unroll
        for (int g = 0; g < 2; ++g) { const int tl = wm * 64 + (i ^ wn) * 32 + 16 * s + 8 * g + 4 * hh; m4[g] = *(const LAS f32x4*)(sMean + tl); r4[g] = *(const LAS f32x4*)(sRstd + tl); }
#pragma unroll
        for (int jn = 0; jn < 2; ++jn) {
          const f32x16& c = i ? (jn ? c11 : c10) : (jn ? c01 : c00);
          const float cs = sCs[wn * 64 + (jn ^ wm) * 32 + r];
          u32x4 v;
#pragma unroll
          for (int g = 0; g < 2; ++g) {
            const int e = 8 * s + 4 * g;
            v[2 * g] = pk2((c[e] - m4[g][0] * cs) * r4[g][0], (c[e + 1] - m4[g][1] * cs) * r4[g][1]);
            v[2 * g + 1] = pk2((c[e + 2] - m4[g][2] * cs) * r4[g][2], (c[e + 3] - m4[g][3] * cs) * r4[g][3]);
          }
          st16s(rimg, soff[jn] + s * 704, tile * KVT, v);
        }
      }
    }
  }
}
struct XRow { u32x4 a, b; u32x2 c; };
DI void xrow_load(XRow& v, const u16* x, int row0) {
  const int lane = threadIdx.x & 63;
  const char* p = (const char*)x + (size_t)row0 * 1280; const unsigned o = (lane >> 5) * 1280 + (lane & 31) * 16, o2 = (lane >> 5) * 1280 + 1024 + (lane & 31) * 8;
  asm volatile("global_load_dwordx4 %0, %3, %5\n\tglobal_load_dwordx4 %1, %3, %5 offset:512\n\tglobal_load_dwordx2 %2, %4, %5"
               : "=&v"(v.a), "=&v"(v.b), "=&v"(v.c) : "v"(o), "v"(o2), "s"(p) : "memory");
}
DI float sum32(float v) {
  v += __shfl_xor(v, 1); v += __shfl_xor(v, 2); v += __shfl_xor(v, 4); v += __shfl_xor(v, 8);
  auto r = __builtin_amdgcn_permlane16_swap(__float_as_uint(v), __float_as_uint(v), false, false); return __uint_as_float(r[0]) + __uint_as_float(r[1]);
}
DI void xrow_publish(XRow& v, char* ws, int row0, u64 tag) {
  asm volatile("" : "+v"(v.a), "+v"(v.b), "+v"(v.c));
  const int lane = threadIdx.x & 63;
  float e[20];
#pragma unroll
  for (int i = 0; i < 4; ++i) { e[2 * i] = bflo(v.a[i]); e[2 * i + 1] = bfhi(v.a[i]); e[8 + 2 * i] = bflo(v.b[i]); e[9 + 2 * i] = bfhi(v.b[i]); }
  e[16] = bflo(v.c[0]); e[17] = bfhi(v.c[0]); e[18] = bflo(v.c[1]); e[19] = bfhi(v.c[1]);
  float s = 0.f;
#pragma unroll
  for (int i = 0; i < 20; ++i) s += e[i];
  const float m = sum32(s) * (1.f / D);
  float q = 0.f;
#pragma unroll
  for (int i = 0; i < 20; ++i) { const float d = e[i] - m; q += d * d; }
  const float rstd = rsqrtf(sum32(q) * (1.f / D) + 1e-5f);
  if ((lane & 31) == 0) st16(mkrsrc(ws + WS_XST, S * 16), (row0 + (lane >> 5)) * 16, u32x4{__float_as_uint(m), (unsigned)tag, __float_as_uint(rstd), (unsigned)(tag >> 32)});
}
DI void k1_helper(const u16* __restrict__ x, const u16* __restrict__ w, char* ws, char* smem, int bm, int bn, int rot, int hw, u64 tag) {
  const int lane = threadIdx.x & 63;
  const unsigned lds0 = (unsigned)(uintptr_t)smem;
  const unsigned voffA0 = (lane >> 3) * 1280 + (((lane & 7) ^ (lane >> 4)) << 4);
  const unsigned voffA1 = (lane >> 3) * 1280 + (((lane & 7) ^ (4 + (lane >> 4))) << 4);
  const unsigned voffB = (lane >> 4) * 3840 + (((((lane & 15) >> 2) ^ (lane >> 4)) * 4 + (lane & 3)) << 4);
  const char* xa = (const char*)x + (size_t)(bm * 128 + hw * 32) * 1280;
  const char* wb = (const char*)w + (size_t)(hw * 16) * 3840 + bn * 256;
  auto issue = [&](int st) {
    int ks = st + rot; if (ks >= 10) ks -= 10;
    const unsigned dst = lds0 + (st % K1_NSLOT) * K1_STAGE + hw * 4096;
#pragma unroll
    for (int i = 0; i < 4; ++i) glds16((i & 1) ? voffA1 : voffA0, xa + i * 8 * 1280 + ks * 128, dst + i * 1024);
#pragma unroll
    for (int i = 0; i < 4; ++i) glds16(voffB, wb + (size_t)(ks * 64 + i * 4) * 3840, dst + 16384 + i * 1024);
  };
  const int row0 = blockIdx.x * 8 + hw * 2;
  XRow xr;
  issue(0);
  xrow_load(xr, x, row0);
#pragma unroll 1
  for (int st = 1; st < K1_PRE; ++st) issue(st);
  WAITV(8 * (K1_PRE - 1)); BAR();
#pragma unroll 1
  for (int st = K1_PRE; st < K1_NSLOT; ++st) issue(st);
  WAITV(8 * (K1_NSLOT - 2)); BAR();
  issue(K1_NSLOT);
  xrow_publish(xr, ws, row0, tag);
#pragma unroll 1
  for (int st = 1; st < 10 - K1_NSLOT; ++st) { WAITV(8 * (K1_NSLOT - 2)); BAR(); issue(st + K1_NSLOT); }
  WAITV(16); BAR();
  WAITV(8); BAR();
  WAITV(0); BAR();
  const rsrc_t rs = mkrsrc(ws + WS_XST, S * 16 + 16384);
  const unsigned go = lane < 32 ? (bm * 128 + hw * 32 + lane) * 16 : S * 16 + (bn * 64 + hw * 16 + (lane & 15)) * 16;
  u32x4 rec;
#pragma unroll 1
  for (unsigned sp = 0; sp < SPIN_CAP; ++sp) {
    asm volatile("" ::: "memory");
    rec = __builtin_amdgcn_raw_buffer_load_b128(rs, go, 0, XAUX);
    if (__all(rec[1] == (unsigned)tag && rec[3] == (unsigned)(tag >> 32))) break;
    __builtin_amdgcn_s_sleep(1);
  }
  LAS unsigned* st = (LAS unsigned*)(smem + K1_NSLOT * K1_STAGE);
  if (lane < 32) { st[hw * 32 + lane] = rec[0]; st[128 + hw * 32 + lane] = rec[2]; }
  else if (lane < 48) { st[256 + hw * 32 + 2 * (lane & 15)] = rec[0]; st[257 + hw * 32 + 2 * (lane & 15)] = rec[2]; }
  BAR();
  LGKM0(); BAR();
}
DI void k1_colsum(const u16* __restrict__ x, const u16* __restrict__ w, char* ws, char* smem, int u, int wid, u64 tag) {
  const int lane = threadIdx.x & 63;
  if (wid >= 4) { XRow xr; const int row0 = blockIdx.x * 8 + (wid - 4) * 2; xrow_load(xr, x, row0); WAITV(0); xrow_publish(xr, ws, row0, tag); }
  if (u < 15) {
    const char* p = (const char*)w + (size_t)(wid * 80 + (lane >> 4)) * 3840 + u * 256 + (lane & 15) * 16;
    u32x4 v[20];
#pragma unroll
    for (int i = 0; i < 20; ++i) v[i] = *(const u32x4*)(p + (size_t)i * 4 * 3840);
    float a[8];
#pragma unroll
    for (int e = 0; e < 8; ++e) a[e] = 0.f;
#pragma unroll
    for (int i = 0; i < 20; ++i)
#pragma unroll
      for (int e = 0; e < 4; ++e) { a[2 * e] += bflo(v[i][e]); a[2 * e + 1] += bfhi(v[i][e]); }
    LAS float* part = (LAS float*)smem;
#pragma unroll
    for (int e = 0; e < 8; ++e) { a[e] = xq_sum(a[e]); if (lane < 16) part[wid * 128 + lane * 8 + e] = a[e]; }
    LGKM0(); BAR();
    if (wid == 0) {
      float c0 = 0.f, c1 = 0.f;
#pragma unroll
      for (int k = 0; k < 8; ++k) { c0 += part[k * 128 + 2 * lane]; c1 += part[k * 128 + 2 * lane + 1]; }
      st16(mkrsrc(ws + WS_CSQ, 16384), (u * 64 + lane) * 16, u32x4{__float_as_uint(c0), (unsigned)tag, __float_as_uint(c1), (unsigned)(tag >> 32)});
    }
  }
}

constexpr int CH_TILES = 8, CH_BYTES = CH_TILES * KVT, NCH = 64 / CH_TILES, CH_PIECES = CH_BYTES / 1024;
#define EX(v) __builtin_amdgcn_exp2f(v)
#define PF(p) __builtin_bit_cast(bf16x8, p)
struct Frag { bf16x8 k1, v0, v1; s16x4 k2; };
#define HSTEP(SC, SN, PC0, PC1, PP0, PP1, O, X, Q1, Q2, L1, L2, L3, L4) do { \
    O = MFMA32(X.v0, PF(PP0), O); SBAR(); L1; SBAR(); \
    PP1[2] = pk2(SN[12], SN[13]); PP1[3] = pk2(SN[14], SN[15]); SBAR(); \
    SC[0] = EX(SC[0]); SC[1] = EX(SC[1]); SC[2] = EX(SC[2]); SC[3] = EX(SC[3]); SBAR(); \
    SN = MFMA32(X.k1, Q1, f32x16{}); SBAR(); L2; SBAR(); \
    SC[4] = EX(SC[4]); SC[5] = EX(SC[5]); PC0[0] = pk2(SC[0], SC[1]); SBAR(); SC[6] = EX(SC[6]); SC[7] = EX(SC[7]); PC0[1] = pk2(SC[2], SC[3]); SBAR(); \
    SN = MFMA32K8(X.k2, Q2, SN); SBAR(); L3; SBAR(); \
    SC[8] = EX(SC[8]); SC[9] = EX(SC[9]); PC0[2] = pk2(SC[4], SC[5]); SBAR(); SC[10] = EX(SC[10]); SC[11] = EX(SC[11]); PC0[3] = pk2(SC[6], SC[7]); SBAR(); \
    O = MFMA32(X.v1, PF(PP1), O); SBAR(); L4; SBAR(); \
    SC[12] = EX(SC[12]); SC[13] = EX(SC[13]); PC1[0] = pk2(SC[8], SC[9]); SBAR(); SC[14] = EX(SC[14]); SC[15] = EX(SC[15]); PC1[1] = pk2(SC[10], SC[11]); SBAR(); \
  } while (0)
#define LDV0(Y, base) Y.v0 = *(const LAS bf16x8*)((base) + voff)
#define LDV1(Y, base) Y.v1 = *(const LAS bf16x8*)((base) + voff + 704)
#define LDK1(Y, base) Y.k1 = *(const LAS bf16x8*)((base) + lane * 16)
#define LDK2(Y, base) Y.k2 = *(const LAS s16x4*)((base) + k2off)
#define NOP_ do {} while (0)
DI float rowmax16(const f32x16& s) {
  float m = s[0];
#pragma unroll
  for (int e = 1; e < 16; ++e) m = fmaxf(m, s[e]);
  return xhalf_max(m);
}
DI s16x4 shift_from(const f32x16& s, s16x4 q2, int hh) {
  const float m = -rowmax16(s);
  const unsigned mh = pk2(m, 0.f) & 0xffffu, ml = pk2(m - bflo(mh), 0.f) & 0xffffu;
  return hh ? s16x4{(short)mh, (short)ml, 0, 0} : q2;
}
DI bool attn_unsafe(const f32x16& o, int hh) {
  const float l = hh ? o[8] : o[12];
  bool bad = !(l >= 1e-20f && l <= 1e30f);
#pragma unroll
  for (int e = 0; e < 12; ++e) bad |= !(fabsf(o[e]) <= 3e38f);
  return __any(bad);
}
DI f32x16 attn_online(rsrc_t kv, bf16x8 q1, s16x4 q2, int lane, int k2off, int voff) {
  if (lane >= 32) q2 = s16x4{0, 0, 0, 0};
  float m = -INFINITY; f32x16 o = f32x16{};
#pragma unroll 1
  for (int t = 0; t < 64; ++t) {
    const int kt = ((t + 64 - KSKEW) & 63) * KVT, vt = t * KVT;
    f32x16 s = MFMA32(ldfrag(kv, lane * 16, kt), q1, f32x16{}); s = MFMA32K8(ldhalf(kv, k2off, kt), q2, s);
    const float mn = fmaxf(m, rowmax16(s)), al = EX(m - mn); m = mn;
#pragma unroll
    for (int e = 0; e < 16; ++e) { o[e] *= al; s[e] = EX(s[e] - mn); }
    const u32x4 p0 = {pk2(s[0], s[1]), pk2(s[2], s[3]), pk2(s[4], s[5]), pk2(s[6], s[7])};
    const u32x4 p1 = {pk2(s[8], s[9]), pk2(s[10], s[11]), pk2(s[12], s[13]), pk2(s[14], s[15])};
    o = MFMA32(ldfrag(kv, voff, vt), PF(p0), o); o = MFMA32(ldfrag(kv, voff + 704, vt), PF(p1), o);
  }
  return o;
}
DI void attn_compute(char* ws, char* smem, int head, int qc, int wid, u64 tag) {
  const int lane = threadIdx.x & 63, r = lane & 31, hh = lane >> 5;
  const rsrc_t kv = mkrsrc(ws + WS_KV + (size_t)head * 64 * KVT, 64 * KVT);
  const int qt = qc * 8 + wid * 2;
  const rsrc_t rq = mkrsrc(ws + WS_Q + (size_t)(head * 64 + qt) * QT, 2 * QT);
  const int k2off = hh ? 1600 : 1024 + r * 8;
  const int voff = 1280 + hh * 352 + (r < 21 ? r : (r == 24 ? 20 : 21)) * 16;
  const bf16x8 qa1 = ldfrag(rq, lane * 16, 0), qb1 = ldfrag(rq, lane * 16, QT);
  s16x4 qa2 = ldhalf(rq, 1024 + r * 8, 0), qb2 = ldhalf(rq, 1024 + r * 8, QT);
  if (hh) { qa2 = s16x4{0, 0, 0, 0}; qb2 = s16x4{0, 0, 0, 0}; }
  Frag x, y;
  x.k1 = ldfrag(kv, lane * 16, 63 * KVT); x.k2 = ldhalf(kv, k2off, 63 * KVT);
  x.v0 = bf16x8{}; x.v1 = bf16x8{};
  const LAS char* sm = (const LAS char*)smem;
  f32x16 sa, sb, oa = f32x16{}, ob = f32x16{};
  u32x4 pa0 = {0, 0, 0, 0}, pa1 = {0, 0, 0, 0}, pb0 = {0, 0, 0, 0}, pb1 = {0, 0, 0, 0};
  sa = MFMA32(x.k1, qa1, f32x16{}); sa = MFMA32K8(x.k2, qa2, sa); qa2 = shift_from(sa, qa2, hh);
  sb = MFMA32(x.k1, qb1, f32x16{}); sb = MFMA32K8(x.k2, qb2, sb); qb2 = shift_from(sb, qb2, hh);
  sa = f32x16{}; sb = f32x16{};
#define ITER(X, Y, base) do { \
    HSTEP(sb, sa, pb0, pb1, pa0, pa1, oa, X, qa1, qa2, LDV0(Y, base), LDK1(Y, base), LDK2(Y, base), LDV1(Y, base)); \
    HSTEP(sa, sb, pa0, pa1, pb0, pb1, ob, X, qb1, qb2, NOP_, NOP_, NOP_, LGKM0()); } while (0)
#pragma unroll 1
  for (int c = 0; c < NCH; c += 2) {
#pragma unroll
    for (int t = 0; t < 2 * CH_TILES; t += 2) {
      if (t % CH_TILES == 0) { BAR(); SBAR(); if (c == 0 && t == 0) STAMPT(1, 1); }
      ITER(x, y, sm + t * KVT); ITER(y, x, sm + (t + 1) * KVT);
    }
  }
  BAR(); SBAR();
  HSTEP(sb, sa, pb0, pb1, pa0, pa1, oa, x, qa1, qa2, NOP_, NOP_, NOP_, NOP_);
  pb1[2] = pk2(sb[12], sb[13]); pb1[3] = pk2(sb[14], sb[15]);
  ob = MFMA32(x.v0, PF(pb0), ob); ob = MFMA32(x.v1, PF(pb1), ob);
  STAMPT(1, 2);
  if (attn_unsafe(oa, hh)) oa = attn_online(kv, qa1, qa2, lane, k2off, voff);
  if (attn_unsafe(ob, hh)) ob = attn_online(kv, qb1, qb2, lane, k2off, voff);
  const rsrc_t ro = mkrsrc(ws + WS_O, S * D * 2);
#pragma unroll
  for (int u = 0; u < 2; ++u) {
    const f32x16& o = u ? ob : oa;
    const float il = 1.f / (hh ? o[8] : o[12]);
    const int rt = (qt + u) * 2 + (r >> 4);
#pragma unroll
    for (int g = 0; g < 3; ++g) {
      const int k = head * 20 + 8 * g + 4 * hh;
      if (g < 2 || hh == 0) st8(ro, (rt * 20 + (k >> 5)) * 1024 + (((k >> 2) & 3) * 16 + (r & 15)) * 16 + ((k >> 4) & 1) * 8,
                                u32x2{pk2(o[4 * g] * il, o[4 * g + 1] * il), pk2(o[4 * g + 2] * il, o[4 * g + 3] * il)});
    }
  }
  WAITV(0);
  if (lane == 0) signal(ws, FL_O + (qc * 4 + wid) * 32 + head, tag);
}

template <int NC> struct GS { f32x4 acc[2][NC]; bf16x8 a[4][2]; bf16x8 b[2][NC]; };
template <int NC, int s, bool LDB> DI void gstep(GS<NC>& g, const LAS char* nb, int cstride, rsrc_t ra, unsigned va0, unsigned va1, unsigned soff) {
#pragma unroll
  for (int m = 0; m < 2 * NC; ++m) {
    g.acc[m & 1][m >> 1] = MFMA16(g.b[s & 1][m >> 1], g.a[s][m & 1], g.acc[m & 1][m >> 1]); SBAR();
    if (LDB && m < NC) { g.b[(s + 1) & 1][m] = *(const LAS bf16x8*)(nb + m * cstride); SBAR(); }
  }
  g.a[s][0] = ldfrag(ra, va0, soff); g.a[s][1] = ldfrag(ra, va1, soff); SBAR();
}
template <int NC, int NKS, int NSLOT, bool BARS, unsigned OOB  > DI void gemm_core(GS<NC>& g, const LAS char* wl, int sstride, int cstride, rsrc_t ra, unsigned va0, unsigned va1, unsigned astep) {
  constexpr int NIT = (NKS - 1) / 4, TAIL = NKS - 4 * NIT;
#pragma unroll
  for (int i = 0; i < 2; ++i)
#pragma unroll
    for (int j = 0; j < NC; ++j) g.acc[i][j] = f32x4{};
  if (BARS) { BAR(); SBAR(); }
#pragma unroll
  for (int j = 0; j < NC; ++j) g.b[0][j] = *(const LAS bf16x8*)(wl + j * cstride);
  int slot = 0;
#pragma unroll 1
  for (int it = 0; it < NIT; ++it) {
    const LAS char* cb = wl + slot * 4 * sstride;
    slot = slot + 1 == NSLOT ? 0 : slot + 1;
    const LAS char* nb = wl + slot * 4 * sstride;
    const int i4 = 4 * it + 4;
    LGKM0(); gstep<NC, 0, true>(g, cb + sstride, cstride, ra, va0, va1, i4 * astep);
    LGKM0(); gstep<NC, 1, true>(g, cb + 2 * sstride, cstride, ra, va0, va1, i4 + 1 < NKS ? (i4 + 1) * astep : OOB);
    LGKM0(); gstep<NC, 2, true>(g, cb + 3 * sstride, cstride, ra, va0, va1, i4 + 2 < NKS ? (i4 + 2) * astep : OOB);
    LGKM0(); if (BARS) { BAR(); SBAR(); }
    gstep<NC, 3, true>(g, nb, cstride, ra, va0, va1, i4 + 3 < NKS ? (i4 + 3) * astep : OOB);
  }
  const LAS char* cb = wl + slot * 4 * sstride;
#pragma unroll
  for (int s = 0; s < TAIL; ++s) {
    LGKM0();
#pragma unroll
    for (int m = 0; m < 2 * NC; ++m) {
      g.acc[m & 1][m >> 1] = MFMA16(g.b[s & 1][m >> 1], g.a[s][m & 1], g.acc[m & 1][m >> 1]); SBAR();
      if (s + 1 < TAIL && m < NC) { g.b[(s + 1) & 1][m] = *(const LAS bf16x8*)(cb + (s + 1) * sstride + m * cstride); SBAR(); }
    }
  }
}
template <int NC> DI void gemm_first(GS<NC>& g, rsrc_t ra, unsigned va0, unsigned va1, unsigned astep) {
#pragma unroll
  for (int s = 0; s < 4; ++s) { g.a[s][0] = ldfrag(ra, va0, s * astep); g.a[s][1] = ldfrag(ra, va1, s * astep); }
}
template <int NC> DI void pair_reduce(GS<NC>& g, char* smem, int wid, f32x4 (&out)[NC]) {
  const int lane = threadIdx.x & 63, kh = wid >> 1;
  LAS f32x4* red = (LAS f32x4*)(smem + 143360) + lane;
  BAR();
#pragma unroll
  for (int j = 0; j < NC; ++j) red[(wid * NC + j) * 64] = kh ? g.acc[0][j] : g.acc[1][j];
  LGKM0(); BAR();
#pragma unroll
  for (int j = 0; j < NC; ++j) out[j] = (kh ? g.acc[1][j] : g.acc[0][j]) + red[((wid ^ 2) * NC + j) * 64];
}
DI unsigned piece_off(int rt, int gct, int nks, int lane) { return (rt * nks + (gct >> 1)) * 1024 + lane * 16 + (gct & 1) * 8; }
template <int NC> DI void store_tiles(rsrc_t r, int rt, int gct0, int nct, int nks, int lane, const u32x2 (&v)[NC]) {
  if (gct0 & 1) {
    st8(r, piece_off(rt, gct0, nks, lane), v[0]);
#pragma unroll
    for (int j = 1; j + 1 < NC; j += 2) {
      if (j + 1 < nct) st16(r, piece_off(rt, gct0 + j, nks, lane), u32x4{v[j][0], v[j][1], v[j + 1][0], v[j + 1][1]});
      else st8(r, piece_off(rt, gct0 + j, nks, lane), v[j]);
    }
  } else {
#pragma unroll
    for (int j = 0; j + 1 < NC; j += 2) st16(r, piece_off(rt, gct0 + j, nks, lane), u32x4{v[j][0], v[j][1], v[j + 1][0], v[j + 1][1]});
    if (nct == NC) st8(r, piece_off(rt, gct0 + NC - 1, nks, lane), v[NC - 1]);
  }
}

DI float gelu_tanh(float v) {
  const float t = v * (-2.302208198f + -0.1029432397f * v * v);
  return v * __builtin_amdgcn_rcpf(1.f + EX(t));
}

constexpr int LDS_WO = 61440, LDS_BYTES = 163840;
struct Args { const u16 *x, *wqkv, *wo, *w1, *b1, *w2, *b2; u16* out; char* ws; };

DI void post_compute(const Args& a, char* smem, int wid, u64 tag) {
  char* ws = a.ws;
  const int lane = threadIdx.x & 63, b = blockIdx.x;
  const int grp = (b & 7) * 4 + ((b >> 3) & 3), c = b >> 5, rh = wid & 1, kh = wid >> 1;
  const int rt = grp * 4 + rh * 2 + kh, tok = rt * 16 + (lane & 15);
  u32x2 hv[5];
  {
    u32x2 xr[5];
#pragma unroll
    for (int j = 0; j < 5; ++j) xr[j] = *(const u32x2*)(a.x + (size_t)tok * D + c * 80 + j * 16 + (lane >> 4) * 4);
    GS<5> g;
    const rsrc_t ra = mkrsrc(ws + WS_O, S * D * 2);
    const unsigned va0 = ((grp * 4 + rh * 2) * 20 + kh) * 1024 + lane * 16;
    await(ws, FL_O + grp * 32 + (lane & 31), tag);
    STAMPT(2, 0);
    gemm_first<5>(g, ra, va0, va0 + 20480, 2048);
    gemm_core<5, 10, 4, false, S * D * 2>(g, (const LAS char*)smem + LDS_WO + kh * 5120 + lane * 16, 10240, 1024, ra, va0, va0 + 20480, 2048);
    STAMPT(2, 1);
    f32x4 o[5]; pair_reduce<5>(g, smem, wid, o);
    float s1 = 0.f, s2 = 0.f;
#pragma unroll
    for (int j = 0; j < 5; ++j) {
      hv[j] = u32x2{pk2(o[j][0] + bflo(xr[j][0]), o[j][1] + bfhi(xr[j][0])), pk2(o[j][2] + bflo(xr[j][1]), o[j][3] + bfhi(xr[j][1]))};
#pragma unroll
      for (int e = 0; e < 2; ++e) { const float lo = bflo(hv[j][e]), hi = bfhi(hv[j][e]); s1 += lo + hi; s2 += lo * lo + hi * hi; }
    }
    store_tiles<5>(mkrsrc(ws + WS_H, S * D * 2), rt, c * 5, 5, 20, lane, hv);
    s1 = xq_sum(s1); s2 = xq_sum(s2);
    if (lane < 16) st8(mkrsrc(ws + WS_ST, S * 64), tok * 64 + c * 8, u32x2{__float_as_uint(s1), __float_as_uint(s2)});
    STAMPT(2, 2);
    WAITV(0);
    if (lane == 0) signal(ws, FL_H + rt * 8 + c, tag);
    STAMPT(2, 3);
  }
  {
    const int g4 = (b & 7) * 2 + ((b >> 4) & 1), c4 = ((b >> 5) << 1) | ((b >> 3) & 1);
    const int gct0 = (c4 * 27) >> 2, nct = (((c4 + 1) * 27) >> 2) - gct0;
    f32x4 cs[7]; u32x2 bb[7];
    const rsrc_t rc = mkrsrc(ws + WS_CS1, 4 * 1792 * 4);
#pragma unroll
    for (int j = 0; j < 7; ++j) {
      const int n = (gct0 + j) * 16 + (lane >> 4) * 4;
      f32x4 t[4];
#pragma unroll
      for (int k = 0; k < 4; ++k) t[k] = __builtin_bit_cast(f32x4, __builtin_amdgcn_raw_buffer_load_b128(rc, n * 4, k * 7168, XAUX));
      cs[j] = (t[0] + t[1]) + (t[2] + t[3]);
      bb[j] = *(const u32x2*)(a.b1 + n);
    }
    GS<7> g;
    const rsrc_t ra = mkrsrc(ws + WS_H, S * D * 2);
    const int rt0 = g4 * 8 + wid * 2;
    const unsigned va0 = rt0 * 20480 + lane * 16;
#pragma unroll
    for (int j = 0; j < 7; ++j) asm volatile("" : "+v"(cs[j]), "+v"(bb[j]));
    await(ws, FL_H + rt0 * 8 + (lane & 15), tag);
    STAMPT(3, 0);
    u32x4 ps[2];
#pragma unroll
    for (int i = 0; i < 2; ++i) ps[i] = __builtin_amdgcn_raw_buffer_load_b128(mkrsrc(ws + WS_ST, S * 64), ((rt0 + i) * 16 + (lane & 15)) * 64 + (lane >> 4) * 16, 0, XAUX);
    gemm_first<7>(g, ra, va0, va0 + 20480, 1024);
    gemm_core<7, 20, 5, true, S * D * 2>(g, (const LAS char*)smem + lane * 16, 7168, 1024, ra, va0, va0 + 20480, 1024);
    BAR();
    STAMPT(3, 1);
    const rsrc_t ro = mkrsrc(ws + WS_H1, S * F * 2);
#pragma unroll
    for (int i = 0; i < 2; ++i) {
      const float m = xq_sum(__uint_as_float(ps[i][0]) + __uint_as_float(ps[i][2])) * (1.f / D);
      const float var = fmaxf(xq_sum(__uint_as_float(ps[i][1]) + __uint_as_float(ps[i][3])) * (1.f / D) - m * m, 0.f);
      const float r1 = rsqrtf(var + 1e-5f);
      const float rstd = r1 * rsqrtf(var * r1 * r1 + 1e-5f);
      u32x2 h1[7];
#pragma unroll
      for (int j = 0; j < 7; ++j) {
        const f32x4 bv = {bflo(bb[j][0]), bfhi(bb[j][0]), bflo(bb[j][1]), bfhi(bb[j][1])};
        const f32x4 pre = (g.acc[i][j] - m * cs[j]) * rstd + bv;
        h1[j] = u32x2{pk2(gelu_tanh(pre[0]), gelu_tanh(pre[1])), pk2(gelu_tanh(pre[2]), gelu_tanh(pre[3]))};
      }
      store_tiles<7>(ro, rt0 + i, gct0, nct, 54, lane, h1);
    }
    STAMPT(3, 2);
    WAITV(0);
    if (lane == 0) signal(ws, FL_H1 + (rt0 >> 1) * 16 + c4, tag);
    STAMPT(3, 3);
  }
  {
    u32x2 bb[5];
#pragma unroll
    for (int j = 0; j < 5; ++j) bb[j] = *(const u32x2*)(a.b2 + c * 80 + j * 16 + (lane >> 4) * 4);
    GS<5> g;
    const rsrc_t ra = mkrsrc(ws + WS_H1, S * F * 2);
    const unsigned va0 = ((grp * 4 + rh * 2) * 54 + kh) * 1024 + lane * 16;
    await(ws, FL_H1 + (grp * 2 + rh) * 16 + (lane & 15), tag);
    STAMPT(4, 0);
    gemm_first<5>(g, ra, va0, va0 + 55296, 2048);
    gemm_core<5, 27, 4, true, S * F * 2>(g, (const LAS char*)smem + kh * 5120 + lane * 16, 10240, 1024, ra, va0, va0 + 55296, 2048);
    STAMPT(4, 1);
    f32x4 o[5]; pair_reduce<5>(g, smem, wid, o);
#pragma unroll
    for (int j = 0; j < 5; ++j)
      *(u32x2*)(a.out + (size_t)tok * D + c * 80 + j * 16 + (lane >> 4) * 4) =
          u32x2{pk2(o[j][0] + bflo(bb[j][0]) + bflo(hv[j][0]), o[j][1] + bfhi(bb[j][0]) + bfhi(hv[j][0])), pk2(o[j][2] + bflo(bb[j][1]) + bflo(hv[j][1]), o[j][3] + bfhi(bb[j][1]) + bfhi(hv[j][1]))};
    STAMPT(4, 2);
  }
}

DI void main_helper(const Args& a, char* smem, int head, int hw, u64 tag) {
  char* ws = a.ws;
  const int lane = threadIdx.x & 63, b = blockIdx.x;
  const char* kv = ws + WS_KV + (size_t)head * 64 * KVT;
  const unsigned lds0 = (unsigned)(uintptr_t)smem, v16 = lane * 16;
  auto issue = [&](int c) {
#pragma unroll
    for (int i = 0; i < 6; ++i) { const int pc = hw + 4 * i; if (pc < CH_PIECES) glds16x(v16, kv + (size_t)c * CH_BYTES + pc * 1024, lds0 + (c & 1) * CH_BYTES + pc * 1024); }
  };
  auto stream = [&](const char* src, unsigned dst, int n) {
#pragma unroll 1
    for (int i = 0; i < n; i += 4) { const int pc = i + hw < n ? i + hw : hw; glds16x(v16, src + pc * 1024, lds0 + dst + pc * 1024); }
  };
  issue(0);
  const int c = b >> 5, c4 = ((b >> 5) << 1) | ((b >> 3) & 1), gct0 = (c4 * 27) >> 2;
  const u16* src; int ld, f0; size_t dbase;
  if (b < 40) { src = a.wo; ld = D; f0 = b * 20; dbase = WS_WO; } else if (b < 148) { src = a.w1; ld = F; f0 = (b - 40) * 20; dbase = WS_W1; } else { src = a.w2; ld = D; f0 = (b - 148) * 20; dbase = WS_W2; }
  const int nks = b < 148 ? 20 : 54;
  const rsrc_t rw = mkrsrc(ws + dbase, D * F * 2);
#pragma unroll 1
  for (int ch = 0; ch < NCH; ++ch) {
    WAITV(0); BAR();
    if (ch + 1 < NCH) issue(ch + 1);
    if (ch == 0) {
      float cs = 0.f; int gct = 0;
      u16 e[5][8];
#pragma unroll
      for (int k = 0; k < 5; ++k) {
        const int f = f0 + hw * 5 + k; gct = f / nks; const int ks = f - gct * nks;
        const u16* p = src + (size_t)(32 * ks + 4 * (lane >> 4)) * ld + 16 * gct + (lane & 15);
#pragma unroll
        for (int j = 0; j < 8; ++j) e[k][j] = p[(16 * (j >> 2) + (j & 3)) * ld];
      }
#pragma unroll
      for (int k = 0; k < 5; ++k) {
        const int f = f0 + hw * 5 + k, gc = f / nks, ks = f - gc * nks;
        u32x4 v;
#pragma unroll
        for (int j = 0; j < 4; ++j) { v[j] = e[k][2 * j] | ((unsigned)e[k][2 * j + 1] << 16); cs += bf2f(e[k][2 * j]) + bf2f(e[k][2 * j + 1]); }
        const int di = (b >= 40 && b < 148) ? f : ((gc / 5) * nks + ks) * 5 + gc % 5;
        st16(rw, di * 1024 + lane * 16, v);
      }
      if (b >= 40 && b < 148) { cs = xq_sum(cs); if (lane < 16) __builtin_amdgcn_raw_buffer_store_b32(__float_as_uint(cs), mkrsrc(ws + WS_CS1, 4 * 1792 * 4), (hw * 1792 + 16 * gct + lane) * 4, 0, XAUX); }
    }
    if (ch == 1 && hw == 0 && lane == 0) signal(ws, FL_W + b, tag);
    if (ch == 3 && b == 0 && hw == 1) {
#pragma unroll 1
      for (int k = 0; k < 4; ++k) await(ws, FL_QKV + k * 64 + lane, tag);
      if (lane == 0) signal(ws, FL_EPOCH, tag);
    }
    if (ch == 4) {
      const int j = lane < 5 ? 5 * c + lane : (lane < 12 ? 40 + gct0 + lane - 5 : 148 + min((27 * c) / 2 + min(lane - 12, 14), 107));
      await(ws, FL_W + j, tag);
    }
    if (ch == 5 || ch == 6) stream(ws + WS_WO + (size_t)c * 102400 + (ch - 5) * 51200, LDS_WO + (ch - 5) * 51200, 50);
  }
  WAITV(0); BAR();
  const char* w1 = ws + WS_W1 + (size_t)gct0 * 20480;
  auto w1chunk = [&](int ch) {
#pragma unroll
    for (int i = 0; i < 7; ++i) glds16x(v16, w1 + (i * 20 + ch * 4 + hw) * 1024, lds0 + ((ch * 4 + hw) * 7 + i) * 1024);
  };
  w1chunk(0); w1chunk(1);
  BAR(); BAR();
  w1chunk(2); w1chunk(3); w1chunk(4);
  const char* w2 = ws + WS_W2 + (size_t)c * 276480;
  WAITV(0); BAR();
  stream(w2 + 143360, 143360, 20);
  BAR(); BAR();
  stream(w2, 0, 40);
  BAR();
  stream(w2 + 40960, 40960, 40);
  BAR();
  stream(w2 + 81920, 81920, 32);
  BAR();
  stream(w2 + 114688, 114688, 28);
  WAITV(0); BAR();
  BAR(); stream(w2 + 4 * 40960, 0, 40);
  BAR(); stream(w2 + 5 * 40960, 40960, 40);
  BAR(); stream(w2 + 6 * 40960, 81920, 30);
  WAITV(18); BAR();
  WAITV(8); BAR();
  WAITV(0); BAR();
  BAR(); BAR();
}

__global__ __launch_bounds__(512, 2) void fused_block(Args a) {
  extern __shared__ __attribute__((aligned(16))) char smem[];
  char* ws = a.ws;
  const int wid = __builtin_amdgcn_readfirstlane(threadIdx.x >> 6), lane = threadIdx.x & 63;
  const int b = blockIdx.x, xg = b & 7, j = b >> 3;
  STAMPT(0, 0);
  u64 e0;
  asm volatile("s_load_dwordx2 %0, %1, 0x0\n\ts_waitcnt lgkmcnt(0)" : "=s"(e0) : "s"(ws + WS_FL + FL_EPOCH * 8) : "memory");
  const u64 tag = e0 * 6364136223846793005ull + 1442695040888963407ull;
  const int bm = (xg >> 1) * 4 + (j & 3), bn = (xg & 1) * 8 + (j >> 2);
#define K1_ROT (j % 10)
  if (bn >= 15) k1_colsum(a.x, a.wqkv, ws, smem, bm, wid, tag);
  else if (wid >= 4) k1_helper(a.x, a.wqkv, ws, smem, bm, bn, K1_ROT, wid - 4, tag);
  else if (bn >= 10) k1_compute<true>(ws, smem, bm, bn, wid); else k1_compute<false>(ws, smem, bm, bn, wid);
  STAMPT(0, 3);
  WAITV(0); BAR();
  if (threadIdx.x == 0) signal(ws, FL_QKV + bn * 16 + bm, tag);
  STAMPT(0, 4);
  const int head = (b & 7) * 4 + ((b >> 3) & 3), qc = b >> 5;
  if (wid == 4) {
    const int t0 = (head * 20) >> 7, t1 = (head * 20 + 19) >> 7;
    await(ws, FL_QKV + (5 + (lane >> 5) * 5 + ((lane & 16) ? t1 : t0)) * 16 + (lane & 15), tag);
    await(ws, FL_QKV + ((lane & 2) ? t1 : t0) * 16 + qc * 2 + (lane & 1), tag);
  }
  BAR();
  STAMPT(1, 0);
  if (wid >= 4) main_helper(a, smem, head, wid - 4, tag);
  else { attn_compute(ws, smem, head, qc, wid, tag); STAMPT(1, 3); post_compute(a, smem, wid, tag); }
}

extern "C" void kernel_launch(void* const* d_in, const int* in_sizes, int n_in, void* d_out, int out_size, void* d_ws, size_t ws_size, hipStream_t stream) {
  Args a;
  a.x = (const u16*)d_in[0]; a.wqkv = (const u16*)d_in[1]; a.wo = (const u16*)d_in[2];
  a.w1 = (const u16*)d_in[4]; a.b1 = (const u16*)d_in[5]; a.w2 = (const u16*)d_in[6]; a.b2 = (const u16*)d_in[7];
  a.out = (u16*)d_out; a.ws = (char*)d_ws;
  (void)hipFuncSetAttribute((const void*)fused_block, hipFuncAttributeMaxDynamicSharedMemorySize, LDS_BYTES);
  fused_block<<<256, 512, LDS_BYTES, stream>>>(a);
}
```

```cpp
#include <hip/hip_runtime.h>
#include <hip/hip_bf16.h>
typedef unsigned short u16;
typedef __attribute__((ext_vector_type(8))) short bf16x8;
typedef __attribute__((ext_vector_type(8))) __bf16 bf8;
typedef __attribute__((ext_vector_type(4))) short s16x4;
typedef __attribute__((ext_vector_type(16))) float f32x16;
typedef __attribute__((ext_vector_type(4))) float f32x4;
typedef __attribute__((ext_vector_type(2))) float f32x2;
typedef __attribute__((ext_vector_type(4))) unsigned u32x4;
typedef __attribute__((ext_vector_type(2))) unsigned u32x2;
typedef __attribute__((ext_vector_type(2))) __bf16 bf2;
#define LAS __attribute__((address_space(3)))
#define DI __device__ __forceinline__
#define SBAR() __builtin_amdgcn_sched_barrier(0)
#define LGKM0() __builtin_amdgcn_s_waitcnt(0xC07F)
#define BAR() asm volatile("s_barrier" ::: "memory")
#define WAITV(n) asm volatile("s_waitcnt vmcnt(%0)" :: "i"(n) : "memory")

constexpr int S = 2048, D = 640, H = 32, DH = 20, F = 1728;
constexpr int QT = 1280, KVT = 2688;
constexpr size_t WS_Q = 0, WS_KV = WS_Q + (size_t)H * 64 * QT, WS_O = WS_KV + (size_t)H * 64 * KVT,
                 WS_H = WS_O + (size_t)S * D * 2, WS_H1 = WS_H + (size_t)S * D * 2,
                 WS_WO = WS_H1 + (size_t)S * F * 2, WS_W1 = WS_WO + (size_t)D * D * 2, WS_W2 = WS_W1 + (size_t)D * F * 2 + 20480,
                 WS_CS1 = WS_W2 + (size_t)D * F * 2, WS_ST = WS_CS1 + 4 * 1792 * 4, WS_XST = WS_ST + (size_t)S * 8 * 8, WS_CSQ = WS_XST + (size_t)S * 16, WS_FL = WS_CSQ + 16384, WS_END = WS_FL + 65536;
#define STAMPT(k, s) do {} while (0)
#define PADAT -1
#define PADT(k, us) do { if (PADAT == (k)) { while (__builtin_amdgcn_s_memrealtime() - t_start < (us) * 100ull) __builtin_amdgcn_s_sleep(2); } } while (0)
constexpr int KSKEW = 1;
constexpr float QSCALE = 0.22360679774997896f * 1.4426950408889634f;

DI float bf2f(u16 v) { return __uint_as_float((unsigned)v << 16); }
DI float bflo(unsigned u) { return __uint_as_float(u << 16); }
DI float bfhi(unsigned u) { return __uint_as_float(u & 0xffff0000u); }
DI unsigned pk2(float lo, float hi) { f32x2 v = {lo, hi}; return __builtin_bit_cast(unsigned, __builtin_convertvector(v, bf2)); }
DI void glds16(unsigned voff, const void* sbase, unsigned lds) {
  asm volatile("s_mov_b32 m0, %2\n\ts_nop 0\n\tglobal_load_lds_dwordx4 %0, %1" :: "v"(voff), "s"(sbase), "s"(__builtin_amdgcn_readfirstlane(lds)) : "memory"); }
DI void glds16x(unsigned voff, const void* sbase, unsigned lds) {
  asm volatile("s_mov_b32 m0, %2\n\ts_nop 0\n\tglobal_load_lds_dwordx4 %0, %1 sc1" :: "v"(voff), "s"(sbase), "s"(__builtin_amdgcn_readfirstlane(lds)) : "memory"); }
DI s16x4 trread(const LAS char* p) { return __builtin_amdgcn_ds_read_tr16_b64_v4i16((LAS s16x4*)p); }
DI bf16x8 cat8(s16x4 a, s16x4 b) { return __builtin_shufflevector(a, b, 0, 1, 2, 3, 4, 5, 6, 7); }
#define MFMA32(a, b, c) __builtin_amdgcn_mfma_f32_32x32x16_bf16(__builtin_bit_cast(bf8, a), __builtin_bit_cast(bf8, b), c, 0, 0, 0)
#define MFMA32K8(a, b, c) __builtin_amdgcn_mfma_f32_32x32x8bf16_1k(a, b, c, 0, 0, 0)
#define MFMA16(a, b, c) __builtin_amdgcn_mfma_f32_16x16x32_bf16(__builtin_bit_cast(bf8, a), __builtin_bit_cast(bf8, b), c, 0, 0, 0)
DI float xhalf_sum(float v) { auto r = __builtin_amdgcn_permlane32_swap(__float_as_uint(v), __float_as_uint(v), false, false); return __uint_as_float(r[0]) + __uint_as_float(r[1]); }
DI float xhalf_max(float v) { auto r = __builtin_amdgcn_permlane32_swap(__float_as_uint(v), __float_as_uint(v), false, false); return fmaxf(__uint_as_float(r[0]), __uint_as_float(r[1])); }
DI float xq_sum(float v) { auto r = __builtin_amdgcn_permlane16_swap(__float_as_uint(v), __float_as_uint(v), false, false); return xhalf_sum(__uint_as_float(r[0]) + __uint_as_float(r[1])); }
typedef __amdgpu_buffer_rsrc_t rsrc_t;
DI rsrc_t mkrsrc(const void* p, unsigned bytes) { return __builtin_amdgcn_make_buffer_rsrc((void*)p, 0, bytes, 0x00020000); }
constexpr int XAUX = 16;
DI bf16x8 ldfrag(rsrc_t r, unsigned voff, unsigned soff) { return __builtin_bit_cast(bf16x8, __builtin_amdgcn_raw_buffer_load_b128(r, voff, soff, XAUX)); }
DI void st16(rsrc_t r, unsigned voff, u32x4 v) { __builtin_amdgcn_raw_buffer_store_b128(v, r, voff, 0, XAUX); }
DI void st8(rsrc_t r, unsigned voff, u32x2 v) { __builtin_amdgcn_raw_buffer_store_b64(v, r, voff, 0, XAUX); }

DI s16x4 ldhalf(rsrc_t r, unsigned voff, unsigned soff) { return __builtin_bit_cast(s16x4, __builtin_amdgcn_raw_buffer_load_b64(r, voff, soff, XAUX)); }
DI void st16s(rsrc_t r, unsigned voff, unsigned soff, u32x4 v) { __builtin_amdgcn_raw_buffer_store_b128(v, r, voff + soff, 0, XAUX); }
DI void st8s(rsrc_t r, unsigned voff, unsigned soff, u32x2 v) { __builtin_amdgcn_raw_buffer_store_b64(v, r, voff, soff, XAUX); }
typedef unsigned long long u64;
typedef __attribute__((address_space(1))) u64 gu64;
#define RLX_AGENT __ATOMIC_RELAXED, __HIP_MEMORY_SCOPE_AGENT
constexpr int FL_EPOCH = 0, FL_QKV = 64  , FL_W = FL_QKV + 256  , FL_O = FL_W + 256  ,
              FL_H = FL_O + 1024  , FL_H1 = FL_H + 1024  ;
constexpr unsigned SPIN_CAP = 1u << 17;
DI gu64* flagp(char* ws, int i) { return (gu64*)(ws + WS_FL) + i; }
DI void signal(char* ws, int i, u64 tag) { __hip_atomic_store(flagp(ws, i), tag, RLX_AGENT); }
DI void await(char* ws, int i  , u64 tag) {
  gu64* p = flagp(ws, i);
#pragma unroll 1
  for (unsigned sp = 0; sp < SPIN_CAP; ++sp) {
    if (__all(__hip_atomic_load(p, RLX_AGENT) == tag)) break;
    __builtin_amdgcn_s_sleep(1);
  }
  asm volatile("" ::: "memory");
}

constexpr int K1_NSLOT = 4, K1_STAGE = 32768;
#define K1_PRE 2
struct F4 { bf16x8 a0, a1, b0, b1; };
template <bool isV> DI void k1_compute(char* ws, char* smem, int bm, int bn, int wid) {
  const int lane = threadIdx.x & 63, r = lane & 31, hh = lane >> 5;
  const int wm = wid >> 1, wn = wid & 1;
  f32x16 c00 = f32x16{}, c01 = f32x16{}, c10 = f32x16{}, c11 = f32x16{};
  const int fA = (r >> 1) & 7;
  const int gi = lane >> 4, q = (lane & 15) >> 2, p = lane & 3;
  const LAS char* sm = (const LAS char*)smem;
  const int aoff = (wm * 64 + wn * 32 + r) * 128;
  const int boff = 16384 + (8 * (gi >> 1) + q) * 256 + 32 * (gi & 1) + 8 * p;
  const int b0off = boff + (((2 * wn + wm) ^ q) << 6), b1off = boff + (((2 * wn + (1 ^ wm)) ^ q) << 6);
#define K1_LDA0(X, sb, s) X.a0 = *(const LAS bf16x8*)(sb + aoff + (((2 * (s) + hh) ^ fA) << 4))
#define K1_LDA1(X, sb, s) X.a1 = *(const LAS bf16x8*)(sb + (aoff ^ 4096) + (((2 * (s) + hh) ^ fA) << 4))
#define K1_LDB0(X, sb, s) X.b0 = cat8(trread(sb + b0off + (s) * 4096), trread(sb + b0off + (s) * 4096 + 1024))
#define K1_LDB1(X, sb, s) X.b1 = cat8(trread(sb + b1off + (s) * 4096), trread(sb + b1off + (s) * 4096 + 1024))
#define K1_MM(c, a, b) c = isV ? MFMA32(a, b, c) : MFMA32(b, a, c)
#define K1_STEP(X, Y, sb, s, LD) do { \
    K1_MM(c00, X.a0, X.b0); SBAR(); if (LD) { K1_LDA0(Y, sb, s); K1_LDB0(Y, sb, s); } SBAR(); \
    K1_MM(c01, X.a0, X.b1); SBAR(); if (LD) { K1_LDB1(Y, sb, s); } SBAR(); \
    K1_MM(c10, X.a1, X.b0); SBAR(); if (LD) { K1_LDA1(Y, sb, s); } SBAR(); \
    K1_MM(c11, X.a1, X.b1); SBAR(); } while (0)
  const bool isQ = bn < 5; const int tstride = isQ ? QT : KVT;
  const rsrc_t rimg = mkrsrc(ws + (isQ ? WS_Q : WS_KV), H * 64 * KVT);
  unsigned soff[8];
  if (!isV) {
#pragma unroll
    for (int u = 0; u < 8; ++u) {
      const int n = (bn % 5) * 128 + wn * 64 + ((u >> 2) ^ wm) * 32 + 8 * (u & 3) + 4 * hh, head = n / 20, d = n - head * 20;
      soff[u] = head * 64 * tstride + (d < 16 ? (d >> 3) * 512 + r * 16 + (d & 7) * 2 : 1024 + r * 8);
      asm volatile("" : "+v"(soff[u]));
    }
  } else {
#pragma unroll
    for (int jn = 0; jn < 2; ++jn) {
      const int n = (bn - 10) * 128 + wn * 64 + (jn ^ wm) * 32 + r, head = n / 20, d = n - head * 20;
      soff[jn] = head * 64 * KVT + 1280 + hh * 352 + d * 16;
      if (d >= 18) {
        const unsigned c1 = d == 18 ? 0x3F803F80u : 0u;
#pragma unroll
        for (int u = 0; u < 4; ++u) st16s(rimg, soff[jn] + (u & 1) * 704 + 32, (bm * 4 + wm * 2 + (u >> 1)) * KVT, u32x4{c1, c1, c1, c1});
      }
      asm volatile("" : "+v"(soff[jn]));
    }
  }
  F4 x, y;
  BAR();
  STAMPT(0, 1);
  K1_LDA0(x, sm, 0); K1_LDB0(x, sm, 0); K1_LDB1(x, sm, 0); K1_LDA1(x, sm, 0);
  int slot = 0;
#pragma unroll 1
  for (int st = 0; st < 10; ++st) {
    const LAS char* sb = sm + slot * K1_STAGE;
    slot = slot + 1 == K1_NSLOT ? 0 : slot + 1;
    const LAS char* sn = sm + slot * K1_STAGE;
    K1_STEP(x, y, sb, 1, true);
    K1_STEP(y, x, sb, 2, true);
    K1_STEP(x, y, sb, 3, true);
    LGKM0(); BAR(); SBAR();
    K1_STEP(y, x, sn, 0, true);
  }
  STAMPT(0, 2);
  LGKM0(); BAR();
  const LAS float* sMean = (const LAS float*)(smem + K1_NSLOT * K1_STAGE); const LAS float* sRstd = sMean + 128; const LAS float* sCs = sMean + 256;
  if (!isV) {
#pragma unroll
    for (int i = 0; i < 2; ++i) {
      const int tl = wm * 64 + (i ^ wn) * 32;
      const float m = sMean[tl + r], rstd = sRstd[tl + r] * (isQ ? QSCALE : 1.f);
      int tile = bm * 4 + wm * 2 + (i ^ wn); if (!isQ) tile = (tile + 64 - KSKEW) & 63;
#pragma unroll
      for (int jn = 0; jn < 2; ++jn) {
        const f32x16& c = i ? (jn ? c11 : c10) : (jn ? c01 : c00);
#pragma unroll
        for (int g = 0; g < 4; ++g) {
          const f32x4 c4 = *(const LAS f32x4*)(sCs + wn * 64 + (jn ^ wm) * 32 + 8 * g + 4 * hh);
          u32x2 v;
          v[0] = pk2((c[4 * g] - m * c4[0]) * rstd, (c[4 * g + 1] - m * c4[1]) * rstd);
          v[1] = pk2((c[4 * g + 2] - m * c4[2]) * rstd, (c[4 * g + 3] - m * c4[3]) * rstd);
          st8s(rimg, soff[jn * 4 + g], tile * tstride, v);
        }
      }
    }
  } else {
#pragma unroll
    for (int i = 0; i < 2; ++i) {
      const int tile = bm * 4 + wm * 2 + (i ^ wn);
#pragma unroll
      for (int s = 0; s < 2; ++s) {
        f32x4 m4[2], r4[2];
#pragma unroll
        for (int g = 0; g < 2; ++g) { const int tl = wm * 64 + (i ^ wn) * 32 + 16 * s + 8 * g + 4 * hh; m4[g] = *(const LAS f32x4*)(sMean + tl); r4[g] = *(const LAS f32x4*)(sRstd + tl); }
#pragma unroll
        for (int jn = 0; jn < 2; ++jn) {
          const f32x16& c = i ? (jn ? c11 : c10) : (jn ? c01 : c00);
          const float cs = sCs[wn * 64 + (jn ^ wm) * 32 + r];
          u32x4 v;
#pragma unroll
          for (int g = 0; g < 2; ++g) {
            const int e = 8 * s + 4 * g;
            v[2 * g] = pk2((c[e] - m4[g][0] * cs) * r4[g][0], (c[e + 1] - m4[g][1] * cs) * r4[g][1]);
            v[2 * g + 1] = pk2((c[e + 2] - m4[g][2] * cs) * r4[g][2], (c[e + 3] - m4[g][3] * cs) * r4[g][3]);
          }
          st16s(rimg, soff[jn] + s * 704, tile * KVT, v);
        }
      }
    }
  }
}
struct XRow { u32x4 a, b; u32x2 c; };
DI void xrow_load(XRow& v, const u16* x, int row0) {
  const int lane = threadIdx.x & 63;
  const char* p = (const char*)x + (size_t)row0 * 1280; const unsigned o = (lane >> 5) * 1280 + (lane & 31) * 16, o2 = (lane >> 5) * 1280 + 1024 + (lane & 31) * 8;
  asm volatile("global_load_dwordx4 %0, %3, %5\n\tglobal_load_dwordx4 %1, %3, %5 offset:512\n\tglobal_load_dwordx2 %2, %4, %5"
               : "=&v"(v.a), "=&v"(v.b), "=&v"(v.c) : "v"(o), "v"(o2), "s"(p) : "memory");
}
DI float sum32(float v) {
  v += __shfl_xor(v, 1); v += __shfl_xor(v, 2); v += __shfl_xor(v, 4); v += __shfl_xor(v, 8);
  auto r = __builtin_amdgcn_permlane16_swap(__float_as_uint(v), __float_as_uint(v), false, false); return __uint_as_float(r[0]) + __uint_as_float(r[1]);
}
DI void xrow_publish(XRow& v, char* ws, int row0, u64 tag) {
  asm volatile("" : "+v"(v.a), "+v"(v.b), "+v"(v.c));
  const int lane = threadIdx.x & 63;
  float e[20];
#pragma unroll
  for (int i = 0; i < 4; ++i) { e[2 * i] = bflo(v.a[i]); e[2 * i + 1] = bfhi(v.a[i]); e[8 + 2 * i] = bflo(v.b[i]); e[9 + 2 * i] = bfhi(v.b[i]); }
  e[16] = bflo(v.c[0]); e[17] = bfhi(v.c[0]); e[18] = bflo(v.c[1]); e[19] = bfhi(v.c[1]);
  float s = 0.f;
#pragma unroll
  for (int i = 0; i < 20; ++i) s += e[i];
  const float m = sum32(s) * (1.f / D);
  float q = 0.f;
#pragma unroll
  for (int i = 0; i < 20; ++i) { const float d = e[i] - m; q += d * d; }
  const float rstd = rsqrtf(sum32(q) * (1.f / D) + 1e-5f);
  if ((lane & 31) == 0) st16(mkrsrc(ws + WS_XST, S * 16), (row0 + (lane >> 5)) * 16, u32x4{__float_as_uint(m), (unsigned)tag, __float_as_uint(rstd), (unsigned)(tag >> 32)});
}
DI void k1_helper(const u16* __restrict__ x, const u16* __restrict__ w, char* ws, char* smem, int bm, int bn, int rot, int hw, u64 tag) {
  const int lane = threadIdx.x & 63;
  const unsigned lds0 = (unsigned)(uintptr_t)smem;
  const unsigned voffA0 = (lane >> 3) * 1280 + (((lane & 7) ^ (lane >> 4)) << 4);
  const unsigned voffA1 = (lane >> 3) * 1280 + (((lane & 7) ^ (4 + (lane >> 4))) << 4);
  const unsigned voffB = (lane >> 4) * 3840 + (((((lane & 15) >> 2) ^ (lane >> 4)) * 4 + (lane & 3)) << 4);
  const char* xa = (const char*)x + (size_t)(bm * 128 + hw * 32) * 1280;
  const char* wb = (const char*)w + (size_t)(hw * 16) * 3840 + bn * 256;
  auto issue = [&](int st) {
    int ks = st + rot; if (ks >= 10) ks -= 10;
    const unsigned dst = lds0 + (st % K1_NSLOT) * K1_STAGE + hw * 4096;
#pragma unroll
    for (int i = 0; i < 4; ++i) glds16((i & 1) ? voffA1 : voffA0, xa + i * 8 * 1280 + ks * 128, dst + i * 1024);
#pragma unroll
    for (int i = 0; i < 4; ++i) glds16(voffB, wb + (size_t)(ks * 64 + i * 4) * 3840, dst + 16384 + i * 1024);
  };
  const int row0 = blockIdx.x * 8 + hw * 2;
  XRow xr;
  issue(0);
  xrow_load(xr, x, row0);
#pragma unroll 1
  for (int st = 1; st < K1_PRE; ++st) issue(st);
  WAITV(8 * (K1_PRE - 1)); BAR();
#pragma unroll 1
  for (int st = K1_PRE; st < K1_NSLOT; ++st) issue(st);
  WAITV(8 * (K1_NSLOT - 2)); BAR();
  issue(K1_NSLOT);
  xrow_publish(xr, ws, row0, tag);
#pragma unroll 1
  for (int st = 1; st < 10 - K1_NSLOT; ++st) { WAITV(8 * (K1_NSLOT - 2)); BAR(); issue(st + K1_NSLOT); }
  WAITV(16); BAR();
  WAITV(8); BAR();
  WAITV(0); BAR();
  const rsrc_t rs = mkrsrc(ws + WS_XST, S * 16 + 16384);
  const unsigned go = lane < 32 ? (bm * 128 + hw * 32 + lane) * 16 : S * 16 + (bn * 64 + hw * 16 + (lane & 15)) * 16;
  u32x4 rec;
#pragma unroll 1
  for (unsigned sp = 0; sp < SPIN_CAP; ++sp) {
    asm volatile("" ::: "memory");
    rec = __builtin_amdgcn_raw_buffer_load_b128(rs, go, 0, XAUX);
    if (__all(rec[1] == (unsigned)tag && rec[3] == (unsigned)(tag >> 32))) break;
    __builtin_amdgcn_s_sleep(1);
  }
  LAS unsigned* st = (LAS unsigned*)(smem + K1_NSLOT * K1_STAGE);
  if (lane < 32) { st[hw * 32 + lane] = rec[0]; st[128 + hw * 32 + lane] = rec[2]; }
  else if (lane < 48) { st[256 + hw * 32 + 2 * (lane & 15)] = rec[0]; st[257 + hw * 32 + 2 * (lane & 15)] = rec[2]; }
  BAR();
  LGKM0(); BAR();
}
DI void k1_colsum(const u16* __restrict__ x, const u16* __restrict__ w, char* ws, char* smem, int u, int wid, u64 tag) {
  const int lane = threadIdx.x & 63;
  if (wid >= 4) { XRow xr; const int row0 = blockIdx.x * 8 + (wid - 4) * 2; xrow_load(xr, x, row0); WAITV(0); xrow_publish(xr, ws, row0, tag); }
  if (u < 15) {
    const char* p = (const char*)w + (size_t)(wid * 80 + (lane >> 4)) * 3840 + u * 256 + (lane & 15) * 16;
    u32x4 v[20];
#pragma unroll
    for (int i = 0; i < 20; ++i) v[i] = *(const u32x4*)(p + (size_t)i * 4 * 3840);
    float a[8];
#pragma unroll
    for (int e = 0; e < 8; ++e) a[e] = 0.f;
#pragma unroll
    for (int i = 0; i < 20; ++i)
#pragma unroll
      for (int e = 0; e < 4; ++e) { a[2 * e] += bflo(v[i][e]); a[2 * e + 1] += bfhi(v[i][e]); }
    LAS float* part = (LAS float*)smem;
#pragma unroll
    for (int e = 0; e < 8; ++e) { a[e] = xq_sum(a[e]); if (lane < 16) part[wid * 128 + lane * 8 + e] = a[e]; }
    LGKM0(); BAR();
    if (wid == 0) {
      float c0 = 0.f, c1 = 0.f;
#pragma unroll
      for (int k = 0; k < 8; ++k) { c0 += part[k * 128 + 2 * lane]; c1 += part[k * 128 + 2 * lane + 1]; }
      st16(mkrsrc(ws + WS_CSQ, 16384), (u * 64 + lane) * 16, u32x4{__float_as_uint(c0), (unsigned)tag, __float_as_uint(c1), (unsigned)(tag >> 32)});
    }
  }
}

constexpr int CH_TILES = 8, CH_BYTES = CH_TILES * KVT, NCH = 64 / CH_TILES, CH_PIECES = CH_BYTES / 1024;
#define EX(v) __builtin_amdgcn_exp2f(v)
#define PF(p) __builtin_bit_cast(bf16x8, p)
struct Frag { bf16x8 k1, v0, v1; s16x4 k2; };
#define HSTEP(SC, SN, PC0, PC1, PP0, PP1, O, X, Q1, Q2, L1, L2, L3, L4) do { \
    O = MFMA32(X.v0, PF(PP0), O); SBAR(); L1; SBAR(); \
    PP1[2] = pk2(SN[12], SN[13]); PP1[3] = pk2(SN[14], SN[15]); SBAR(); \
    SC[0] = EX(SC[0]); SC[1] = EX(SC[1]); SC[2] = EX(SC[2]); SC[3] = EX(SC[3]); SBAR(); \
    SN = MFMA32(X.k1, Q1, f32x16{}); SBAR(); L2; SBAR(); \
    SC[4] = EX(SC[4]); SC[5] = EX(SC[5]); PC0[0] = pk2(SC[0], SC[1]); SBAR(); SC[6] = EX(SC[6]); SC[7] = EX(SC[7]); PC0[1] = pk2(SC[2], SC[3]); SBAR(); \
    SN = MFMA32K8(X.k2, Q2, SN); SBAR(); L3; SBAR(); \
    SC[8] = EX(SC[8]); SC[9] = EX(SC[9]); PC0[2] = pk2(SC[4], SC[5]); SBAR(); SC[10] = EX(SC[10]); SC[11] = EX(SC[11]); PC0[3] = pk2(SC[6], SC[7]); SBAR(); \
    O = MFMA32(X.v1, PF(PP1), O); SBAR(); L4; SBAR(); \
    SC[12] = EX(SC[12]); SC[13] = EX(SC[13]); PC1[0] = pk2(SC[8], SC[9]); SBAR(); SC[14] = EX(SC[14]); SC[15] = EX(SC[15]); PC1[1] = pk2(SC[10], SC[11]); SBAR(); \
  } while (0)
#define LDV0(Y, base) Y.v0 = *(const LAS bf16x8*)((base) + voff)
#define LDV1(Y, base) Y.v1 = *(const LAS bf16x8*)((base) + voff + 704)
#define LDK1(Y, base) Y.k1 = *(const LAS bf16x8*)((base) + lane * 16)
#define LDK2(Y, base) Y.k2 = *(const LAS s16x4*)((base) + k2off)
#define NOP_ do {} while (0)
DI float rowmax16(const f32x16& s) {
  float m = s[0];
#pragma unroll
  for (int e = 1; e < 16; ++e) m = fmaxf(m, s[e]);
  return xhalf_max(m);
}
DI s16x4 shift_from(const f32x16& s, s16x4 q2, int hh) {
  const float m = -rowmax16(s);
  const unsigned mh = pk2(m, 0.f) & 0xffffu, ml = pk2(m - bflo(mh), 0.f) & 0xffffu;
  return hh ? s16x4{(short)mh, (short)ml, 0, 0} : q2;
}
DI bool attn_unsafe(const f32x16& o, int hh) {
  const float l = hh ? o[8] : o[12];
  bool bad = !(l >= 1e-20f && l <= 1e30f);
#pragma unroll
  for (int e = 0; e < 12; ++e) bad |= !(fabsf(o[e]) <= 3e38f);
  return __any(bad);
}
DI f32x16 attn_online(rsrc_t kv, bf16x8 q1, s16x4 q2, int lane, int k2off, int voff) {
  if (lane >= 32) q2 = s16x4{0, 0, 0, 0};
  float m = -INFINITY; f32x16 o = f32x16{};
#pragma unroll 1
  for (int t = 0; t < 64; ++t) {
    const int kt = ((t + 64 - KSKEW) & 63) * KVT, vt = t * KVT;
    f32x16 s = MFMA32(ldfrag(kv, lane * 16, kt), q1, f32x16{}); s = MFMA32K8(ldhalf(kv, k2off, kt), q2, s);
    const float mn = fmaxf(m, rowmax16(s)), al = EX(m - mn); m = mn;
#pragma unroll
    for (int e = 0; e < 16; ++e) { o[e] *= al; s[e] = EX(s[e] - mn); }
    const u32x4 p0 = {pk2(s[0], s[1]), pk2(s[2], s[3]), pk2(s[4], s[5]), pk2(s[6], s[7])};
    const u32x4 p1 = {pk2(s[8], s[9]), pk2(s[10], s[11]), pk2(s[12], s[13]), pk2(s[14], s[15])};
    o = MFMA32(ldfrag(kv, voff, vt), PF(p0), o); o = MFMA32(ldfrag(kv, voff + 704, vt), PF(p1), o);
  }
  return o;
}
DI void attn_compute(char* ws, char* smem, int head, int qc, int wid, u64 tag) {
  const int lane = threadIdx.x & 63, r = lane & 31, hh = lane >> 5;
  const rsrc_t kv = mkrsrc(ws + WS_KV + (size_t)head * 64 * KVT, 64 * KVT);
  const int qt = qc * 8 + wid * 2;
  const rsrc_t rq = mkrsrc(ws + WS_Q + (size_t)(head * 64 + qt) * QT, 2 * QT);
  const int k2off = hh ? 1600 : 1024 + r * 8;
  const int voff = 1280 + hh * 352 + (r < 21 ? r : (r == 24 ? 20 : 21)) * 16;
  const bf16x8 qa1 = ldfrag(rq, lane * 16, 0), qb1 = ldfrag(rq, lane * 16, QT);
  s16x4 qa2 = ldhalf(rq, 1024 + r * 8, 0), qb2 = ldhalf(rq, 1024 + r * 8, QT);
  if (hh) { qa2 = s16x4{0, 0, 0, 0}; qb2 = s16x4{0, 0, 0, 0}; }
  Frag x, y;
  x.k1 = ldfrag(kv, lane * 16, 63 * KVT); x.k2 = ldhalf(kv, k2off, 63 * KVT);
  x.v0 = bf16x8{}; x.v1 = bf16x8{};
  const LAS char* sm = (const LAS char*)smem;
  f32x16 sa, sb, oa = f32x16{}, ob = f32x16{};
  u32x4 pa0 = {0, 0, 0, 0}, pa1 = {0, 0, 0, 0}, pb0 = {0, 0, 0, 0}, pb1 = {0, 0, 0, 0};
  sa = MFMA32(x.k1, qa1, f32x16{}); sa = MFMA32K8(x.k2, qa2, sa); qa2 = shift_from(sa, qa2, hh);
  sb = MFMA32(x.k1, qb1, f32x16{}); sb = MFMA32K8(x.k2, qb2, sb); qb2 = shift_from(sb, qb2, hh);
  sa = f32x16{}; sb = f32x16{};
#define ITER(X, Y, base) do { \
    HSTEP(sb, sa, pb0, pb1, pa0, pa1, oa, X, qa1, qa2, LDV0(Y, base), LDK1(Y, base), LDK2(Y, base), LDV1(Y, base)); \
    HSTEP(sa, sb, pa0, pa1, pb0, pb1, ob, X, qb1, qb2, NOP_, NOP_, NOP_, LGKM0()); } while (0)
#pragma unroll 1
  for (int c = 0; c < NCH; c += 2) {
#pragma unroll
    for (int t = 0; t < 2 * CH_TILES; t += 2) {
      if (t % CH_TILES == 0) { BAR(); SBAR(); if (c == 0 && t == 0) STAMPT(1, 1); }
      ITER(x, y, sm + t * KVT); ITER(y, x, sm + (t + 1) * KVT);
    }
  }
  BAR(); SBAR();
  HSTEP(sb, sa, pb0, pb1, pa0, pa1, oa, x, qa1, qa2, NOP_, NOP_, NOP_, NOP_);
  pb1[2] = pk2(sb[12], sb[13]); pb1[3] = pk2(sb[14], sb[15]);
  ob = MFMA32(x.v0, PF(pb0), ob); ob = MFMA32(x.v1, PF(pb1), ob);
  STAMPT(1, 2);
  if (attn_unsafe(oa, hh)) oa = attn_online(kv, qa1, qa2, lane, k2off, voff);
  if (attn_unsafe(ob, hh)) ob = attn_online(kv, qb1, qb2, lane, k2off, voff);
  const rsrc_t ro = mkrsrc(ws + WS_O, S * D * 2);
#pragma unroll
  for (int u = 0; u < 2; ++u) {
    const f32x16& o = u ? ob : oa;
    const float il = 1.f / (hh ? o[8] : o[12]);
    const int rt = (qt + u) * 2 + (r >> 4);
#pragma unroll
    for (int g = 0; g < 3; ++g) {
      const int k = head * 20 + 8 * g + 4 * hh;
      if (g < 2 || hh == 0) st8(ro, (rt * 20 + (k >> 5)) * 1024 + (((k >> 2) & 3) * 16 + (r & 15)) * 16 + ((k >> 4) & 1) * 8,
                                u32x2{pk2(o[4 * g] * il, o[4 * g + 1] * il), pk2(o[4 * g + 2] * il, o[4 * g + 3] * il)});
    }
  }
  WAITV(0);
  if (lane == 0) signal(ws, FL_O + (qc * 4 + wid) * 32 + head, tag);
}

template <int NC> struct GS { f32x4 acc[2][NC]; bf16x8 a[4][2]; bf16x8 b[2][NC]; };
template <int NC, int s, bool LDB> DI void gstep(GS<NC>& g, const LAS char* nb, int cstride, rsrc_t ra, unsigned va0, unsigned va1, unsigned soff) {
#pragma unroll
  for (int m = 0; m < 2 * NC; ++m) {
    g.acc[m & 1][m >> 1] = MFMA16(g.b[s & 1][m >> 1], g.a[s][m & 1], g.acc[m & 1][m >> 1]); SBAR();
    if (LDB && m < NC) { g.b[(s + 1) & 1][m] = *(const LAS bf16x8*)(nb + m * cstride); SBAR(); }
  }
  g.a[s][0] = ldfrag(ra, va0, soff); g.a[s][1] = ldfrag(ra, va1, soff); SBAR();
}
template <int NC, int NKS, int NSLOT, bool BARS, unsigned OOB  > DI void gemm_core(GS<NC>& g, const LAS char* wl, int sstride, int cstride, rsrc_t ra, unsigned va0, unsigned va1, unsigned astep) {
  constexpr int NIT = (NKS - 1) / 4, TAIL = NKS - 4 * NIT;
#pragma unroll
  for (int i = 0; i < 2; ++i)
#pragma unroll
    for (int j = 0; j < NC; ++j) g.acc[i][j] = f32x4{};
  if (BARS) { BAR(); SBAR(); }
#pragma unroll
  for (int j = 0; j < NC; ++j) g.b[0][j] = *(const LAS bf16x8*)(wl + j * cstride);
  int slot = 0;
#pragma unroll 1
  for (int it = 0; it < NIT; ++it) {
    const LAS char* cb = wl + slot * 4 * sstride;
    slot = slot + 1 == NSLOT ? 0 : slot + 1;
    const LAS char* nb = wl + slot * 4 * sstride;
    const int i4 = 4 * it + 4;
    LGKM0(); gstep<NC, 0, true>(g, cb + sstride, cstride, ra, va0, va1, i4 * astep);
    LGKM0(); gstep<NC, 1, true>(g, cb + 2 * sstride, cstride, ra, va0, va1, i4 + 1 < NKS ? (i4 + 1) * astep : OOB);
    LGKM0(); gstep<NC, 2, true>(g, cb + 3 * sstride, cstride, ra, va0, va1, i4 + 2 < NKS ? (i4 + 2) * astep : OOB);
    LGKM0(); if (BARS) { BAR(); SBAR(); }
    gstep<NC, 3, true>(g, nb, cstride, ra, va0, va1, i4 + 3 < NKS ? (i4 + 3) * astep : OOB);
  }
  const LAS char* cb = wl + slot * 4 * sstride;
#pragma unroll
  for (int s = 0; s < TAIL; ++s) {
    LGKM0();
#pragma unroll
    for (int m = 0; m < 2 * NC; ++m) {
      g.acc[m & 1][m >> 1] = MFMA16(g.b[s & 1][m >> 1], g.a[s][m & 1], g.acc[m & 1][m >> 1]); SBAR();
      if (s + 1 < TAIL && m < NC) { g.b[(s + 1) & 1][m] = *(const LAS bf16x8*)(cb + (s + 1) * sstride + m * cstride); SBAR(); }
    }
  }
}
template <int NC> DI void gemm_first(GS<NC>& g, rsrc_t ra, unsigned va0, unsigned va1, unsigned astep) {
#pragma unroll
  for (int s = 0; s < 4; ++s) { g.a[s][0] = ldfrag(ra, va0, s * astep); g.a[s][1] = ldfrag(ra, va1, s * astep); }
}
template <int NC> DI void pair_reduce(GS<NC>& g, char* smem, int wid, f32x4 (&out)[NC]) {
  const int lane = threadIdx.x & 63, kh = wid >> 1;
  LAS f32x4* red = (LAS f32x4*)(smem + 143360) + lane;
  BAR();
#pragma unroll
  for (int j = 0; j < NC; ++j) red[(wid * NC + j) * 64] = kh ? g.acc[0][j] : g.acc[1][j];
  LGKM0(); BAR();
#pragma unroll
  for (int j = 0; j < NC; ++j) out[j] = (kh ? g.acc[1][j] : g.acc[0][j]) + red[((wid ^ 2) * NC + j) * 64];
}
DI unsigned piece_off(int rt, int gct, int nks, int lane) { return (rt * nks + (gct >> 1)) * 1024 + lane * 16 + (gct & 1) * 8; }
template <int NC> DI void store_tiles(rsrc_t r, int rt, int gct0, int nct, int nks, int lane, const u32x2 (&v)[NC]) {
  if (gct0 & 1) {
    st8(r, piece_off(rt, gct0, nks, lane), v[0]);
#pragma unroll
    for (int j = 1; j + 1 < NC; j += 2) {
      if (j + 1 < nct) st16(r, piece_off(rt, gct0 + j, nks, lane), u32x4{v[j][0], v[j][1], v[j + 1][0], v[j + 1][1]});
      else st8(r, piece_off(rt, gct0 + j, nks, lane), v[j]);
    }
  } else {
#pragma unroll
    for (int j = 0; j + 1 < NC; j += 2) st16(r, piece_off(rt, gct0 + j, nks, lane), u32x4{v[j][0], v[j][1], v[j + 1][0], v[j + 1][1]});
    if (nct == NC) st8(r, piece_off(rt, gct0 + NC - 1, nks, lane), v[NC - 1]);
  }
}

DI float gelu_tanh(float v) {
  const float t = v * (-2.302208198f + -0.1029432397f * v * v);
  return v * __builtin_amdgcn_rcpf(1.f + EX(t));
}

constexpr int LDS_WO = 61440, LDS_BYTES = 163840;
struct Args { const u16 *x, *wqkv, *wo, *w1, *b1, *w2, *b2; u16* out; char* ws; };

DI void post_compute(const Args& a, char* smem, int wid, u64 tag) {
  char* ws = a.ws;
  const int lane = threadIdx.x & 63, b = blockIdx.x;
  const int grp = (b & 7) * 4 + ((b >> 3) & 3), c = b >> 5, rh = wid & 1, kh = wid >> 1;
  const int rt = grp * 4 + rh * 2 + kh, tok = rt * 16 + (lane & 15);
  u32x2 hv[5];
  {
    u32x2 xr[5];
#pragma unroll
    for (int j = 0; j < 5; ++j) xr[j] = *(const u32x2*)(a.x + (size_t)tok * D + c * 80 + j * 16 + (lane >> 4) * 4);
    GS<5> g;
    const rsrc_t ra = mkrsrc(ws + WS_O, S * D * 2);
    const unsigned va0 = ((grp * 4 + rh * 2) * 20 + kh) * 1024 + lane * 16;
    await(ws, FL_O + grp * 32 + (lane & 31), tag);
    STAMPT(2, 0);
    gemm_first<5>(g, ra, va0, va0 + 20480, 2048);
    gemm_core<5, 10, 4, false, S * D * 2>(g, (const LAS char*)smem + LDS_WO + kh * 5120 + lane * 16, 10240, 1024, ra, va0, va0 + 20480, 2048);
    STAMPT(2, 1);
    f32x4 o[5]; pair_reduce<5>(g, smem, wid, o);
    float s1 = 0.f, s2 = 0.f;
#pragma unroll
    for (int j = 0; j < 5; ++j) {
      hv[j] = u32x2{pk2(o[j][0] + bflo(xr[j][0]), o[j][1] + bfhi(xr[j][0])), pk2(o[j][2] + bflo(xr[j][1]), o[j][3] + bfhi(xr[j][1]))};
#pragma unroll
      for (int e = 0; e < 2; ++e) { const float lo = bflo(hv[j][e]), hi = bfhi(hv[j][e]); s1 += lo + hi; s2 += lo * lo + hi * hi; }
    }
    store_tiles<5>(mkrsrc(ws + WS_H, S * D * 2), rt, c * 5, 5, 20, lane, hv);
    s1 = xq_sum(s1); s2 = xq_sum(s2);
    if (lane < 16) st8(mkrsrc(ws + WS_ST, S * 64), tok * 64 + c * 8, u32x2{__float_as_uint(s1), __float_as_uint(s2)});
    STAMPT(2, 2);
    WAITV(0);
    if (lane == 0) signal(ws, FL_H + rt * 8 + c, tag);
    STAMPT(2, 3);
  }
  {
    const int g4 = (b & 7) * 2 + ((b >> 4) & 1), c4 = ((b >> 5) << 1) | ((b >> 3) & 1);
    const int gct0 = (c4 * 27) >> 2, nct = (((c4 + 1) * 27) >> 2) - gct0;
    f32x4 cs[7]; u32x2 bb[7];
    const rsrc_t rc = mkrsrc(ws + WS_CS1, 4 * 1792 * 4);
#pragma unroll
    for (int j = 0; j < 7; ++j) {
      const int n = (gct0 + j) * 16 + (lane >> 4) * 4;
      f32x4 t[4];
#pragma unroll
      for (int k = 0; k < 4; ++k) t[k] = __builtin_bit_cast(f32x4, __builtin_amdgcn_raw_buffer_load_b128(rc, n * 4, k * 7168, XAUX));
      cs[j] = (t[0] + t[1]) + (t[2] + t[3]);
      bb[j] = *(const u32x2*)(a.b1 + n);
    }
    GS<7> g;
    const rsrc_t ra = mkrsrc(ws + WS_H, S * D * 2);
    const int rt0 = g4 * 8 + wid * 2;
    const unsigned va0 = rt0 * 20480 + lane * 16;
#pragma unroll
    for (int j = 0; j < 7; ++j) asm volatile("" : "+v"(cs[j]), "+v"(bb[j]));
    await(ws, FL_H + rt0 * 8 + (lane & 15), tag);
    STAMPT(3, 0);
    u32x4 ps[2];
#pragma unroll
    for (int i = 0; i < 2; ++i) ps[i] = __builtin_amdgcn_raw_buffer_load_b128(mkrsrc(ws + WS_ST, S * 64), ((rt0 + i) * 16 + (lane & 15)) * 64 + (lane >> 4) * 16, 0, XAUX);
    gemm_first<7>(g, ra, va0, va0 + 20480, 1024);
    gemm_core<7, 20, 5, true, S * D * 2>(g, (const LAS char*)smem + lane * 16, 7168, 1024, ra, va0, va0 + 20480, 1024);
    BAR();
    STAMPT(3, 1);
    const rsrc_t ro = mkrsrc(ws + WS_H1, S * F * 2);
#pragma unroll
    for (int i = 0; i < 2; ++i) {
      const float m = xq_sum(__uint_as_float(ps[i][0]) + __uint_as_float(ps[i][2])) * (1.f / D);
      const float var = fmaxf(xq_sum(__uint_as_float(ps[i][1]) + __uint_as_float(ps[i][3])) * (1.f / D) - m * m, 0.f);
      const float r1 = rsqrtf(var + 1e-5f);
      const float rstd = r1 * rsqrtf(var * r1 * r1 + 1e-5f);
      u32x2 h1[7];
#pragma unroll
      for (int j = 0; j < 7; ++j) {
        const f32x4 bv = {bflo(bb[j][0]), bfhi(bb[j][0]), bflo(bb[j][1]), bfhi(bb[j][1])};
        const f32x4 pre = (g.acc[i][j] - m * cs[j]) * rstd + bv;
        h1[j] = u32x2{pk2(gelu_tanh(pre[0]), gelu_tanh(pre[1])), pk2(gelu_tanh(pre[2]), gelu_tanh(pre[3]))};
      }
      store_tiles<7>(ro, rt0 + i, gct0, nct, 54, lane, h1);
    }
    STAMPT(3, 2);
    WAITV(0);
    if (lane == 0) signal(ws, FL_H1 + (rt0 >> 1) * 16 + c4, tag);
    STAMPT(3, 3);
  }
  {
    u32x2 bb[5];
#pragma unroll
    for (int j = 0; j < 5; ++j) bb[j] = *(const u32x2*)(a.b2 + c * 80 + j * 16 + (lane >> 4) * 4);
    GS<5> g;
    const rsrc_t ra = mkrsrc(ws + WS_H1, S * F * 2);
    const unsigned va0 = ((grp * 4 + rh * 2) * 54 + kh) * 1024 + lane * 16;
    await(ws, FL_H1 + (grp * 2 + rh) * 16 + (lane & 15), tag);
    STAMPT(4, 0);
    gemm_first<5>(g, ra, va0, va0 + 55296, 2048);
    gemm_core<5, 27, 4, true, S * F * 2>(g, (const LAS char*)smem + kh * 5120 + lane * 16, 10240, 1024, ra, va0, va0 + 55296, 2048);
    STAMPT(4, 1);
    f32x4 o[5]; pair_reduce<5>(g, smem, wid, o);
#pragma unroll
    for (int j = 0; j < 5; ++j)
      *(u32x2*)(a.out + (size_t)tok * D + c * 80 + j * 16 + (lane >> 4) * 4) =
          u32x2{pk2(o[j][0] + bflo(bb[j][0]) + bflo(hv[j][0]), o[j][1] + bfhi(bb[j][0]) + bfhi(hv[j][0])), pk2(o[j][2] + bflo(bb[j][1]) + bflo(hv[j][1]), o[j][3] + bfhi(bb[j][1]) + bfhi(hv[j][1]))};
    STAMPT(4, 2);
  }
}

DI void main_helper(const Args& a, char* smem, int head, int hw, u64 tag) {
  char* ws = a.ws;
  const int lane = threadIdx.x & 63, b = blockIdx.x;
  const char* kv = ws + WS_KV + (size_t)head * 64 * KVT;
  const unsigned lds0 = (unsigned)(uintptr_t)smem, v16 = lane * 16;
  auto issue = [&](int c) {
#pragma unroll
    for (int i = 0; i < 6; ++i) { const int pc = hw + 4 * i; if (pc < CH_PIECES) glds16x(v16, kv + (size_t)c * CH_BYTES + pc * 1024, lds0 + (c & 1) * CH_BYTES + pc * 1024); }
  };
  auto stream = [&](const char* src, unsigned dst, int n) {
#pragma unroll 1
    for (int i = 0; i < n; i += 4) { const int pc = i + hw < n ? i + hw : hw; glds16x(v16, src + pc * 1024, lds0 + dst + pc * 1024); }
  };
  issue(0);
  const int c = b >> 5, c4 = ((b >> 5) << 1) | ((b >> 3) & 1), gct0 = (c4 * 27) >> 2;
  const u16* src; int ld, f0; size_t dbase;
  if (b < 40) { src = a.wo; ld = D; f0 = b * 20; dbase = WS_WO; } else if (b < 148) { src = a.w1; ld = F; f0 = (b - 40) * 20; dbase = WS_W1; } else { src = a.w2; ld = D; f0 = (b - 148) * 20; dbase = WS_W2; }
  const int nks = b < 148 ? 20 : 54;
  const rsrc_t rw = mkrsrc(ws + dbase, D * F * 2);
#pragma unroll 1
  for (int ch = 0; ch < NCH; ++ch) {
    WAITV(0); BAR();
    if (ch + 1 < NCH) issue(ch + 1);
    if (ch == 0) {
      float cs = 0.f; int gct = 0;
      u16 e[5][8];
#pragma unroll
      for (int k = 0; k < 5; ++k) {
        const int f = f0 + hw * 5 + k; gct = f / nks; const int ks = f - gct * nks;
        const u16* p = src + (size_t)(32 * ks + 4 * (lane >> 4)) * ld + 16 * gct + (lane & 15);
#pragma unroll
        for (int j = 0; j < 8; ++j) e[k][j] = p[(16 * (j >> 2) + (j & 3)) * ld];
      }
#pragma unroll
      for (int k = 0; k < 5; ++k) {
        const int f = f0 + hw * 5 + k, gc = f / nks, ks = f - gc * nks;
        u32x4 v;
#pragma unroll
        for (int j = 0; j < 4; ++j) { v[j] = e[k][2 * j] | ((unsigned)e[k][2 * j + 1] << 16); cs += bf2f(e[k][2 * j]) + bf2f(e[k][2 * j + 1]); }
        const int di = (b >= 40 && b < 148) ? f : ((gc / 5) * nks + ks) * 5 + gc % 5;
        st16(rw, di * 1024 + lane * 16, v);
      }
      if (b >= 40 && b < 148) { cs = xq_sum(cs); if (lane < 16) __builtin_amdgcn_raw_buffer_store_b32(__float_as_uint(cs), mkrsrc(ws + WS_CS1, 4 * 1792 * 4), (hw * 1792 + 16 * gct + lane) * 4, 0, XAUX); }
    }
    if (ch == 1 && hw == 0 && lane == 0) signal(ws, FL_W + b, tag);
    if (ch == 3 && b == 0 && hw == 1) {
#pragma unroll 1
      for (int k = 0; k < 4; ++k) await(ws, FL_QKV + k * 64 + lane, tag);
      if (lane == 0) signal(ws, FL_EPOCH, tag);
    }
    if (ch == 4) {
      const int j = lane < 5 ? 5 * c + lane : (lane < 12 ? 40 + gct0 + lane - 5 : 148 + min((27 * c) / 2 + min(lane - 12, 14), 107));
      await(ws, FL_W + j, tag);
    }
    if (ch == 5 || ch == 6) stream(ws + WS_WO + (size_t)c * 102400 + (ch - 5) * 51200, LDS_WO + (ch - 5) * 51200, 50);
  }
  WAITV(0); BAR();
  const char* w1 = ws + WS_W1 + (size_t)gct0 * 20480;
  auto w1chunk = [&](int ch) {
#pragma unroll
    for (int i = 0; i < 7; ++i) glds16x(v16, w1 + (i * 20 + ch * 4 + hw) * 1024, lds0 + ((ch * 4 + hw) * 7 + i) * 1024);
  };
  w1chunk(0); w1chunk(1);
  BAR(); BAR();
  w1chunk(2); w1chunk(3); w1chunk(4);
  const char* w2 = ws + WS_W2 + (size_t)c * 276480;
  WAITV(0); BAR();
  stream(w2 + 143360, 143360, 20);
  BAR(); BAR();
  stream(w2, 0, 40);
  BAR();
  stream(w2 + 40960, 40960, 40);
  BAR();
  stream(w2 + 81920, 81920, 32);
  BAR();
  stream(w2 + 114688, 114688, 28);
  WAITV(0); BAR();
  BAR(); stream(w2 + 4 * 40960, 0, 40);
  BAR(); stream(w2 + 5 * 40960, 40960, 40);
  BAR(); stream(w2 + 6 * 40960, 81920, 30);
  WAITV(18); BAR();
  WAITV(8); BAR();
  WAITV(0); BAR();
  BAR(); BAR();
}

__global__ __launch_bounds__(512, 2) void fused_block(Args a) {
  extern __shared__ __attribute__((aligned(16))) char smem[];
  char* ws = a.ws;
  const int wid = __builtin_amdgcn_readfirstlane(threadIdx.x >> 6), lane = threadIdx.x & 63;
  const int b = blockIdx.x, xg = b & 7, j = b >> 3;
  STAMPT(0, 0);
  const u64 t_start = PADAT >= 0 ? __builtin_amdgcn_s_memrealtime() : 0;
  u64 e0;
  asm volatile("s_load_dwordx2 %0, %1, 0x0\n\ts_waitcnt lgkmcnt(0)" : "=s"(e0) : "s"(ws + WS_FL + FL_EPOCH * 8) : "memory");
  const u64 tag = e0 * 6364136223846793005ull + 1442695040888963407ull;
  const int bm = (xg >> 1) * 4 + (j & 3), bn = (xg & 1) * 8 + (j >> 2);
#define K1_ROT (j % 10)
  if (bn >= 15) k1_colsum(a.x, a.wqkv, ws, smem, bm, wid, tag);
  else if (wid >= 4) k1_helper(a.x, a.wqkv, ws, smem, bm, bn, K1_ROT, wid - 4, tag);
  else if (bn >= 10) k1_compute<true>(ws, smem, bm, bn, wid); else k1_compute<false>(ws, smem, bm, bn, wid);
  STAMPT(0, 3);
  WAITV(0); BAR();
  if (threadIdx.x == 0) signal(ws, FL_QKV + bn * 16 + bm, tag);
  STAMPT(0, 4);
  const int head = (b & 7) * 4 + ((b >> 3) & 3), qc = b >> 5;
  if (wid == 4) {
    const int t0 = (head * 20) >> 7, t1 = (head * 20 + 19) >> 7;
    await(ws, FL_QKV + (5 + (lane >> 5) * 5 + ((lane & 16) ? t1 : t0)) * 16 + (lane & 15), tag);
    await(ws, FL_QKV + ((lane & 2) ? t1 : t0) * 16 + qc * 2 + (lane & 1), tag);
  }
  PADT(1, 30);
  BAR();
  STAMPT(1, 0);
  if (wid >= 4) main_helper(a, smem, head, wid - 4, tag);
  else { attn_compute(ws, smem, head, qc, wid, tag); STAMPT(1, 3); PADT(2, 60); post_compute(a, smem, wid, tag); PADT(0, 100); }
}

extern "C" void kernel_launch(void* const* d_in, const int* in_sizes, int n_in, void* d_out, int out_size, void* d_ws, size_t ws_size, hipStream_t stream) {
  Args a;
  a.x = (const u16*)d_in[0]; a.wqkv = (const u16*)d_in[1]; a.wo = (const u16*)d_in[2];
  a.w1 = (const u16*)d_in[4]; a.b1 = (const u16*)d_in[5]; a.w2 = (const u16*)d_in[6]; a.b2 = (const u16*)d_in[7];
  a.out = (u16*)d_out; a.ws = (char*)d_ws;
  (void)hipFuncSetAttribute((const void*)fused_block, hipFuncAttributeMaxDynamicSharedMemorySize, LDS_BYTES);
  fused_block<<<256, 512, LDS_BYTES, stream>>>(a);
}
```

```cpp
#include <hip/hip_runtime.h>
#include <hip/hip_bf16.h>
typedef unsigned short u16;
typedef __attribute__((ext_vector_type(8))) short bf16x8;
typedef __attribute__((ext_vector_type(8))) __bf16 bf8;
typedef __attribute__((ext_vector_type(4))) short s16x4;
typedef __attribute__((ext_vector_type(16))) float f32x16;
typedef __attribute__((ext_vector_type(4))) float f32x4;
typedef __attribute__((ext_vector_type(2))) float f32x2;
typedef __attribute__((ext_vector_type(4))) unsigned u32x4;
typedef __attribute__((ext_vector_type(2))) unsigned u32x2;
typedef __attribute__((ext_vector_type(2))) __bf16 bf2;
#define LAS __attribute__((address_space(3)))
#define DI __device__ __forceinline__
#define SBAR() __builtin_amdgcn_sched_barrier(0)
#define LGKM0() __builtin_amdgcn_s_waitcnt(0xC07F)
#define BAR() asm volatile("s_barrier" ::: "memory")
#define WAITV(n) asm volatile("s_waitcnt vmcnt(%0)" :: "i"(n) : "memory")

constexpr int S = 2048, D = 640, H = 32, DH = 20, F = 1728;
constexpr int QT = 1280, KVT = 2688;
constexpr size_t WS_Q = 0, WS_KV = WS_Q + (size_t)H * 64 * QT, WS_O = WS_KV + (size_t)H * 64 * KVT,
                 WS_H = WS_O + (size_t)S * D * 2, WS_H1 = WS_H + (size_t)S * D * 2,
                 WS_WO = WS_H1 + (size_t)S * F * 2, WS_W1 = WS_WO + (size_t)D * D * 2, WS_W2 = WS_W1 + (size_t)D * F * 2 + 20480,
                 WS_CS1 = WS_W2 + (size_t)D * F * 2, WS_ST = WS_CS1 + 4 * 1792 * 4, WS_XST = WS_ST + (size_t)S * 8 * 8, WS_CSQ = WS_XST + (size_t)S * 16, WS_FL = WS_CSQ + 16384, WS_END = WS_FL + 65536;
#define STAMPT(k, s) do {} while (0)
#define STAMPH(k, s) do {} while (0)
#define PADAT -1
#define PADT(k, us) do { if (PADAT == (k)) { while (__builtin_amdgcn_s_memrealtime() - t_start < (us) * 100ull) __builtin_amdgcn_s_sleep(2); } } while (0)
constexpr int KSKEW = 1;
constexpr float QSCALE = 0.22360679774997896f * 1.4426950408889634f;

DI float bf2f(u16 v) { return __uint_as_float((unsigned)v << 16); }
DI float bflo(unsigned u) { return __uint_as_float(u << 16); }
DI float bfhi(unsigned u) { return __uint_as_float(u & 0xffff0000u); }
DI unsigned pk2(float lo, float hi) { f32x2 v = {lo, hi}; return __builtin_bit_cast(unsigned, __builtin_convertvector(v, bf2)); }
DI void glds16(unsigned voff, const void* sbase, unsigned lds) {
  asm volatile("s_mov_b32 m0, %2\n\ts_nop 0\n\tglobal_load_lds_dwordx4 %0, %1" :: "v"(voff), "s"(sbase), "s"(__builtin_amdgcn_readfirstlane(lds)) : "memory"); }
DI void glds16x(unsigned voff, const void* sbase, unsigned lds) {
  asm volatile("s_mov_b32 m0, %2\n\ts_nop 0\n\tglobal_load_lds_dwordx4 %0, %1 sc1" :: "v"(voff), "s"(sbase), "s"(__builtin_amdgcn_readfirstlane(lds)) : "memory"); }
DI s16x4 trread(const LAS char* p) { return __builtin_amdgcn_ds_read_tr16_b64_v4i16((LAS s16x4*)p); }
DI bf16x8 cat8(s16x4 a, s16x4 b) { return __builtin_shufflevector(a, b, 0, 1, 2, 3, 4, 5, 6, 7); }
#define MFMA32(a, b, c) __builtin_amdgcn_mfma_f32_32x32x16_bf16(__builtin_bit_cast(bf8, a), __builtin_bit_cast(bf8, b), c, 0, 0, 0)
#define MFMA32K8(a, b, c) __builtin_amdgcn_mfma_f32_32x32x8bf16_1k(a, b, c, 0, 0, 0)
#define MFMA16(a, b, c) __builtin_amdgcn_mfma_f32_16x16x32_bf16(__builtin_bit_cast(bf8, a), __builtin_bit_cast(bf8, b), c, 0, 0, 0)
DI void mfma16_acc(f32x4& c, bf16x8 a, bf16x8 b) { asm volatile("v_mfma_f32_16x16x32_bf16 %0, %1, %2, %0" : "+v"(c) : "v"(a), "v"(b)); }
DI float xhalf_sum(float v) { auto r = __builtin_amdgcn_permlane32_swap(__float_as_uint(v), __float_as_uint(v), false, false); return __uint_as_float(r[0]) + __uint_as_float(r[1]); }
DI float xhalf_max(float v) { auto r = __builtin_amdgcn_permlane32_swap(__float_as_uint(v), __float_as_uint(v), false, false); return fmaxf(__uint_as_float(r[0]), __uint_as_float(r[1])); }
DI float xq_sum(float v) { auto r = __builtin_amdgcn_permlane16_swap(__float_as_uint(v), __float_as_uint(v), false, false); return xhalf_sum(__uint_as_float(r[0]) + __uint_as_float(r[1])); }
typedef __amdgpu_buffer_rsrc_t rsrc_t;
DI rsrc_t mkrsrc(const void* p, unsigned bytes) { return __builtin_amdgcn_make_buffer_rsrc((void*)p, 0, bytes, 0x00020000); }
constexpr int XAUX = 16;
DI bf16x8 ldfrag(rsrc_t r, unsigned voff, unsigned soff) { return __builtin_bit_cast(bf16x8, __builtin_amdgcn_raw_buffer_load_b128(r, voff, soff, XAUX)); }
DI void st16(rsrc_t r, unsigned voff, u32x4 v) { __builtin_amdgcn_raw_buffer_store_b128(v, r, voff, 0, XAUX); }
DI void st8(rsrc_t r, unsigned voff, u32x2 v) { __builtin_amdgcn_raw_buffer_store_b64(v, r, voff, 0, XAUX); }

DI s16x4 ldhalf(rsrc_t r, unsigned voff, unsigned soff) { return __builtin_bit_cast(s16x4, __builtin_amdgcn_raw_buffer_load_b64(r, voff, soff, XAUX)); }
DI void st16s(rsrc_t r, unsigned voff, unsigned soff, u32x4 v) { __builtin_amdgcn_raw_buffer_store_b128(v, r, voff + soff, 0, XAUX); }
DI void st8s(rsrc_t r, unsigned voff, unsigned soff, u32x2 v) { __builtin_amdgcn_raw_buffer_store_b64(v, r, voff, soff, XAUX); }
typedef unsigned long long u64;
typedef __attribute__((address_space(1))) u64 gu64;
#define RLX_AGENT __ATOMIC_RELAXED, __HIP_MEMORY_SCOPE_AGENT
constexpr int FL_EPOCH = 0, FL_QKV = 64  , FL_W = FL_QKV + 256  , FL_O = FL_W + 256  ,
              FL_H = FL_O + 1024  , FL_H1 = FL_H + 1024  ;
constexpr unsigned SPIN_CAP = 1u << 17;
DI u64 get_tag(char* ws) {
  u64 e;
  asm volatile("s_load_dwordx2 %0, %1, 0x0\n\ts_waitcnt lgkmcnt(0)" : "=s"(e) : "s"(ws + WS_FL + FL_EPOCH * 8) : "memory");
  return e * 6364136223846793005ull + 1442695040888963407ull;
}
DI gu64* flagp(char* ws, int i) { return (gu64*)(ws + WS_FL) + i; }
DI void signal(char* ws, int i, u64 tag) { __hip_atomic_store(flagp(ws, i), tag, RLX_AGENT); }
DI void await(char* ws, int i  , u64 tag) {
  gu64* p = flagp(ws, i);
#pragma unroll 1
  for (unsigned sp = 0; sp < SPIN_CAP; ++sp) {
    if (__all(__hip_atomic_load(p, RLX_AGENT) == tag)) break;
    __builtin_amdgcn_s_sleep(1);
  }
  asm volatile("" ::: "memory");
}

DI void lds_post(unsigned addr, unsigned one) { asm volatile("s_mov_b64 exec, 1\n\tds_add_u32 %0, %1\n\ts_mov_b64 exec, -1" :: "v"(addr), "v"(one) : "memory"); }
template <int NAP = 1> DI void lds_wait(const LAS char* p) {
#pragma unroll 1
  for (unsigned sp = 0; sp < (1u << 22) && __builtin_amdgcn_readfirstlane(*(const volatile LAS unsigned*)p) < 4u; ++sp) __builtin_amdgcn_s_sleep(NAP);
  asm volatile("" ::: "memory");
}
#define NAP1 3
#define NAP2 12
constexpr int CTL1 = 133120  , CTL2 = 57344  ;

constexpr int K1_NSLOT = 4, K1_STAGE = 32768;
#define K1_PRE 2
template <int CTRL> DI float dpp_add(float v) { return v + __builtin_bit_cast(float, __builtin_amdgcn_update_dpp(0, __builtin_bit_cast(int, v), CTRL, 0xf, 0xf, true)); }
DI float sum32(float v) {
  v = dpp_add<0x128>(v); v = dpp_add<0x124>(v); v = dpp_add<0x122>(v); v = dpp_add<0x121>(v);
  auto r = __builtin_amdgcn_permlane16_swap(__float_as_uint(v), __float_as_uint(v), false, false); return __uint_as_float(r[0]) + __uint_as_float(r[1]);
}
struct XRow { u32x4 a, b; u32x2 c; };
DI void xrow_load(XRow& v, const u16* x, int row0) {
  const int lane = threadIdx.x & 63;
  const char* p = (const char*)x + (size_t)(row0 + (lane >> 5)) * 1280;
  v.a = *(const u32x4*)(p + (lane & 31) * 16); v.b = *(const u32x4*)(p + 512 + (lane & 31) * 16); v.c = *(const u32x2*)(p + 1024 + (lane & 31) * 8);
}
DI void xrow_publish(const XRow& v, char* ws, int row0, u64 tag) {
  const int lane = threadIdx.x & 63;
  const u32x4 va = v.a, vb = v.b; const u32x2 vc = v.c;
  float e[20];
#pragma unroll
  for (int i = 0; i < 4; ++i) { e[2 * i] = bflo(va[i]); e[2 * i + 1] = bfhi(va[i]); e[8 + 2 * i] = bflo(vb[i]); e[9 + 2 * i] = bfhi(vb[i]); }
  e[16] = bflo(vc[0]); e[17] = bfhi(vc[0]); e[18] = bflo(vc[1]); e[19] = bfhi(vc[1]);
  f32x2 s = {0.f, 0.f};
#pragma unroll
  for (int i = 0; i < 20; i += 2) s += f32x2{e[i], e[i + 1]};
  const float m = sum32(s[0] + s[1]) * (1.f / D);
  f32x2 q = {0.f, 0.f};
#pragma unroll
  for (int i = 0; i < 20; i += 2) { const f32x2 d = f32x2{e[i], e[i + 1]} - m; q += d * d; }
  const float rstd = __builtin_amdgcn_rsqf(sum32(q[0] + q[1]) * (1.f / D) + 1e-5f);
  if ((lane & 31) == 0) st16(mkrsrc(ws + WS_XST, S * 16), (row0 + (lane >> 5)) * 16, u32x4{__float_as_uint(m), (unsigned)tag, __float_as_uint(rstd), (unsigned)(tag >> 32)});
}
struct F4 { bf16x8 a0, a1, b0, b1; };
template <bool isV> DI void k1_compute(char* ws, char* smem, int bm, int bn, int wid, XRow& xr, u64 tag) {
  const int lane = threadIdx.x & 63, r = lane & 31, hh = lane >> 5;
  const int wm = wid >> 1, wn = wid & 1;
  f32x16 c00 = f32x16{}, c01 = f32x16{}, c10 = f32x16{}, c11 = f32x16{};
  const int fA = (r >> 1) & 7;
  const int gi = lane >> 4, q = (lane & 15) >> 2, p = lane & 3;
  const LAS char* sm = (const LAS char*)smem;
  const LAS char* pa[2][4]; const LAS char* pb[2][2];
#pragma unroll
  for (int w = 0; w < 2; ++w) {
#pragma unroll
    for (int s = 0; s < 4; ++s) { pa[w][s] = sm + w * 65536 + (wm * 64 + r) * 128 + (((2 * s + hh) ^ fA) << 4); asm volatile("" : "+v"(pa[w][s])); }
#pragma unroll
    for (int jn = 0; jn < 2; ++jn) { pb[w][jn] = sm + w * 65536 + 16384 + (8 * (gi >> 1) + q) * 256 + 32 * (gi & 1) + 8 * p + (((2 * wn + jn) ^ q) << 6); asm volatile("" : "+v"(pb[w][jn])); }
  }
  auto lda = [&](int i, int t) { const int sl = (i >> 2) % K1_NSLOT; return *(const LAS bf16x8*)(pa[sl >> 1][i & 3] + (sl & 1) * K1_STAGE + t * 4096); };
  auto ldb = [&](int i, int jn) { const int sl = (i >> 2) % K1_NSLOT; const LAS char* b = pb[sl >> 1][jn] + (sl & 1) * K1_STAGE + (i & 3) * 4096; return cat8(trread(b), trread(b + 1024)); };
#define K1_MM(c, a, b) asm volatile("v_mfma_f32_32x32x16_bf16 %0, %1, %2, %0" : "+v"(c) : "v"(isV ? a : b), "v"(isV ? b : a))
  const bool isQ = bn < 5; const int tstride = isQ ? QT : KVT;
  const rsrc_t rimg = mkrsrc(ws + (isQ ? WS_Q : WS_KV), H * 64 * KVT);
  unsigned soff[8];
  if (!isV) {
#pragma unroll
    for (int u = 0; u < 8; ++u) {
      const int n = (bn % 5) * 128 + wn * 64 + (u >> 2) * 32 + 8 * (u & 3) + 4 * hh, head = n / 20, d = n - head * 20;
      soff[u] = head * 64 * tstride + (d < 16 ? (d >> 3) * 512 + r * 16 + (d & 7) * 2 : 1024 + r * 8);
      asm volatile("" : "+v"(soff[u]));
    }
  } else {
#pragma unroll
    for (int jn = 0; jn < 2; ++jn) {
      const int n = (bn - 10) * 128 + wn * 64 + jn * 32 + r, head = n / 20, d = n - head * 20;
      soff[jn] = head * 64 * KVT + 1280 + hh * 352 + d * 16;
      {
        const unsigned c1 = d == 18 ? 0x3F803F80u : 0u, o = d >= 18 ? soff[jn] + 32 : H * 64 * KVT;
#pragma unroll
        for (int u = 0; u < 4; ++u) st16s(rimg, o + (u & 1) * 704, (bm * 4 + wm * 2 + (u >> 1)) * KVT, u32x4{c1, c1, c1, c1});
      }
      asm volatile("" : "+v"(soff[jn]));
    }
  }
  asm volatile("" : "+v"(c00), "+v"(c01), "+v"(c10), "+v"(c11));
  SBAR();
  xrow_publish(xr, ws, blockIdx.x * 8 + wid * 2, tag);
  BAR(); SBAR();
  STAMPT(0, 1);
  F4 f[3];
#pragma unroll
  for (int i = 0; i < 2; ++i) { f[i].a0 = lda(i, 0); f[i].b0 = ldb(i, 0); f[i].b1 = ldb(i, 1); f[i].a1 = lda(i, 1); }
  SBAR();
#pragma unroll
  for (int i = 0; i < 40; ++i) {
    F4& x = f[i % 3]; F4& y = f[(i + 2) % 3];
    const bool ld = i + 2 < 40;
    if (ld && (i & 3) == 2) { BAR(); SBAR(); }
    K1_MM(c00, x.a0, x.b0); SBAR(); if (ld) { y.a0 = lda(i + 2, 0); y.b0 = ldb(i + 2, 0); } SBAR();
    K1_MM(c01, x.a0, x.b1); SBAR(); if (ld) { y.b1 = ldb(i + 2, 1); } SBAR();
    K1_MM(c10, x.a1, x.b0); SBAR(); if (ld) { y.a1 = lda(i + 2, 1); } SBAR();
    K1_MM(c11, x.a1, x.b1); SBAR();
  }
  asm volatile("s_nop 11" ::: "memory"); SBAR();
  STAMPT(0, 2);
  BAR();
  const LAS float* sMean = (const LAS float*)(smem + K1_NSLOT * K1_STAGE); const LAS float* sRstd = sMean + 128; const LAS float* sCs = sMean + 256;
  if (!isV) {
#pragma unroll
    for (int i = 0; i < 2; ++i) {
      const int tl = wm * 64 + i * 32;
      const float m = sMean[tl + r], rstd = sRstd[tl + r] * (isQ ? QSCALE : 1.f);
      int tile = bm * 4 + wm * 2 + i; if (!isQ) tile = (tile + 64 - KSKEW) & 63;
#pragma unroll
      for (int jn = 0; jn < 2; ++jn) {
        const f32x16& c = i ? (jn ? c11 : c10) : (jn ? c01 : c00);
#pragma unroll
        for (int g = 0; g < 4; ++g) {
          const f32x4 c4 = *(const LAS f32x4*)(sCs + wn * 64 + jn * 32 + 8 * g + 4 * hh);
          u32x2 v;
          v[0] = pk2((c[4 * g] - m * c4[0]) * rstd, (c[4 * g + 1] - m * c4[1]) * rstd);
          v[1] = pk2((c[4 * g + 2] - m * c4[2]) * rstd, (c[4 * g + 3] - m * c4[3]) * rstd);
          st8s(rimg, soff[jn * 4 + g], tile * tstride, v);
        }
      }
    }
  } else {
#pragma unroll
    for (int i = 0; i < 2; ++i) {
      const int tile = bm * 4 + wm * 2 + i;
#pragma unroll
      for (int s = 0; s < 2; ++s) {
        f32x4 m4[2], r4[2];
#pragma unroll
        for (int g = 0; g < 2; ++g) { const int tl = wm * 64 + i * 32 + 16 * s + 8 * g + 4 * hh; m4[g] = *(const LAS f32x4*)(sMean + tl); r4[g] = *(const LAS f32x4*)(sRstd + tl); }
#pragma unroll
        for (int jn = 0; jn < 2; ++jn) {
          const f32x16& c = i ? (jn ? c11 : c10) : (jn ? c01 : c00);
          const float cs = sCs[wn * 64 + jn * 32 + r];
          u32x4 v;
#pragma unroll
          for (int g = 0; g < 2; ++g) {
            const int e = 8 * s + 4 * g;
            v[2 * g] = pk2((c[e] - m4[g][0] * cs) * r4[g][0], (c[e + 1] - m4[g][1] * cs) * r4[g][1]);
            v[2 * g + 1] = pk2((c[e + 2] - m4[g][2] * cs) * r4[g][2], (c[e + 3] - m4[g][3] * cs) * r4[g][3]);
          }
          st16s(rimg, soff[jn] + s * 704, tile * KVT, v);
        }
      }
    }
  }
}
struct Args { const u16 *x, *wqkv, *wo, *w1, *b1, *w2, *b2; u16* out; char* ws; };
constexpr int LDS_RT = 143360;
DI void retile_dma(const Args& a, unsigned lds, int hw) {
  const int lane = threadIdx.x & 63, b = blockIdx.x;
  if (b >= 40 && b < 148) {
    const int jb = b - 40;
    const char* src = (const char*)a.w1 + (size_t)((jb & 3) * 160) * (F * 2) + (jb >> 2) * 128;
#pragma unroll
    for (int i = 0; i < 5; ++i) glds16((lane >> 3) * (F * 2) + (lane & 7) * 16, src + (size_t)(hw * 5 + i) * 8 * (F * 2), lds + (hw * 5 + i) * 1024);
  } else {
    const int jb = b < 40 ? b : b - 148;
    const char* src = (const char*)(b < 40 ? a.wo : a.w2) + (size_t)((jb >> 1) * 32 + hw * 8) * (D * 2) + (jb & 1) * 640;
#pragma unroll
    for (int i = 0; i < 5; ++i) { const int idx = i * 64 + lane, row = idx / 40; glds16(row * (D * 2) + (idx - row * 40) * 16, src, lds + (hw * 5 + i) * 1024); }
  }
}
DI void retile_store(char* ws, const LAS char* img, int hw) {
  const int lane = threadIdx.x & 63, b = blockIdx.x;
  const bool is1 = b >= 40 && b < 148;
  const int rs = is1 ? 128 : 640;
  const LAS char* p = img + (lane >> 2) * rs + (lane & 3) * 8 + (is1 ? hw * 32 : hw * 160);
  const rsrc_t rw = mkrsrc(ws + (b < 40 ? WS_WO : (is1 ? WS_W1 : WS_W2)), D * F * 2);
  const int jb = b < 40 ? b : (is1 ? b - 40 : b - 148);
  const int d0 = is1 ? ((jb >> 2) * 4 + hw) * 20 + (jb & 3) * 5 : (((jb & 1) * 4 + hw) * (b < 40 ? 20 : 54) + (jb >> 1)) * 5;
  float cs = 0.f;
#pragma unroll
  for (int k = 0; k < 5; ++k) {
    const LAS char* q = p + k * (is1 ? 4096 : 32);
    const u32x4 v = __builtin_bit_cast(u32x4, cat8(trread(q), trread(q + 16 * rs)));
    st16(rw, (d0 + k) * 1024 + lane * 16, v);
#pragma unroll
    for (int j = 0; j < 4; ++j) { asm volatile("v_add_f32 %0, %0, %1" : "+v"(cs) : "v"(bflo(v[j]))); asm volatile("v_add_f32 %0, %0, %1" : "+v"(cs) : "v"(bfhi(v[j]))); }
  }
  if (is1) { cs = xq_sum(cs); if (lane < 16) __builtin_amdgcn_raw_buffer_store_b32(__float_as_uint(cs), mkrsrc(ws + WS_CS1, 4 * 1792 * 4), ((jb & 3) * 1792 + 16 * ((jb >> 2) * 4 + hw) + lane) * 4, 0, XAUX); }
}
DI void k1_helper(const Args& a, const u16* __restrict__ x, const u16* __restrict__ w, char* ws, char* smem, int bm, int bn, int rot, int hw, u64& tag) {
  const int lane = threadIdx.x & 63;
  const unsigned lds0 = (unsigned)(uintptr_t)smem;
  const unsigned voffA0 = (lane >> 3) * 1280 + (((lane & 7) ^ (lane >> 4)) << 4);
  const unsigned voffA1 = (lane >> 3) * 1280 + (((lane & 7) ^ (4 + (lane >> 4))) << 4);
  const unsigned voffB = (lane >> 4) * 3840 + (((((lane & 15) >> 2) ^ (lane >> 4)) * 4 + (lane & 3)) << 4);
  const char* xa = (const char*)x + (size_t)(bm * 128 + hw * 32) * 1280;
  const char* wb = (const char*)w + (size_t)(hw * 16) * 3840 + bn * 256;
  auto issue = [&](int st) {
    int ks = st + rot; if (ks >= 10) ks -= 10;
    const unsigned dst = lds0 + (st % K1_NSLOT) * K1_STAGE + hw * 4096;
#pragma unroll
    for (int i = 0; i < 4; ++i) glds16((i & 1) ? voffA1 : voffA0, xa + i * 8 * 1280 + ks * 128, dst + i * 1024);
#pragma unroll
    for (int i = 0; i < 4; ++i) glds16(voffB, wb + (size_t)(ks * 64 + i * 4) * 3840, dst + 16384 + i * 1024);
  };
  issue(0); issue(1);
  if (K1_PRE == 3) issue(2);
  WAITV(8 * (K1_PRE - 1)); BAR();
  if (K1_PRE == 2) issue(2);
  WAITV(8); BAR();
  issue(3);
  tag = get_tag(ws);
#pragma unroll 1
  for (int st = 1; st < 7; ++st) { WAITV(8); BAR(); issue(st + 3); }
  retile_dma(a, lds0 + LDS_RT, hw);
  WAITV(13); BAR();
  WAITV(5); BAR();
  const rsrc_t rs = mkrsrc(ws + WS_XST, S * 16 + 16384);
  const unsigned go = lane < 32 ? (bm * 128 + hw * 32 + lane) * 16 : S * 16 + (bn * 64 + hw * 16 + (lane & 15)) * 16;
  u32x4 rec;
#pragma unroll 1
  for (unsigned sp = 0; sp < SPIN_CAP; ++sp) {
    asm volatile("" ::: "memory");
    rec = __builtin_amdgcn_raw_buffer_load_b128(rs, go, 0, XAUX);
    if (__all(rec[1] == (unsigned)tag && rec[3] == (unsigned)(tag >> 32))) break;
    __builtin_amdgcn_s_sleep(1);
  }
  LAS unsigned* st = (LAS unsigned*)(smem + K1_NSLOT * K1_STAGE);
  if (lane < 32) { st[hw * 32 + lane] = rec[0]; st[128 + hw * 32 + lane] = rec[2]; }
  else if (lane < 48) { st[256 + hw * 32 + 2 * (lane & 15)] = rec[0]; st[257 + hw * 32 + 2 * (lane & 15)] = rec[2]; }
  LGKM0(); BAR();
}
DI void k1_colsum(const u16* __restrict__ x, const u16* __restrict__ w, char* ws, char* smem, int u, int wid, u64 tag) {
  const int lane = threadIdx.x & 63;
  if (u < 15) {
    const char* p = (const char*)w + (size_t)(wid * 80 + (lane >> 4)) * 3840 + u * 256 + (lane & 15) * 16;
    u32x4 v[20];
#pragma unroll
    for (int i = 0; i < 20; ++i) v[i] = *(const u32x4*)(p + (size_t)i * 4 * 3840);
    float a[8];
#pragma unroll
    for (int e = 0; e < 8; ++e) a[e] = 0.f;
#pragma unroll
    for (int i = 0; i < 20; ++i)
#pragma unroll
      for (int e = 0; e < 4; ++e) { a[2 * e] += bflo(v[i][e]); a[2 * e + 1] += bfhi(v[i][e]); }
    LAS float* part = (LAS float*)smem;
#pragma unroll
    for (int e = 0; e < 8; ++e) { a[e] = xq_sum(a[e]); if (lane < 16) part[wid * 128 + lane * 8 + e] = a[e]; }
    LGKM0(); BAR();
    if (wid == 0) {
      float c0 = 0.f, c1 = 0.f;
#pragma unroll
      for (int k = 0; k < 8; ++k) { c0 += part[k * 128 + 2 * lane]; c1 += part[k * 128 + 2 * lane + 1]; }
      st16(mkrsrc(ws + WS_CSQ, 16384), (u * 64 + lane) * 16, u32x4{__float_as_uint(c0), (unsigned)tag, __float_as_uint(c1), (unsigned)(tag >> 32)});
    }
  }
}

constexpr int CH_TILES = 8, CH_BYTES = CH_TILES * KVT, NCH = 64 / CH_TILES, CH_PIECES = CH_BYTES / 1024;
#define EX(v) __builtin_amdgcn_exp2f(v)
#define PF(p) __builtin_bit_cast(bf16x8, p)
DI float rowmax16(const f32x16& s) {
  float m = s[0];
#pragma unroll
  for (int e = 1; e < 16; ++e) m = fmaxf(m, s[e]);
  return xhalf_max(m);
}
DI s16x4 shift_from(const f32x16& s, s16x4 q2, int hh) {
  const float m = -rowmax16(s);
  const unsigned mh = pk2(m, 0.f) & 0xffffu, ml = pk2(m - bflo(mh), 0.f) & 0xffffu;
  return hh ? s16x4{(short)mh, (short)ml, 0, 0} : q2;
}
DI bool attn_unsafe(const f32x16& o, int hh) {
  const float l = hh ? o[8] : o[12];
  bool bad = !(l >= 1e-20f && l <= 1e30f);
#pragma unroll
  for (int e = 0; e < 12; ++e) bad |= !(fabsf(o[e]) <= 3e38f);
  return __any(bad);
}
DI f32x16 attn_online(rsrc_t kv, bf16x8 q1, s16x4 q2, int lane, int k2off, int voff) {
  if (lane >= 32) q2 = s16x4{0, 0, 0, 0};
  float m = -INFINITY; f32x16 o = f32x16{};
#pragma unroll 1
  for (int t = 0; t < 64; ++t) {
    const int kt = ((t + 64 - KSKEW) & 63) * KVT, vt = t * KVT;
    f32x16 s = MFMA32(ldfrag(kv, lane * 16, kt), q1, f32x16{}); s = MFMA32K8(ldhalf(kv, k2off, kt), q2, s);
    const float mn = fmaxf(m, rowmax16(s)), al = EX(m - mn); m = mn;
#pragma unroll
    for (int e = 0; e < 16; ++e) { o[e] *= al; s[e] = EX(s[e] - mn); }
    const u32x4 p0 = {pk2(s[0], s[1]), pk2(s[2], s[3]), pk2(s[4], s[5]), pk2(s[6], s[7])};
    const u32x4 p1 = {pk2(s[8], s[9]), pk2(s[10], s[11]), pk2(s[12], s[13]), pk2(s[14], s[15])};
    o = MFMA32(ldfrag(kv, voff, vt), PF(p0), o); o = MFMA32(ldfrag(kv, voff + 704, vt), PF(p1), o);
  }
  return o;
}
DI void attn_compute(char* ws, char* smem, int head, int qc, int wid, u64 tag) {
  const int lane = threadIdx.x & 63, r = lane & 31, hh = lane >> 5;
  const rsrc_t kv = mkrsrc(ws + WS_KV + (size_t)head * 64 * KVT, 64 * KVT);
  const int qt = qc * 8 + wid * 2;
  const rsrc_t rq = mkrsrc(ws + WS_Q + (size_t)(head * 64 + qt) * QT, 2 * QT);
  const int k2off = hh ? 1600 : 1024 + r * 8;
  const int voff = 1280 + hh * 352 + (r < 21 ? r : (r == 24 ? 20 : 21)) * 16;
  const bf16x8 qa1 = ldfrag(rq, lane * 16, 0), qb1 = ldfrag(rq, lane * 16, QT);
  s16x4 qa2 = ldhalf(rq, 1024 + r * 8, 0), qb2 = ldhalf(rq, 1024 + r * 8, QT);
  if (hh) { qa2 = s16x4{0, 0, 0, 0}; qb2 = s16x4{0, 0, 0, 0}; }
  unsigned ooff[3];
#pragma unroll
  for (int g = 0; g < 3; ++g) {
    const int k = head * 20 + 8 * g + 4 * hh;
    ooff[g] = (g == 2 && hh) ? S * D * 2 : ((qt * 2 + (r >> 4)) * 20 + (k >> 5)) * 1024 + (((k >> 2) & 3) * 16 + (r & 15)) * 16 + ((k >> 4) & 1) * 8;
    asm volatile("" : "+v"(ooff[g]));
  }
  bf16x8 k1 = ldfrag(kv, lane * 16, 63 * KVT); s16x4 k2 = ldhalf(kv, k2off, 63 * KVT);
  f32x16 oa, ob;
  { f32x16 s = MFMA32(k1, qa1, f32x16{}); s = MFMA32K8(k2, qa2, s); qa2 = shift_from(s, qa2, hh);
    s = MFMA32(k1, qb1, f32x16{}); s = MFMA32K8(k2, qb2, s); qb2 = shift_from(s, qb2, hh); }
  const unsigned lds0 = (unsigned)(uintptr_t)smem;
  unsigned ctl = lds0 + CTL2;
  STAMPT(1, 1);
  asm volatile(
    ".macro AT_E r\n v_exp_f32 v[\\r], v[\\r]\n v_exp_f32 v[\\r+1], v[\\r+1]\n.endm\n"
    ".macro AT_C d, r\n v_cvt_pk_bf16_f32 v[\\d], v[\\r], v[\\r+1]\n.endm\n"
    ".macro AT_H sc, sn, pc, pp, o, x, xk2, q1, q2, y, yk2, off, mode\n"
    " v_mfma_f32_32x32x16_bf16 v[\\o:\\o+15], v[\\x+4:\\x+7], v[\\pp:\\pp+3], v[\\o:\\o+15]\n"
    " .if \\mode & 1\n ds_read_b128 v[\\y+4:\\y+7], v130 offset:\\off\n .endif\n"
    " AT_C \\pp+6, \\sn+12\n AT_C \\pp+7, \\sn+14\n AT_E \\sc\n AT_E \\sc+2\n"
    " .if (\\mode & 16) == 0\n v_mfma_f32_32x32x16_bf16 v[\\sn:\\sn+15], v[\\x:\\x+3], v[\\q1:\\q1+3], 0\n .endif\n"
    " .if \\mode & 1\n ds_read_b128 v[\\y:\\y+3], v128 offset:\\off\n .endif\n"
    " .if \\mode & 4\n ds_read_b32 v133, v131 offset:\\off\n .endif\n"
    " AT_E \\sc+4\n AT_C \\pc, \\sc\n AT_E \\sc+6\n AT_C \\pc+1, \\sc+2\n"
    " .if (\\mode & 16) == 0\n v_mfma_f32_32x32x8_bf16 v[\\sn:\\sn+15], v[\\xk2:\\xk2+1], v[\\q2:\\q2+1], v[\\sn:\\sn+15]\n .endif\n"
    " AT_E \\sc+8\n AT_C \\pc+2, \\sc+4\n AT_E \\sc+10\n AT_C \\pc+3, \\sc+6\n"
    " v_mfma_f32_32x32x16_bf16 v[\\o:\\o+15], v[\\x+8:\\x+11], v[\\pp+4:\\pp+7], v[\\o:\\o+15]\n"
    " .if \\mode & 1\n ds_read_b64 v[\\yk2:\\yk2+1], v129 offset:\\off\n ds_read_b128 v[\\y+8:\\y+11], v130 offset:\\off+704\n .endif\n"
    " .if \\mode & 2\n s_waitcnt lgkmcnt(0)\n .endif\n"
    " .if \\mode & 4\n s_mov_b64 exec, 1\n ds_add_u32 v131, v132 offset:\\off+60\n s_mov_b64 exec, -1\n v_cmp_gt_u32 vcc, 4, v133\n .endif\n"
    " AT_E \\sc+12\n AT_C \\pc+4, \\sc+8\n AT_E \\sc+14\n AT_C \\pc+5, \\sc+10\n"
    ".endm\n"
    ".macro AT_A x, xk2, y, yk2, t\n AT_H 56, 40, 80, 72, 8, \\x, \\xk2, 116, 124, \\y, \\yk2, (\\t)*2688, 1\n.endm\n"
    ".macro AT_B x, xk2, off, mode\n AT_H 40, 56, 72, 80, 24, \\x, \\xk2, 120, 126, 0, 0, \\off, \\mode\n.endm\n"
    ".macro AT_2 t\n AT_A 104, 102, 88, 100, \\t\n AT_B 104, 102, 0, 2\n AT_A 88, 100, 104, 102, \\t+1\n AT_B 88, 100, 0, 2\n.endm\n"
    ".macro AT_SPIN off, back\n"
    "1: s_cmp_eq_u32 s41, 0\n s_cbranch_scc1 \\back\n s_sub_u32 s41, s41, 1\n s_sleep 1\n ds_read_b32 v133, v131 offset:\\off\n s_waitcnt lgkmcnt(0)\n v_cmp_gt_u32 vcc, 4, v133\n s_nop 4\n s_cbranch_vccnz 1b\n s_branch \\back\n"
    ".endm\n"
    " s_nop 1\n"
    " v_mfma_f32_32x32x16_bf16 v[40:55], v[88:91], v[116:119], 0\n"
    " v_mfma_f32_32x32x8_bf16 v[40:55], v[100:101], v[124:125], v[40:55]\n"
    " v_mov_b32 v132, 1\n"
    " .irp r, 8,9,10,11,12,13,14,15,16,17,18,19,20,21,22,23,24,25,26,27,28,29,30,31,32,33,34,35,36,37,38,39,68,69,70,71,80,81,82,83,84,85,92,93,94,95,96,97,98,99\n v_mov_b32 v[\\r], 0\n .endr\n"
    " s_mov_b32 s40, 4\n s_mov_b32 s41, 0x400000\n"
    ".Lat_w0: s_sub_u32 s41, s41, 1\n s_cbranch_scc1 .Lat_w1\n ds_read_b32 v133, v131\n s_waitcnt lgkmcnt(0)\n v_cmp_gt_u32 vcc, 4, v133\n s_nop 4\n s_cbranch_vccnz .Lat_w0\n"
    ".Lat_w1: s_mov_b32 s41, 0x400000\n"
    " ds_read_b128 v[108:111], v130\n ds_read_b128 v[104:107], v128\n ds_read_b64 v[102:103], v129\n ds_read_b128 v[112:115], v130 offset:704\n"
    " s_branch .Lat_mid\n"
    ".Lat_s1: AT_SPIN 4, .Lat_b1\n"
    ".Lat_s2: AT_SPIN 8, .Lat_b2\n"
    " .p2align 6\n"
    ".Lat_loop:\n"
    " AT_A 88, 100, 104, 102, 0\n"
    ".Lat_mid:\n"
    " AT_B 88, 100, 0, 2\n"
    " AT_2 1\n AT_2 3\n AT_2 5\n"
    " AT_A 104, 102, 88, 100, 7\n AT_B 104, 102, 4, 6\n"
    " s_cbranch_vccnz .Lat_s1\n"
    ".Lat_b1:\n"
    " AT_A 88, 100, 104, 102, 8\n AT_B 88, 100, 0, 2\n"
    " AT_2 9\n AT_2 11\n AT_2 13\n"
    " AT_A 104, 102, 88, 100, 15\n AT_B 104, 102, 8, 6\n"
    " s_cbranch_vccnz .Lat_s2\n"
    ".Lat_b2:\n"
    " v_add_u32 v131, 8, v131\n"
    " s_sub_u32 s40, s40, 1\n s_cmp_lg_u32 s40, 0\n s_cbranch_scc1 .Lat_loop\n"
    " AT_H 56, 40, 80, 72, 8, 88, 100, 116, 124, 0, 0, 0, 16\n"
    " AT_C 86, 68\n AT_C 87, 70\n"
    " v_mfma_f32_32x32x16_bf16 v[24:39], v[92:95], v[80:83], v[24:39]\n s_nop 0\n"
    " v_mfma_f32_32x32x16_bf16 v[24:39], v[96:99], v[84:87], v[24:39]\n"
    " s_nop 12\n"
    : "=&{v[8:23]}"(oa), "=&{v[24:39]}"(ob), "+{v131}"(ctl), "+{v[88:91]}"(k1), "+{v[100:101]}"(k2)
    : "{v[116:119]}"(qa1), "{v[120:123]}"(qb1), "{v[124:125]}"(qa2), "{v[126:127]}"(qb2),
      "{v128}"(lds0 + lane * 16), "{v129}"(lds0 + k2off), "{v130}"(lds0 + voff)
    : "memory", "vcc", "scc", "s40", "s41", "v40","v41","v42","v43","v44","v45","v46","v47","v48","v49","v50","v51","v52","v53","v54","v55","v56","v57","v58","v59","v60","v61","v62","v63",
      "v64","v65","v66","v67","v68","v69","v70","v71","v72","v73","v74","v75","v76","v77","v78","v79","v80","v81","v82","v83","v84","v85","v86","v87","v92","v93","v94","v95","v96","v97","v98","v99",
      "v102","v103","v104","v105","v106","v107","v108","v109","v110","v111","v112","v113","v114","v115","v132","v133");
  STAMPT(1, 2);
  const rsrc_t ro = mkrsrc(ws + WS_O, S * D * 2);
  auto put = [&](const f32x16& o, int u) {
    const float il = __builtin_amdgcn_rcpf(hh ? o[8] : o[12]);
#pragma unroll
    for (int g = 0; g < 3; ++g) st8s(ro, ooff[g], u * 40960, u32x2{pk2(o[4 * g] * il, o[4 * g + 1] * il), pk2(o[4 * g + 2] * il, o[4 * g + 3] * il)});
  };
  put(oa, 0); put(ob, 1);
  const bool fa = attn_unsafe(oa, hh), fb = attn_unsafe(ob, hh);
  if (fa) { WAITV(0); put(attn_online(kv, qa1, qa2, lane, k2off, voff), 0); }
  if (fb) { WAITV(0); put(attn_online(kv, qb1, qb2, lane, k2off, voff), 1); }
  STAMPT(1, 3);
  WAITV(0);
  if (lane == 0) signal(ws, FL_O + (qc * 4 + wid) * 32 + head, tag);
}

template <int NC> struct GS { f32x4 acc[2][NC]; };
template <int NC, int NKS, int PD, int SSTRIDE, int RING, bool BARS, class Mid> DI void gemm_run(GS<NC>& g, const LAS char* wl, rsrc_t ra, unsigned va0, unsigned va1, unsigned astep, int rot, Mid mid) {
  bf16x8 af[PD][2], bf[2][NC];
  auto soff = [&](int s) { int k = s + rot; if (k >= NKS) k -= NKS; return (unsigned)k * astep; };
#pragma unroll
  for (int s = 0; s < PD; ++s) { const unsigned so = soff(s); af[s][0] = ldfrag(ra, va0, so); af[s][1] = ldfrag(ra, va1, so); }
  SBAR();
  const LAS char* wb[3] = {wl, wl + 61440, wl + 122880};
  asm volatile("" : "+v"(wb[0]), "+v"(wb[1]), "+v"(wb[2]));
  auto rdw = [&](int off) { return *(const LAS bf16x8*)(wb[off / 61440] + off % 61440); };
  mid(-1);
#pragma unroll
  for (int j = 0; j < NC; ++j) bf[0][j] = rdw(j * 1024);
#pragma unroll
  for (int s = 0; s < NKS; ++s) {
    LGKM0();
    if (BARS && (s + 1) % 4 == 0 && s + 1 < NKS) BAR();
    mid(s);
    SBAR();
#pragma unroll
    for (int m = 0; m < 2 * NC; ++m) {
      mfma16_acc(g.acc[m & 1][m >> 1], bf[s & 1][m >> 1], af[s % PD][m & 1]); SBAR();
      if (s + 1 < NKS && m < NC) { bf[(s + 1) & 1][m] = rdw(((s + 1) % RING) * SSTRIDE + m * 1024); SBAR(); }
      if (s >= 1 && s - 1 + PD < NKS && (m == NC + 1 || m == NC + 3)) { af[(s - 1) % PD][m == NC + 3] = ldfrag(ra, m == NC + 3 ? va1 : va0, soff(s - 1 + PD)); SBAR(); }
    }
  }
  asm volatile("s_nop 7" ::: "memory"); SBAR();
}
template <int NC> DI void pair_reduce(GS<NC>& g, char* smem, int wid) {
  const int lane = threadIdx.x & 63;
  LAS f32x4* red = (LAS f32x4*)(smem + 143360) + lane;
#pragma unroll
  for (int j = 0; j < NC; ++j) red[(wid * NC + j) * 64] = g.acc[1][j];
  LGKM0(); BAR();
#pragma unroll
  for (int j = 0; j < NC; ++j) g.acc[0][j] += red[((wid ^ 2) * NC + j) * 64];
}
DI f32x4 unpack4(u32x2 v) { return f32x4{bflo(v[0]), bfhi(v[0]), bflo(v[1]), bfhi(v[1])}; }
DI u32x2 pack4(f32x4 v) { return u32x2{pk2(v[0], v[1]), pk2(v[2], v[3])}; }
DI unsigned piece_off(int rt, int gct, int nks, int lane) { return (rt * nks + (gct >> 1)) * 1024 + lane * 16 + (gct & 1) * 8; }
template <int NC> DI void store_tiles(rsrc_t r, int rt, int gct0, int nct, int nks, int lane, const u32x2 (&v)[NC]) {
  if (gct0 & 1) {
    st8(r, piece_off(rt, gct0, nks, lane), v[0]);
#pragma unroll
    for (int j = 1; j + 1 < NC; j += 2) {
      if (j + 1 < nct) st16(r, piece_off(rt, gct0 + j, nks, lane), u32x4{v[j][0], v[j][1], v[j + 1][0], v[j + 1][1]});
      else st8(r, piece_off(rt, gct0 + j, nks, lane), v[j]);
    }
  } else {
#pragma unroll
    for (int j = 0; j + 1 < NC; j += 2) st16(r, piece_off(rt, gct0 + j, nks, lane), u32x4{v[j][0], v[j][1], v[j + 1][0], v[j + 1][1]});
    if (nct == NC) st8(r, piece_off(rt, gct0 + NC - 1, nks, lane), v[NC - 1]);
  }
}

#define P4_PD 8
#define P5_PD 8
#define ROT3(c) 0
#define ROT4(c4) 0
#define ROT5(c) 0
#define PACE 1
constexpr int LDS_WO = 61440, LDS_CB = 162816, LDS_BYTES = 163840;
constexpr int CODE_BYTES = 45056;

DI void post_compute(const Args& a, char* smem, int wid, u64 tag) {
  char* ws = a.ws;
  const int lane = threadIdx.x & 63, b = blockIdx.x;
  const int grp = (b & 7) * 4 + ((b >> 3) & 3), c = b >> 5, rh = wid & 1, kh = wid >> 1;
  const int rt = grp * 4 + rh * 2 + kh, tok = rt * 16 + (lane & 15);
  u32x2 hv[5];
  {
    GS<5> g;
#pragma unroll
    for (int j = 0; j < 5; ++j) { g.acc[0][j] = unpack4(*(const u32x2*)(a.x + (size_t)tok * D + c * 80 + j * 16 + (lane >> 4) * 4)); g.acc[1][j] = f32x4{}; }
#pragma unroll
    for (int j = 0; j < 5; ++j) asm volatile("" : "+v"(g.acc[0][j]), "+v"(g.acc[1][j]));
    const rsrc_t ra = mkrsrc(ws + WS_O, S * D * 2);
    const unsigned va0 = (rt * 20 + kh) * 1024 + lane * 16, va1 = ((rt ^ 1) * 20 + kh) * 1024 + lane * 16;
    await(ws, FL_O + grp * 32 + (lane & 31), tag);
    STAMPT(2, 0);
    gemm_run<5, 10, 10, 10240, 10, false>(g, (const LAS char*)smem + LDS_WO + kh * 5120 + lane * 16, ra, va0, va1, 2048, ROT3(c),
                                          [&](int s) { if (s == 9) BAR(); });
    STAMPT(2, 1);
    pair_reduce<5>(g, smem, wid);
#pragma unroll
    for (int j = 0; j < 5; ++j) hv[j] = pack4(g.acc[0][j]);
    store_tiles<5>(mkrsrc(ws + WS_H, S * D * 2), rt, c * 5, 5, 20, lane, hv);
    f32x4 s1 = g.acc[0][0], s2 = g.acc[0][0] * g.acc[0][0];
#pragma unroll
    for (int j = 1; j < 5; ++j) { s1 += g.acc[0][j]; s2 += g.acc[0][j] * g.acc[0][j]; }
    const float t1 = xq_sum((s1[0] + s1[1]) + (s1[2] + s1[3])), t2 = xq_sum((s2[0] + s2[1]) + (s2[2] + s2[3]));
    st8(mkrsrc(ws + WS_ST, S * 64), lane < 16 ? tok * 64 + c * 8 : S * 64, u32x2{__float_as_uint(t1), __float_as_uint(t2)});
    STAMPT(2, 2);
    WAITV(0);
    if (lane == 0) signal(ws, FL_H + rt * 8 + c, tag);
    STAMPT(2, 3);
  }
  {
    const int g4 = (b & 7) * 2 + ((b >> 4) & 1), c4 = ((b >> 5) << 1) | ((b >> 3) & 1);
    const int gct0 = (c4 * 27) >> 2, nct = (((c4 + 1) * 27) >> 2) - gct0;
    GS<7> g;
#pragma unroll
    for (int j = 0; j < 7; ++j) { g.acc[0][j] = f32x4{}; g.acc[1][j] = f32x4{}; asm volatile("" : "+v"(g.acc[0][j]), "+v"(g.acc[1][j])); }
    const rsrc_t ra = mkrsrc(ws + WS_H, S * D * 2);
    const int rt0 = g4 * 8 + wid * 2;
    const unsigned va0 = rt0 * 20480 + lane * 16;
    await(ws, FL_H + rt0 * 8 + (lane & 15), tag);
    STAMPT(3, 0);
    u32x4 ps[2]; f32x4 cs[7], bv[7];
    const LAS f32x4* cb = (const LAS f32x4*)(smem + LDS_CB) + (lane >> 4);
    gemm_run<7, 20, P4_PD, 7168, 20, false>(g, (const LAS char*)smem + lane * 16, ra, va0, va0 + 20480, 1024, ROT4(c4), [&](int s) {
      if (s == -1) {
#pragma unroll
        for (int i = 0; i < 2; ++i) ps[i] = __builtin_amdgcn_raw_buffer_load_b128(mkrsrc(ws + WS_ST, S * 64), ((rt0 + i) * 16 + (lane & 15)) * 64 + (lane >> 4) * 16, 0, XAUX);
        BAR();
      }
      if (s == 0) BAR();
      if (s == 18) {
#pragma unroll
        for (int j = 0; j < 7; ++j) { cs[j] = cb[j * 4]; bv[j] = cb[28 + j * 4]; }
      }
      if (s == 19) BAR();
    });
    STAMPT(3, 1);
    const rsrc_t ro = mkrsrc(ws + WS_H1, S * F * 2);
#pragma unroll
    for (int i = 0; i < 2; ++i) {
      const float m = xq_sum(__uint_as_float(ps[i][0]) + __uint_as_float(ps[i][2])) * (1.f / D);
      const float var = fmaxf(xq_sum(__uint_as_float(ps[i][1]) + __uint_as_float(ps[i][3])) * (1.f / D) - m * m, 0.f);
      const float r1 = __builtin_amdgcn_rsqf(var + 1e-5f);
      const float rstd = r1 * __builtin_amdgcn_rsqf(var * r1 * r1 + 1e-5f);
      const float mr = -m * rstd;
      f32x4 v[7], t[7];
#pragma unroll
      for (int j = 0; j < 7; ++j) t[j] = cs[j] * mr + bv[j];
      SBAR();
#pragma unroll
      for (int j = 0; j < 7; ++j) v[j] = g.acc[i][j] * rstd + t[j];
      SBAR();
#pragma unroll
      for (int j = 0; j < 7; ++j) t[j] = v[j] * v[j];
      SBAR();
#pragma unroll
      for (int j = 0; j < 7; ++j) t[j] = t[j] * -0.1029432397f + -2.302208198f;
      SBAR();
#pragma unroll
      for (int j = 0; j < 7; ++j) t[j] = t[j] * v[j];
      SBAR();
#pragma unroll
      for (int j = 0; j < 7; ++j) t[j] = f32x4{EX(t[j][0]), EX(t[j][1]), EX(t[j][2]), EX(t[j][3])};
      SBAR();
#pragma unroll
      for (int j = 0; j < 7; ++j) t[j] = t[j] + 1.f;
      SBAR();
#pragma unroll
      for (int j = 0; j < 7; ++j) t[j] = f32x4{__builtin_amdgcn_rcpf(t[j][0]), __builtin_amdgcn_rcpf(t[j][1]), __builtin_amdgcn_rcpf(t[j][2]), __builtin_amdgcn_rcpf(t[j][3])};
      SBAR();
#pragma unroll
      for (int j = 0; j < 7; ++j) t[j] = t[j] * v[j];
      SBAR();
      u32x2 h1[7];
#pragma unroll
      for (int j = 0; j < 7; ++j) h1[j] = pack4(t[j]);
      store_tiles<7>(ro, rt0 + i, gct0, nct, 54, lane, h1);
      SBAR();
    }
    STAMPT(3, 2);
    WAITV(0);
    if (lane == 0) signal(ws, FL_H1 + (rt0 >> 1) * 16 + c4, tag);
    STAMPT(3, 3);
  }
  {
    GS<5> g;
#pragma unroll
    for (int j = 0; j < 5; ++j) { g.acc[0][j] = unpack4(*(const u32x2*)(a.b2 + c * 80 + j * 16 + (lane >> 4) * 4)) + unpack4(hv[j]); g.acc[1][j] = f32x4{}; }
#pragma unroll
    for (int j = 0; j < 5; ++j) asm volatile("" : "+v"(g.acc[0][j]), "+v"(g.acc[1][j]));
    const rsrc_t ra = mkrsrc(ws + WS_H1, S * F * 2);
    const unsigned va0 = (rt * 54 + kh) * 1024 + lane * 16, va1 = ((rt ^ 1) * 54 + kh) * 1024 + lane * 16;
    await(ws, FL_H1 + (grp * 2 + rh) * 16 + (lane & 15), tag);
    STAMPT(4, 0);
    gemm_run<5, 27, P5_PD, 10240, 16, true>(g, (const LAS char*)smem + kh * 5120 + lane * 16, ra, va0, va1, 2048, ROT5(c), [&](int s) { if (s == -1) BAR(); });
    STAMPT(4, 1);
    pair_reduce<5>(g, smem, wid);
#pragma unroll
    for (int j = 0; j < 5; ++j) *(u32x2*)(a.out + (size_t)tok * D + c * 80 + j * 16 + (lane >> 4) * 4) = pack4(g.acc[0][j]);
    STAMPT(4, 2);
  }
}

DI void main_helper(const Args& a, char* smem, int head, int hw, u64 tag) {
  char* ws = a.ws;
  const int lane = threadIdx.x & 63, b = blockIdx.x;
  const char* kv = ws + WS_KV + (size_t)head * 64 * KVT;
  const unsigned lds0 = (unsigned)(uintptr_t)smem, v16 = lane * 16;
  auto issue = [&](int c) {
#pragma unroll
    for (int i = 0; i < 6; ++i) { const int pc = hw + 4 * i; if (pc < CH_PIECES) glds16x(v16, kv + (size_t)c * CH_BYTES + pc * 1024, lds0 + (c & 1) * CH_BYTES + pc * 1024); }
  };
  const unsigned ctl = lds0 + CTL2;
  auto refill = [&](int c) { lds_wait<NAP2>((const LAS char*)smem + CTL2 + 64 + (c - 2) * 4); STAMPH(6, c); issue(c); WAITV(0); lds_post(ctl + c * 4, 1); STAMPH(5, c); };
  __builtin_amdgcn_s_setprio(3);
  issue(0); issue(1);
  WAITV(5); lds_post(ctl, 1); STAMPH(5, 0);
  WAITV(0); lds_post(ctl + 4, 1); STAMPH(5, 1);
  const int c = b >> 5, c4 = ((b >> 5) << 1) | ((b >> 3) & 1), gct0 = (c4 * 27) >> 2;
  retile_store(ws, (const LAS char*)smem + LDS_RT, hw);
  WAITV(0);
  if (lane == 0 && __hip_atomic_fetch_add((LAS unsigned*)(smem + CTL2 + 60), 1u, __ATOMIC_RELAXED, __HIP_MEMORY_SCOPE_WORKGROUP) == 3u) signal(ws, FL_W + b, tag);
  refill(2);
  refill(3);
  {
    gu64* p = flagp(ws, FL_W + lane);
#pragma unroll 1
    for (unsigned sp = 0; sp < SPIN_CAP; ++sp) {
      const u64 f0 = __hip_atomic_load(p, RLX_AGENT), f1 = __hip_atomic_load(p + 64, RLX_AGENT), f2 = __hip_atomic_load(p + 128, RLX_AGENT), f3 = __hip_atomic_load(p + 192, RLX_AGENT);
      if (__all(f0 == tag && f1 == tag && f2 == tag && f3 == tag)) break;
      __builtin_amdgcn_s_sleep(4);
    }
    asm volatile("" ::: "memory");
  }
  if (b == 0 && hw == 0 && lane == 0) signal(ws, FL_EPOCH, tag);
  f32x4 cst;
  {
    const int n = gct0 * 16 + (lane < 28 ? lane : (lane < 56 ? lane - 28 : 0)) * 4;
    const rsrc_t rc = mkrsrc(ws + WS_CS1, 4 * 1792 * 4);
    f32x4 t[4];
#pragma unroll
    for (int k = 0; k < 4; ++k) t[k] = __builtin_bit_cast(f32x4, __builtin_amdgcn_raw_buffer_load_b128(rc, n * 4, k * 7168, XAUX));
    const f32x4 bv = unpack4(*(const u32x2*)(a.b1 + n));
    cst = lane < 28 ? (t[0] + t[1]) + (t[2] + t[3]) : bv;
    asm volatile("" : "+v"(cst));
  }
  const char* wo = ws + WS_WO + (size_t)c * 102400;
  auto wo_dma = [&](int i0, int i1) {
#pragma unroll 1
    for (int i = i0; i < i1; ++i) { const int p = i * 4 + hw; int q = p + ROT3(c) * 10; if (q >= 100) q -= 100; glds16x(v16, wo + q * 1024, lds0 + LDS_WO + p * 1024); }
  };
  bf16x8 wa[35], wb[20];
  const rsrc_t r1 = mkrsrc(ws + WS_W1 + (size_t)gct0 * 20480, 7 * 20480), r2 = mkrsrc(ws + WS_W2 + (size_t)c * 276480, 276480);
  auto ld1 = [&](int i) { int k = (i / 7) * 4 + hw + ROT4(c4); if (k >= 20) k -= 20; return ldfrag(r1, v16, ((i % 7) * 20 + k) * 1024); };
  auto ld2 = [&](int i) { int p = i * 4 + hw + ROT5(c) * 10; if (p >= 270) p -= 270; return ldfrag(r2, v16, p * 1024); };
  refill(4);
  wo_dma(0, 25);
  refill(5);
  lds_post(ctl + 32, 1);
#pragma unroll
  for (int i = 0; i < 20; ++i) wa[i] = ld1(i);
  SBAR();
  refill(6);
#pragma unroll
  for (int i = 20; i < 35; ++i) wa[i] = ld1(i);
  SBAR();
  refill(7);
#pragma unroll
  for (int i = 0; i < 20; ++i) wb[i] = ld2(i);
  SBAR();
  lds_wait<NAP2>((const LAS char*)smem + CTL2 + 64 + 28);
  __builtin_amdgcn_s_setprio(0);
  LAS bf16x8* lw = (LAS bf16x8*)smem + hw * 7 * 64 + lane;
#pragma unroll
  for (int i = 0; i < 14; ++i) lw[((i / 7) * 28 + i % 7) * 64] = wa[i];
  BAR();
#pragma unroll
  for (int i = 14; i < 21; ++i) lw[((i / 7) * 28 + i % 7) * 64] = wa[i];
  BAR();
#pragma unroll
  for (int i = 21; i < 35; ++i) lw[((i / 7) * 28 + i % 7) * 64] = wa[i];
  LGKM0();
  BAR();
  if (hw == 0 && lane < 56) *((LAS f32x4*)(smem + LDS_CB) + lane) = cst;
  LGKM0(); BAR();
#pragma unroll
  for (int i = 0; i < 35; ++i) { wa[i] = ld2(20 + i); SBAR(); __builtin_amdgcn_s_sleep(PACE); SBAR(); }
  BAR();
  LAS bf16x8* l2 = (LAS bf16x8*)smem + hw * 64 + lane;
#pragma unroll
  for (int i = 0; i < 20; ++i) l2[i * 256] = wb[i];
  LGKM0(); SBAR();
#pragma unroll
  for (int i = 0; i < 13; ++i) wb[i] = ld2(55 + i);
  SBAR();
#pragma unroll
  for (int i = 0; i < 20; ++i) l2[(20 + i) * 256] = wa[i];
  LGKM0(); BAR();
  BAR();
#pragma unroll
  for (int i = 0; i < 10; ++i) l2[i * 256] = wa[20 + i];
  BAR();
#pragma unroll
  for (int i = 0; i < 10; ++i) l2[(10 + i) * 256] = i < 5 ? wa[30 + i] : wb[i - 5];
  BAR();
#pragma unroll
  for (int i = 0; i < 8; ++i) l2[(20 + i) * 256] = wb[5 + i];
  LGKM0(); BAR();
  BAR(); BAR();
  BAR();
}

__global__ __launch_bounds__(512, 2) void fused_block(Args a) {
  extern __shared__ __attribute__((aligned(16))) char smem[];
  char* ws = a.ws;
  const int wid = __builtin_amdgcn_readfirstlane(threadIdx.x >> 6), lane = threadIdx.x & 63;
  const int b = blockIdx.x, xg = b & 7, j = b >> 3;
  STAMPT(0, 0);
  const u64 t_start = PADAT >= 0 ? __builtin_amdgcn_s_memrealtime() : 0;
  asm volatile("" :: "s"(a.x), "s"(a.wqkv), "s"(a.ws));
  const int bm = (xg >> 1) * 4 + (j & 3), bn = (xg & 1) * 8 + (j >> 2);
#define K1_ROT (j % 10)
  const int head = (b & 7) * 4 + ((b >> 3) & 3), qc = b >> 5;
  u64 tag = 0;
  if (bn >= 15) {
    tag = get_tag(ws);
    if (wid < 4) { XRow xr; xrow_load(xr, a.x, b * 8 + wid * 2); xrow_publish(xr, ws, b * 8 + wid * 2, tag); }
    k1_colsum(a.x, a.wqkv, ws, smem, bm, wid, tag);
    if (wid >= 4) retile_dma(a, (unsigned)(uintptr_t)smem + LDS_RT, wid - 4);
  }
  if (wid >= 4) {
    if (bn < 15) k1_helper(a, a.x, a.wqkv, ws, smem, bm, bn, K1_ROT, wid - 4, tag);
    WAITV(0); BAR(); BAR();
    main_helper(a, smem, head, wid - 4, tag);
  } else {
    if (bn < 15) {
      XRow xr;
      xrow_load(xr, a.x, b * 8 + wid * 2);
      {
        const char* pc = (const char*)(__builtin_amdgcn_s_getpc() & ~127ll);
        const unsigned off = min((((j & 1) * 4 + wid) * 64 + lane) * 128, CODE_BYTES);
        asm volatile("s_mov_b32 m0, %2\n\ts_nop 0\n\tglobal_load_lds_dword %0, %1" :: "v"(off), "s"(pc), "s"(__builtin_amdgcn_readfirstlane((unsigned)(uintptr_t)smem + 140000 + wid * 256)) : "memory");
      }
      tag = get_tag(ws);
      if (bn < 10) k1_compute<false>(ws, smem, bm, bn, wid, xr, tag); else k1_compute<true>(ws, smem, bm, bn, wid, xr, tag);
    }
    STAMPT(0, 3);
    if (wid == 3 && lane < 32) { ((LAS unsigned*)(smem + CTL2))[lane] = 0; LGKM0(); }
    WAITV(0); BAR();
    if (threadIdx.x == 0) signal(ws, FL_QKV + bn * 16 + bm, tag);
    STAMPT(0, 4);
    const int t0 = (head * 20) >> 7, t1 = (head * 20 + 19) >> 7;
    if (wid == 1) await(ws, FL_QKV + (5 + (lane >> 5) * 5 + ((lane & 16) ? t1 : t0)) * 16 + (lane & 15), tag);
    if (wid == 2) await(ws, FL_QKV + ((lane & 2) ? t1 : t0) * 16 + qc * 2 + (lane & 1), tag);
    PADT(1, 30);
    BAR();
    STAMPT(1, 0);
    attn_compute(ws, smem, head, qc, wid, tag); STAMPT(1, 4); PADT(2, 60); post_compute(a, smem, wid, tag); PADT(0, 100);
  }
}

extern "C" void kernel_launch(void* const* d_in, const int* in_sizes, int n_in, void* d_out, int out_size, void* d_ws, size_t ws_size, hipStream_t stream) {
  Args a;
  a.x = (const u16*)d_in[0]; a.wqkv = (const u16*)d_in[1]; a.wo = (const u16*)d_in[2];
  a.w1 = (const u16*)d_in[4]; a.b1 = (const u16*)d_in[5]; a.w2 = (const u16*)d_in[6]; a.b2 = (const u16*)d_in[7];
  a.out = (u16*)d_out; a.ws = (char*)d_ws;
  (void)hipFuncSetAttribute((const void*)fused_block, hipFuncAttributeMaxDynamicSharedMemorySize, LDS_BYTES);
  fused_block<<<256, 512, LDS_BYTES, stream>>>(a);
}
```

```cpp
#include <hip/hip_runtime.h>
#include <hip/hip_bf16.h>
typedef unsigned short u16;
typedef __attribute__((ext_vector_type(8))) short bf16x8;
typedef __attribute__((ext_vector_type(8))) __bf16 bf8;
typedef __attribute__((ext_vector_type(4))) short s16x4;
typedef __attribute__((ext_vector_type(16))) float f32x16;
typedef __attribute__((ext_vector_type(4))) float f32x4;
typedef __attribute__((ext_vector_type(2))) float f32x2;
typedef __attribute__((ext_vector_type(4))) unsigned u32x4;
typedef __attribute__((ext_vector_type(2))) unsigned u32x2;
typedef __attribute__((ext_vector_type(2))) __bf16 bf2;
#define LAS __attribute__((address_space(3)))
#define DI __device__ __forceinline__
#define SBAR() __builtin_amdgcn_sched_barrier(0)
#define LGKM0() __builtin_amdgcn_s_waitcnt(0xC07F)
#define BAR() asm volatile("s_barrier" ::: "memory")
#define WAITV(n) asm volatile("s_waitcnt vmcnt(%0)" :: "i"(n) : "memory")

constexpr int S = 2048, D = 640, H = 32, DH = 20, F = 1728;
constexpr int QT = 1280, KVT = 2688;
constexpr size_t WS_Q = 0, WS_KV = WS_Q + (size_t)H * 64 * QT, WS_O = WS_KV + (size_t)H * 64 * KVT,
                 WS_H = WS_O + (size_t)S * D * 2, WS_H1 = WS_H + (size_t)S * D * 2,
                 WS_WO = WS_H1 + (size_t)S * F * 2, WS_W1 = WS_WO + (size_t)D * D * 2, WS_W2 = WS_W1 + (size_t)D * F * 2 + 20480,
                 WS_CS1 = WS_W2 + (size_t)D * F * 2, WS_ST = WS_CS1 + 4 * 1792 * 4, WS_XST = WS_ST + (size_t)S * 8 * 8, WS_CSQ = WS_XST + (size_t)S * 16, WS_FL = WS_CSQ + 16384, WS_END = WS_FL + 65536;
#define STAMPT(k, s) do {} while (0)
#define STAMPH(k, s) do {} while (0)
#define PADAT -1
#define PADT(k, us) do { if (PADAT == (k)) { while (__builtin_amdgcn_s_memrealtime() - t_start < (us) * 100ull) __builtin_amdgcn_s_sleep(2); } } while (0)
constexpr int KSKEW = 1;
constexpr float QSCALE = 0.22360679774997896f * 1.4426950408889634f;

DI float bf2f(u16 v) { return __uint_as_float((unsigned)v << 16); }
DI float bflo(unsigned u) { return __uint_as_float(u << 16); }
DI float bfhi(unsigned u) { return __uint_as_float(u & 0xffff0000u); }
DI unsigned pk2(float lo, float hi) { f32x2 v = {lo, hi}; return __builtin_bit_cast(unsigned, __builtin_convertvector(v, bf2)); }
DI void glds16(unsigned voff, const void* sbase, unsigned lds) {
  asm volatile("s_mov_b32 m0, %2\n\ts_nop 0\n\tglobal_load_lds_dwordx4 %0, %1" :: "v"(voff), "s"(sbase), "s"(__builtin_amdgcn_readfirstlane(lds)) : "memory"); }
DI void glds16x(unsigned voff, const void* sbase, unsigned lds) {
  asm volatile("s_mov_b32 m0, %2\n\ts_nop 0\n\tglobal_load_lds_dwordx4 %0, %1 sc1" :: "v"(voff), "s"(sbase), "s"(__builtin_amdgcn_readfirstlane(lds)) : "memory"); }
DI s16x4 trread(const LAS char* p) { return __builtin_amdgcn_ds_read_tr16_b64_v4i16((LAS s16x4*)p); }
DI bf16x8 cat8(s16x4 a, s16x4 b) { return __builtin_shufflevector(a, b, 0, 1, 2, 3, 4, 5, 6, 7); }
#define MFMA32(a, b, c) __builtin_amdgcn_mfma_f32_32x32x16_bf16(__builtin_bit_cast(bf8, a), __builtin_bit_cast(bf8, b), c, 0, 0, 0)
#define MFMA32K8(a, b, c) __builtin_amdgcn_mfma_f32_32x32x8bf16_1k(a, b, c, 0, 0, 0)
#define MFMA16(a, b, c) __builtin_amdgcn_mfma_f32_16x16x32_bf16(__builtin_bit_cast(bf8, a), __builtin_bit_cast(bf8, b), c, 0, 0, 0)
DI void mfma16_acc(f32x4& c, bf16x8 a, bf16x8 b) { asm volatile("v_mfma_f32_16x16x32_bf16 %0, %1, %2, %0" : "+v"(c) : "v"(a), "v"(b)); }
DI float xhalf_sum(float v) { auto r = __builtin_amdgcn_permlane32_swap(__float_as_uint(v), __float_as_uint(v), false, false); return __uint_as_float(r[0]) + __uint_as_float(r[1]); }
DI float xhalf_max(float v) { auto r = __builtin_amdgcn_permlane32_swap(__float_as_uint(v), __float_as_uint(v), false, false); return fmaxf(__uint_as_float(r[0]), __uint_as_float(r[1])); }
DI float xq_sum(float v) { auto r = __builtin_amdgcn_permlane16_swap(__float_as_uint(v), __float_as_uint(v), false, false); return xhalf_sum(__uint_as_float(r[0]) + __uint_as_float(r[1])); }
typedef __amdgpu_buffer_rsrc_t rsrc_t;
DI rsrc_t mkrsrc(const void* p, unsigned bytes) { return __builtin_amdgcn_make_buffer_rsrc((void*)p, 0, bytes, 0x00020000); }
constexpr int XAUX = 16;
DI bf16x8 ldfrag(rsrc_t r, unsigned voff, unsigned soff) { return __builtin_bit_cast(bf16x8, __builtin_amdgcn_raw_buffer_load_b128(r, voff, soff, XAUX)); }
DI void st16(rsrc_t r, unsigned voff, u32x4 v) { __builtin_amdgcn_raw_buffer_store_b128(v, r, voff, 0, XAUX); }
DI void st8(rsrc_t r, unsigned voff, u32x2 v) { __builtin_amdgcn_raw_buffer_store_b64(v, r, voff, 0, XAUX); }

DI s16x4 ldhalf(rsrc_t r, unsigned voff, unsigned soff) { return __builtin_bit_cast(s16x4, __builtin_amdgcn_raw_buffer_load_b64(r, voff, soff, XAUX)); }
DI void st16s(rsrc_t r, unsigned voff, unsigned soff, u32x4 v) { __builtin_amdgcn_raw_buffer_store_b128(v, r, voff + soff, 0, XAUX); }
DI void st8s(rsrc_t r, unsigned voff, unsigned soff, u32x2 v) { __builtin_amdgcn_raw_buffer_store_b64(v, r, voff, soff, XAUX); }
typedef unsigned long long u64;
typedef __attribute__((address_space(1))) u64 gu64;
#define RLX_AGENT __ATOMIC_RELAXED, __HIP_MEMORY_SCOPE_AGENT
constexpr int FL_EPOCH = 0, FL_QKV = 64  , FL_W = FL_QKV + 256  , FL_O = FL_W + 256  ,
              FL_H = FL_O + 1024  , FL_H1 = FL_H + 1024  ;
constexpr unsigned SPIN_CAP = 1u << 17;
DI u64 get_tag(char* ws) {
  u64 e;
  asm volatile("s_load_dwordx2 %0, %1, 0x0\n\ts_waitcnt lgkmcnt(0)" : "=s"(e) : "s"(ws + WS_FL + FL_EPOCH * 8) : "memory");
  return e * 6364136223846793005ull + 1442695040888963407ull;
}
DI gu64* flagp(char* ws, int i) { return (gu64*)(ws + WS_FL) + i; }
DI void signal(char* ws, int i, u64 tag) { __hip_atomic_store(flagp(ws, i), tag, RLX_AGENT); }
DI void await(char* ws, int i  , u64 tag) {
  gu64* p = flagp(ws, i);
#pragma unroll 1
  for (unsigned sp = 0; sp < SPIN_CAP; ++sp) {
    if (__all(__hip_atomic_load(p, RLX_AGENT) == tag)) break;
    __builtin_amdgcn_s_sleep(1);
  }
  asm volatile("" ::: "memory");
}

DI void lds_post(unsigned addr, unsigned one) { asm volatile("s_mov_b64 exec, 1\n\tds_add_u32 %0, %1\n\ts_mov_b64 exec, -1" :: "v"(addr), "v"(one) : "memory"); }
template <int NAP = 1> DI void lds_wait(const LAS char* p) {
#pragma unroll 1
  for (unsigned sp = 0; sp < (1u << 22) && __builtin_amdgcn_readfirstlane(*(const volatile LAS unsigned*)p) < 4u; ++sp) __builtin_amdgcn_s_sleep(NAP);
  asm volatile("" ::: "memory");
}
#define NAP1 3
#define NAP2 12
constexpr int CTL1 = 133120  , CTL2 = 57344  ;

constexpr int K1_NSLOT = 4, K1_STAGE = 32768;
#define K1_PRE 2
template <int CTRL> DI float dpp_add(float v) { return v + __builtin_bit_cast(float, __builtin_amdgcn_update_dpp(0, __builtin_bit_cast(int, v), CTRL, 0xf, 0xf, true)); }
DI float sum32(float v) {
  v = dpp_add<0x128>(v); v = dpp_add<0x124>(v); v = dpp_add<0x122>(v); v = dpp_add<0x121>(v);
  auto r = __builtin_amdgcn_permlane16_swap(__float_as_uint(v), __float_as_uint(v), false, false); return __uint_as_float(r[0]) + __uint_as_float(r[1]);
}
struct XRow { u32x4 a, b; u32x2 c; };
DI void xrow_load(XRow& v, const u16* x, int row0) {
  const int lane = threadIdx.x & 63;
  const char* p = (const char*)x + (size_t)(row0 + (lane >> 5)) * 1280;
  v.a = *(const u32x4*)(p + (lane & 31) * 16); v.b = *(const u32x4*)(p + 512 + (lane & 31) * 16); v.c = *(const u32x2*)(p + 1024 + (lane & 31) * 8);
}
DI void xrow_publish(const XRow& v, char* ws, int row0, u64 tag) {
  const int lane = threadIdx.x & 63;
  const u32x4 va = v.a, vb = v.b; const u32x2 vc = v.c;
  float e[20];
#pragma unroll
  for (int i = 0; i < 4; ++i) { e[2 * i] = bflo(va[i]); e[2 * i + 1] = bfhi(va[i]); e[8 + 2 * i] = bflo(vb[i]); e[9 + 2 * i] = bfhi(vb[i]); }
  e[16] = bflo(vc[0]); e[17] = bfhi(vc[0]); e[18] = bflo(vc[1]); e[19] = bfhi(vc[1]);
  f32x2 s = {0.f, 0.f};
#pragma unroll
  for (int i = 0; i < 20; i += 2) s += f32x2{e[i], e[i + 1]};
  const float m = sum32(s[0] + s[1]) * (1.f / D);
  f32x2 q = {0.f, 0.f};
#pragma unroll
  for (int i = 0; i < 20; i += 2) { const f32x2 d = f32x2{e[i], e[i + 1]} - m; q += d * d; }
  const float rstd = __builtin_amdgcn_rsqf(sum32(q[0] + q[1]) * (1.f / D) + 1e-5f);
  if ((lane & 31) == 0) st16(mkrsrc(ws + WS_XST, S * 16), (row0 + (lane >> 5)) * 16, u32x4{__float_as_uint(m), (unsigned)tag, __float_as_uint(rstd), (unsigned)(tag >> 32)});
}
struct F4 { bf16x8 a0, a1, b0, b1; };
template <bool isV> DI void k1_compute(char* ws, char* smem, int bm, int bn, int wid, XRow& xr, u64 tag, u64 t_start) {
  const int lane = threadIdx.x & 63, r = lane & 31, hh = lane >> 5;
  const int wm = wid >> 1, wn = wid & 1;
  f32x16 c00 = f32x16{}, c01 = f32x16{}, c10 = f32x16{}, c11 = f32x16{};
  const int fA = (r >> 1) & 7;
  const int gi = lane >> 4, q = (lane & 15) >> 2, p = lane & 3;
  const LAS char* sm = (const LAS char*)smem;
  const LAS char* pa[2][4]; const LAS char* pb[2][2];
#pragma unroll
  for (int w = 0; w < 2; ++w) {
#pragma unroll
    for (int s = 0; s < 4; ++s) { pa[w][s] = sm + w * 65536 + (wm * 64 + r) * 128 + (((2 * s + hh) ^ fA) << 4); asm volatile("" : "+v"(pa[w][s])); }
#pragma unroll
    for (int jn = 0; jn < 2; ++jn) { pb[w][jn] = sm + w * 65536 + 16384 + (8 * (gi >> 1) + q) * 256 + 32 * (gi & 1) + 8 * p + (((2 * wn + jn) ^ q) << 6); asm volatile("" : "+v"(pb[w][jn])); }
  }
  auto lda = [&](int i, int t) { const int sl = (i >> 2) % K1_NSLOT; return *(const LAS bf16x8*)(pa[sl >> 1][i & 3] + (sl & 1) * K1_STAGE + t * 4096); };
  auto ldb = [&](int i, int jn) { const int sl = (i >> 2) % K1_NSLOT; const LAS char* b = pb[sl >> 1][jn] + (sl & 1) * K1_STAGE + (i & 3) * 4096; return cat8(trread(b), trread(b + 1024)); };
#define K1_MM(c, a, b) asm volatile("v_mfma_f32_32x32x16_bf16 %0, %1, %2, %0" : "+v"(c) : "v"(isV ? a : b), "v"(isV ? b : a))
  const bool isQ = bn < 5; const int tstride = isQ ? QT : KVT;
  const rsrc_t rimg = mkrsrc(ws + (isQ ? WS_Q : WS_KV), H * 64 * KVT);
  unsigned soff[8];
  if (!isV) {
#pragma unroll
    for (int u = 0; u < 8; ++u) {
      const int n = (bn % 5) * 128 + wn * 64 + (u >> 2) * 32 + 8 * (u & 3) + 4 * hh, head = n / 20, d = n - head * 20;
      soff[u] = head * 64 * tstride + (d < 16 ? (d >> 3) * 512 + r * 16 + (d & 7) * 2 : 1024 + r * 8);
      asm volatile("" : "+v"(soff[u]));
    }
  } else {
#pragma unroll
    for (int jn = 0; jn < 2; ++jn) {
      const int n = (bn - 10) * 128 + wn * 64 + jn * 32 + r, head = n / 20, d = n - head * 20;
      soff[jn] = head * 64 * KVT + 1280 + hh * 352 + d * 16;
      {
        const unsigned c1 = d == 18 ? 0x3F803F80u : 0u, o = d >= 18 ? soff[jn] + 32 : H * 64 * KVT;
#pragma unroll
        for (int u = 0; u < 4; ++u) st16s(rimg, o + (u & 1) * 704, (bm * 4 + wm * 2 + (u >> 1)) * KVT, u32x4{c1, c1, c1, c1});
      }
      asm volatile("" : "+v"(soff[jn]));
    }
  }
  asm volatile("" : "+v"(c00), "+v"(c01), "+v"(c10), "+v"(c11));
  SBAR();
  xrow_publish(xr, ws, blockIdx.x * 8 + wid * 2, tag);
  BAR(); SBAR();
  PADT(3, 15);
  STAMPT(0, 1);
  F4 f[3];
#pragma unroll
  for (int i = 0; i < 2; ++i) { f[i].a0 = lda(i, 0); f[i].b0 = ldb(i, 0); f[i].b1 = ldb(i, 1); f[i].a1 = lda(i, 1); }
  SBAR();
#pragma unroll
  for (int i = 0; i < 40; ++i) {
    F4& x = f[i % 3]; F4& y = f[(i + 2) % 3];
    const bool ld = i + 2 < 40;
    if (ld && (i & 3) == 2) { BAR(); SBAR(); }
    K1_MM(c00, x.a0, x.b0); SBAR(); if (ld) { y.a0 = lda(i + 2, 0); y.b0 = ldb(i + 2, 0); } SBAR();
    K1_MM(c01, x.a0, x.b1); SBAR(); if (ld) { y.b1 = ldb(i + 2, 1); } SBAR();
    K1_MM(c10, x.a1, x.b0); SBAR(); if (ld) { y.a1 = lda(i + 2, 1); } SBAR();
    K1_MM(c11, x.a1, x.b1); SBAR();
  }
  asm volatile("s_nop 11" ::: "memory"); SBAR();
  PADT(6, 22);
  STAMPT(0, 2);
  BAR();
  const LAS float* sMean = (const LAS float*)(smem + K1_NSLOT * K1_STAGE); const LAS float* sRstd = sMean + 128; const LAS float* sCs = sMean + 256;
  if (!isV) {
#pragma unroll
    for (int i = 0; i < 2; ++i) {
      const int tl = wm * 64 + i * 32;
      const float m = sMean[tl + r], rstd = sRstd[tl + r] * (isQ ? QSCALE : 1.f);
      int tile = bm * 4 + wm * 2 + i; if (!isQ) tile = (tile + 64 - KSKEW) & 63;
#pragma unroll
      for (int jn = 0; jn < 2; ++jn) {
        const f32x16& c = i ? (jn ? c11 : c10) : (jn ? c01 : c00);
#pragma unroll
        for (int g = 0; g < 4; ++g) {
          const f32x4 c4 = *(const LAS f32x4*)(sCs + wn * 64 + jn * 32 + 8 * g + 4 * hh);
          u32x2 v;
          v[0] = pk2((c[4 * g] - m * c4[0]) * rstd, (c[4 * g + 1] - m * c4[1]) * rstd);
          v[1] = pk2((c[4 * g + 2] - m * c4[2]) * rstd, (c[4 * g + 3] - m * c4[3]) * rstd);
          st8s(rimg, soff[jn * 4 + g], tile * tstride, v);
        }
      }
    }
  } else {
#pragma unroll
    for (int i = 0; i < 2; ++i) {
      const int tile = bm * 4 + wm * 2 + i;
#pragma unroll
      for (int s = 0; s < 2; ++s) {
        f32x4 m4[2], r4[2];
#pragma unroll
        for (int g = 0; g < 2; ++g) { const int tl = wm * 64 + i * 32 + 16 * s + 8 * g + 4 * hh; m4[g] = *(const LAS f32x4*)(sMean + tl); r4[g] = *(const LAS f32x4*)(sRstd + tl); }
#pragma unroll
        for (int jn = 0; jn < 2; ++jn) {
          const f32x16& c = i ? (jn ? c11 : c10) : (jn ? c01 : c00);
          const float cs = sCs[wn * 64 + jn * 32 + r];
          u32x4 v;
#pragma unroll
          for (int g = 0; g < 2; ++g) {
            const int e = 8 * s + 4 * g;
            v[2 * g] = pk2((c[e] - m4[g][0] * cs) * r4[g][0], (c[e + 1] - m4[g][1] * cs) * r4[g][1]);
            v[2 * g + 1] = pk2((c[e + 2] - m4[g][2] * cs) * r4[g][2], (c[e + 3] - m4[g][3] * cs) * r4[g][3]);
          }
          st16s(rimg, soff[jn] + s * 704, tile * KVT, v);
        }
      }
    }
  }
}
struct Args { const u16 *x, *wqkv, *wo, *w1, *b1, *w2, *b2; u16* out; char* ws; };
constexpr int LDS_RT = 143360;
DI void retile_dma(const Args& a, unsigned lds, int hw) {
  const int lane = threadIdx.x & 63, b = blockIdx.x;
  if (b >= 40 && b < 148) {
    const int jb = b - 40;
    const char* src = (const char*)a.w1 + (size_t)((jb & 3) * 160) * (F * 2) + (jb >> 2) * 128;
#pragma unroll
    for (int i = 0; i < 5; ++i) glds16((lane >> 3) * (F * 2) + (lane & 7) * 16, src + (size_t)(hw * 5 + i) * 8 * (F * 2), lds + (hw * 5 + i) * 1024);
  } else {
    const int jb = b < 40 ? b : b - 148;
    const char* src = (const char*)(b < 40 ? a.wo : a.w2) + (size_t)((jb >> 1) * 32 + hw * 8) * (D * 2) + (jb & 1) * 640;
#pragma unroll
    for (int i = 0; i < 5; ++i) { const int idx = i * 64 + lane, row = idx / 40; glds16(row * (D * 2) + (idx - row * 40) * 16, src, lds + (hw * 5 + i) * 1024); }
  }
}
DI void retile_store(char* ws, const LAS char* img, int hw) {
  const int lane = threadIdx.x & 63, b = blockIdx.x;
  const bool is1 = b >= 40 && b < 148;
  const int rs = is1 ? 128 : 640;
  const LAS char* p = img + (lane >> 2) * rs + (lane & 3) * 8 + (is1 ? hw * 32 : hw * 160);
  const rsrc_t rw = mkrsrc(ws + (b < 40 ? WS_WO : (is1 ? WS_W1 : WS_W2)), D * F * 2);
  const int jb = b < 40 ? b : (is1 ? b - 40 : b - 148);
  const int d0 = is1 ? ((jb >> 2) * 4 + hw) * 20 + (jb & 3) * 5 : (((jb & 1) * 4 + hw) * (b < 40 ? 20 : 54) + (jb >> 1)) * 5;
  float cs = 0.f;
#pragma unroll
  for (int k = 0; k < 5; ++k) {
    const LAS char* q = p + k * (is1 ? 4096 : 32);
    const u32x4 v = __builtin_bit_cast(u32x4, cat8(trread(q), trread(q + 16 * rs)));
    st16(rw, (d0 + k) * 1024 + lane * 16, v);
#pragma unroll
    for (int j = 0; j < 4; ++j) { asm volatile("v_add_f32 %0, %0, %1" : "+v"(cs) : "v"(bflo(v[j]))); asm volatile("v_add_f32 %0, %0, %1" : "+v"(cs) : "v"(bfhi(v[j]))); }
  }
  if (is1) { cs = xq_sum(cs); if (lane < 16) __builtin_amdgcn_raw_buffer_store_b32(__float_as_uint(cs), mkrsrc(ws + WS_CS1, 4 * 1792 * 4), ((jb & 3) * 1792 + 16 * ((jb >> 2) * 4 + hw) + lane) * 4, 0, XAUX); }
}
DI void k1_helper(const Args& a, const u16* __restrict__ x, const u16* __restrict__ w, char* ws, char* smem, int bm, int bn, int rot, int hw, u64& tag) {
  const int lane = threadIdx.x & 63;
  const unsigned lds0 = (unsigned)(uintptr_t)smem;
  const unsigned voffA0 = (lane >> 3) * 1280 + (((lane & 7) ^ (lane >> 4)) << 4);
  const unsigned voffA1 = (lane >> 3) * 1280 + (((lane & 7) ^ (4 + (lane >> 4))) << 4);
  const unsigned voffB = (lane >> 4) * 3840 + (((((lane & 15) >> 2) ^ (lane >> 4)) * 4 + (lane & 3)) << 4);
  const char* xa = (const char*)x + (size_t)(bm * 128 + hw * 32) * 1280;
  const char* wb = (const char*)w + (size_t)(hw * 16) * 3840 + bn * 256;
  auto issue = [&](int st) {
    int ks = st + rot; if (ks >= 10) ks -= 10;
    const unsigned dst = lds0 + (st % K1_NSLOT) * K1_STAGE + hw * 4096;
#pragma unroll
    for (int i = 0; i < 4; ++i) glds16((i & 1) ? voffA1 : voffA0, xa + i * 8 * 1280 + ks * 128, dst + i * 1024);
#pragma unroll
    for (int i = 0; i < 4; ++i) glds16(voffB, wb + (size_t)(ks * 64 + i * 4) * 3840, dst + 16384 + i * 1024);
  };
  issue(0); issue(1);
  if (K1_PRE == 3) issue(2);
  WAITV(8 * (K1_PRE - 1)); BAR();
  if (K1_PRE == 2) issue(2);
  WAITV(8); BAR();
  issue(3);
  tag = get_tag(ws);
#pragma unroll 1
  for (int st = 1; st < 7; ++st) { WAITV(8); BAR(); issue(st + 3); }
  retile_dma(a, lds0 + LDS_RT, hw);
  WAITV(13); BAR();
  WAITV(5); BAR();
  const rsrc_t rs = mkrsrc(ws + WS_XST, S * 16 + 16384);
  const unsigned go = lane < 32 ? (bm * 128 + hw * 32 + lane) * 16 : S * 16 + (bn * 64 + hw * 16 + (lane & 15)) * 16;
  u32x4 rec;
#pragma unroll 1
  for (unsigned sp = 0; sp < SPIN_CAP; ++sp) {
    asm volatile("" ::: "memory");
    rec = __builtin_amdgcn_raw_buffer_load_b128(rs, go, 0, XAUX);
    if (__all(rec[1] == (unsigned)tag && rec[3] == (unsigned)(tag >> 32))) break;
    __builtin_amdgcn_s_sleep(1);
  }
  LAS unsigned* st = (LAS unsigned*)(smem + K1_NSLOT * K1_STAGE);
  if (lane < 32) { st[hw * 32 + lane] = rec[0]; st[128 + hw * 32 + lane] = rec[2]; }
  else if (lane < 48) { st[256 + hw * 32 + 2 * (lane & 15)] = rec[0]; st[257 + hw * 32 + 2 * (lane & 15)] = rec[2]; }
  LGKM0(); BAR();
}
DI void k1_colsum(const u16* __restrict__ x, const u16* __restrict__ w, char* ws, char* smem, int u, int wid, u64 tag) {
  const int lane = threadIdx.x & 63;
  if (u < 15) {
    const char* p = (const char*)w + (size_t)(wid * 80 + (lane >> 4)) * 3840 + u * 256 + (lane & 15) * 16;
    u32x4 v[20];
#pragma unroll
    for (int i = 0; i < 20; ++i) v[i] = *(const u32x4*)(p + (size_t)i * 4 * 3840);
    float a[8];
#pragma unroll
    for (int e = 0; e < 8; ++e) a[e] = 0.f;
#pragma unroll
    for (int i = 0; i < 20; ++i)
#pragma unroll
      for (int e = 0; e < 4; ++e) { a[2 * e] += bflo(v[i][e]); a[2 * e + 1] += bfhi(v[i][e]); }
    LAS float* part = (LAS float*)smem;
#pragma unroll
    for (int e = 0; e < 8; ++e) { a[e] = xq_sum(a[e]); if (lane < 16) part[wid * 128 + lane * 8 + e] = a[e]; }
    LGKM0(); BAR();
    if (wid == 0) {
      float c0 = 0.f, c1 = 0.f;
#pragma unroll
      for (int k = 0; k < 8; ++k) { c0 += part[k * 128 + 2 * lane]; c1 += part[k * 128 + 2 * lane + 1]; }
      st16(mkrsrc(ws + WS_CSQ, 16384), (u * 64 + lane) * 16, u32x4{__float_as_uint(c0), (unsigned)tag, __float_as_uint(c1), (unsigned)(tag >> 32)});
    }
  }
}

constexpr int CH_TILES = 8, CH_BYTES = CH_TILES * KVT, NCH = 64 / CH_TILES, CH_PIECES = CH_BYTES / 1024;
#define EX(v) __builtin_amdgcn_exp2f(v)
#define PF(p) __builtin_bit_cast(bf16x8, p)
DI float rowmax16(const f32x16& s) {
  float m = s[0];
#pragma unroll
  for (int e = 1; e < 16; ++e) m = fmaxf(m, s[e]);
  return xhalf_max(m);
}
DI s16x4 shift_from(const f32x16& s, s16x4 q2, int hh) {
  const float m = -rowmax16(s);
  const unsigned mh = pk2(m, 0.f) & 0xffffu, ml = pk2(m - bflo(mh), 0.f) & 0xffffu;
  return hh ? s16x4{(short)mh, (short)ml, 0, 0} : q2;
}
DI bool attn_unsafe(const f32x16& o, int hh) {
  const float l = hh ? o[8] : o[12];
  bool bad = !(l >= 1e-20f && l <= 1e30f);
#pragma unroll
  for (int e = 0; e < 12; ++e) bad |= !(fabsf(o[e]) <= 3e38f);
  return __any(bad);
}
DI f32x16 attn_online(rsrc_t kv, bf16x8 q1, s16x4 q2, int lane, int k2off, int voff) {
  if (lane >= 32) q2 = s16x4{0, 0, 0, 0};
  float m = -INFINITY; f32x16 o = f32x16{};
#pragma unroll 1
  for (int t = 0; t < 64; ++t) {
    const int kt = ((t + 64 - KSKEW) & 63) * KVT, vt = t * KVT;
    f32x16 s = MFMA32(ldfrag(kv, lane * 16, kt), q1, f32x16{}); s = MFMA32K8(ldhalf(kv, k2off, kt), q2, s);
    const float mn = fmaxf(m, rowmax16(s)), al = EX(m - mn); m = mn;
#pragma unroll
    for (int e = 0; e < 16; ++e) { o[e] *= al; s[e] = EX(s[e] - mn); }
    const u32x4 p0 = {pk2(s[0], s[1]), pk2(s[2], s[3]), pk2(s[4], s[5]), pk2(s[6], s[7])};
    const u32x4 p1 = {pk2(s[8], s[9]), pk2(s[10], s[11]), pk2(s[12], s[13]), pk2(s[14], s[15])};
    o = MFMA32(ldfrag(kv, voff, vt), PF(p0), o); o = MFMA32(ldfrag(kv, voff + 704, vt), PF(p1), o);
  }
  return o;
}
DI void attn_compute(char* ws, char* smem, int head, int qc, int wid, u64 tag) {
  const int lane = threadIdx.x & 63, r = lane & 31, hh = lane >> 5;
  const rsrc_t kv = mkrsrc(ws + WS_KV + (size_t)head * 64 * KVT, 64 * KVT);
  const int qt = qc * 8 + wid * 2;
  const rsrc_t rq = mkrsrc(ws + WS_Q + (size_t)(head * 64 + qt) * QT, 2 * QT);
  const int k2off = hh ? 1600 : 1024 + r * 8;
  const int voff = 1280 + hh * 352 + (r < 21 ? r : (r == 24 ? 20 : 21)) * 16;
  const bf16x8 qa1 = ldfrag(rq, lane * 16, 0), qb1 = ldfrag(rq, lane * 16, QT);
  s16x4 qa2 = ldhalf(rq, 1024 + r * 8, 0), qb2 = ldhalf(rq, 1024 + r * 8, QT);
  if (hh) { qa2 = s16x4{0, 0, 0, 0}; qb2 = s16x4{0, 0, 0, 0}; }
  unsigned ooff[3];
#pragma unroll
  for (int g = 0; g < 3; ++g) {
    const int k = head * 20 + 8 * g + 4 * hh;
    ooff[g] = (g == 2 && hh) ? S * D * 2 : ((qt * 2 + (r >> 4)) * 20 + (k >> 5)) * 1024 + (((k >> 2) & 3) * 16 + (r & 15)) * 16 + ((k >> 4) & 1) * 8;
    asm volatile("" : "+v"(ooff[g]));
  }
  bf16x8 k1 = ldfrag(kv, lane * 16, 63 * KVT); s16x4 k2 = ldhalf(kv, k2off, 63 * KVT);
  f32x16 oa, ob;
  { f32x16 s = MFMA32(k1, qa1, f32x16{}); s = MFMA32K8(k2, qa2, s); qa2 = shift_from(s, qa2, hh);
    s = MFMA32(k1, qb1, f32x16{}); s = MFMA32K8(k2, qb2, s); qb2 = shift_from(s, qb2, hh); }
  const unsigned lds0 = (unsigned)(uintptr_t)smem;
  unsigned ctl = lds0 + CTL2;
  STAMPT(1, 1);
  asm volatile(
    ".macro AT_E r\n v_exp_f32 v[\\r], v[\\r]\n v_exp_f32 v[\\r+1], v[\\r+1]\n.endm\n"
    ".macro AT_C d, r\n v_cvt_pk_bf16_f32 v[\\d], v[\\r], v[\\r+1]\n.endm\n"
    ".macro AT_H sc, sn, pc, pp, o, x, xk2, q1, q2, y, yk2, off, mode\n"
    " v_mfma_f32_32x32x16_bf16 v[\\o:\\o+15], v[\\x+4:\\x+7], v[\\pp:\\pp+3], v[\\o:\\o+15]\n"
    " .if \\mode & 1\n ds_read_b128 v[\\y+4:\\y+7], v130 offset:\\off\n .endif\n"
    " AT_C \\pp+6, \\sn+12\n AT_C \\pp+7, \\sn+14\n AT_E \\sc\n AT_E \\sc+2\n"
    " .if (\\mode & 16) == 0\n v_mfma_f32_32x32x16_bf16 v[\\sn:\\sn+15], v[\\x:\\x+3], v[\\q1:\\q1+3], 0\n .endif\n"
    " .if \\mode & 1\n ds_read_b128 v[\\y:\\y+3], v128 offset:\\off\n .endif\n"
    " .if \\mode & 4\n ds_read_b32 v133, v131 offset:\\off\n .endif\n"
    " AT_E \\sc+4\n AT_C \\pc, \\sc\n AT_E \\sc+6\n AT_C \\pc+1, \\sc+2\n"
    " .if (\\mode & 16) == 0\n v_mfma_f32_32x32x8_bf16 v[\\sn:\\sn+15], v[\\xk2:\\xk2+1], v[\\q2:\\q2+1], v[\\sn:\\sn+15]\n .endif\n"
    " AT_E \\sc+8\n AT_C \\pc+2, \\sc+4\n AT_E \\sc+10\n AT_C \\pc+3, \\sc+6\n"
    " v_mfma_f32_32x32x16_bf16 v[\\o:\\o+15], v[\\x+8:\\x+11], v[\\pp+4:\\pp+7], v[\\o:\\o+15]\n"
    " .if \\mode & 1\n ds_read_b64 v[\\yk2:\\yk2+1], v129 offset:\\off\n ds_read_b128 v[\\y+8:\\y+11], v130 offset:\\off+704\n .endif\n"
    " .if \\mode & 2\n s_waitcnt lgkmcnt(0)\n .endif\n"
    " .if \\mode & 4\n s_mov_b64 exec, 1\n ds_add_u32 v131, v132 offset:\\off+60\n s_mov_b64 exec, -1\n v_cmp_gt_u32 vcc, 4, v133\n .endif\n"
    " AT_E \\sc+12\n AT_C \\pc+4, \\sc+8\n AT_E \\sc+14\n AT_C \\pc+5, \\sc+10\n"
    ".endm\n"
    ".macro AT_A x, xk2, y, yk2, t\n AT_H 56, 40, 80, 72, 8, \\x, \\xk2, 116, 124, \\y, \\yk2, (\\t)*2688, 1\n.endm\n"
    ".macro AT_B x, xk2, off, mode\n AT_H 40, 56, 72, 80, 24, \\x, \\xk2, 120, 126, 0, 0, \\off, \\mode\n.endm\n"
    ".macro AT_2 t\n AT_A 104, 102, 88, 100, \\t\n AT_B 104, 102, 0, 2\n AT_A 88, 100, 104, 102, \\t+1\n AT_B 88, 100, 0, 2\n.endm\n"
    ".macro AT_SPIN off, back\n"
    "1: s_cmp_eq_u32 s41, 0\n s_cbranch_scc1 \\back\n s_sub_u32 s41, s41, 1\n s_sleep 1\n ds_read_b32 v133, v131 offset:\\off\n s_waitcnt lgkmcnt(0)\n v_cmp_gt_u32 vcc, 4, v133\n s_nop 4\n s_cbranch_vccnz 1b\n s_branch \\back\n"
    ".endm\n"
    " s_nop 1\n"
    " v_mfma_f32_32x32x16_bf16 v[40:55], v[88:91], v[116:119], 0\n"
    " v_mfma_f32_32x32x8_bf16 v[40:55], v[100:101], v[124:125], v[40:55]\n"
    " v_mov_b32 v132, 1\n"
    " .irp r, 8,9,10,11,12,13,14,15,16,17,18,19,20,21,22,23,24,25,26,27,28,29,30,31,32,33,34,35,36,37,38,39,68,69,70,71,80,81,82,83,84,85,92,93,94,95,96,97,98,99\n v_mov_b32 v[\\r], 0\n .endr\n"
    " s_mov_b32 s40, 4\n s_mov_b32 s41, 0x400000\n"
    ".Lat_w0: s_sub_u32 s41, s41, 1\n s_cbranch_scc1 .Lat_w1\n ds_read_b32 v133, v131\n s_waitcnt lgkmcnt(0)\n v_cmp_gt_u32 vcc, 4, v133\n s_nop 4\n s_cbranch_vccnz .Lat_w0\n"
    ".Lat_w1: s_mov_b32 s41, 0x400000\n"
    " ds_read_b128 v[108:111], v130\n ds_read_b128 v[104:107], v128\n ds_read_b64 v[102:103], v129\n ds_read_b128 v[112:115], v130 offset:704\n"
    " s_branch .Lat_mid\n"
    ".Lat_s1: AT_SPIN 4, .Lat_b1\n"
    ".Lat_s2: AT_SPIN 8, .Lat_b2\n"
    " .p2align 6\n"
    ".Lat_loop:\n"
    " AT_A 88, 100, 104, 102, 0\n"
    ".Lat_mid:\n"
    " AT_B 88, 100, 0, 2\n"
    " AT_2 1\n AT_2 3\n AT_2 5\n"
    " AT_A 104, 102, 88, 100, 7\n AT_B 104, 102, 4, 6\n"
    " s_cbranch_vccnz .Lat_s1\n"
    ".Lat_b1:\n"
    " AT_A 88, 100, 104, 102, 8\n AT_B 88, 100, 0, 2\n"
    " AT_2 9\n AT_2 11\n AT_2 13\n"
    " AT_A 104, 102, 88, 100, 15\n AT_B 104, 102, 8, 6\n"
    " s_cbranch_vccnz .Lat_s2\n"
    ".Lat_b2:\n"
    " v_add_u32 v131, 8, v131\n"
    " s_sub_u32 s40, s40, 1\n s_cmp_lg_u32 s40, 0\n s_cbranch_scc1 .Lat_loop\n"
    " AT_H 56, 40, 80, 72, 8, 88, 100, 116, 124, 0, 0, 0, 16\n"
    " AT_C 86, 68\n AT_C 87, 70\n"
    " v_mfma_f32_32x32x16_bf16 v[24:39], v[92:95], v[80:83], v[24:39]\n s_nop 0\n"
    " v_mfma_f32_32x32x16_bf16 v[24:39], v[96:99], v[84:87], v[24:39]\n"
    " s_nop 12\n"
    : "=&{v[8:23]}"(oa), "=&{v[24:39]}"(ob), "+{v131}"(ctl), "+{v[88:91]}"(k1), "+{v[100:101]}"(k2)
    : "{v[116:119]}"(qa1), "{v[120:123]}"(qb1), "{v[124:125]}"(qa2), "{v[126:127]}"(qb2),
      "{v128}"(lds0 + lane * 16), "{v129}"(lds0 + k2off), "{v130}"(lds0 + voff)
    : "memory", "vcc", "scc", "s40", "s41", "v40","v41","v42","v43","v44","v45","v46","v47","v48","v49","v50","v51","v52","v53","v54","v55","v56","v57","v58","v59","v60","v61","v62","v63",
      "v64","v65","v66","v67","v68","v69","v70","v71","v72","v73","v74","v75","v76","v77","v78","v79","v80","v81","v82","v83","v84","v85","v86","v87","v92","v93","v94","v95","v96","v97","v98","v99",
      "v102","v103","v104","v105","v106","v107","v108","v109","v110","v111","v112","v113","v114","v115","v132","v133");
  STAMPT(1, 2);
  const rsrc_t ro = mkrsrc(ws + WS_O, S * D * 2);
  auto put = [&](const f32x16& o, int u) {
    const float il = __builtin_amdgcn_rcpf(hh ? o[8] : o[12]);
#pragma unroll
    for (int g = 0; g < 3; ++g) st8s(ro, ooff[g], u * 40960, u32x2{pk2(o[4 * g] * il, o[4 * g + 1] * il), pk2(o[4 * g + 2] * il, o[4 * g + 3] * il)});
  };
  put(oa, 0); put(ob, 1);
  const bool fa = attn_unsafe(oa, hh), fb = attn_unsafe(ob, hh);
  if (fa) { WAITV(0); put(attn_online(kv, qa1, qa2, lane, k2off, voff), 0); }
  if (fb) { WAITV(0); put(attn_online(kv, qb1, qb2, lane, k2off, voff), 1); }
  STAMPT(1, 3);
  WAITV(0);
  if (lane == 0) signal(ws, FL_O + (qc * 4 + wid) * 32 + head, tag);
}

template <int NC> struct GS { f32x4 acc[2][NC]; };
template <int NC, int NKS, int PD, int SSTRIDE, int RING, bool BARS, class Mid> DI void gemm_run(GS<NC>& g, const LAS char* wl, rsrc_t ra, unsigned va0, unsigned va1, unsigned astep, int rot, Mid mid) {
  bf16x8 af[PD][2], bf[2][NC];
  auto soff = [&](int s) { int k = s + rot; if (k >= NKS) k -= NKS; return (unsigned)k * astep; };
#pragma unroll
  for (int s = 0; s < PD; ++s) { const unsigned so = soff(s); af[s][0] = ldfrag(ra, va0, so); af[s][1] = ldfrag(ra, va1, so); }
  SBAR();
  const LAS char* wb[3] = {wl, wl + 61440, wl + 122880};
  asm volatile("" : "+v"(wb[0]), "+v"(wb[1]), "+v"(wb[2]));
  auto rdw = [&](int off) { return *(const LAS bf16x8*)(wb[off / 61440] + off % 61440); };
  mid(-1);
#pragma unroll
  for (int j = 0; j < NC; ++j) bf[0][j] = rdw(j * 1024);
#pragma unroll
  for (int s = 0; s < NKS; ++s) {
    LGKM0();
    if (BARS && (s + 1) % 4 == 0 && s + 1 < NKS) BAR();
    mid(s);
    SBAR();
#pragma unroll
    for (int m = 0; m < 2 * NC; ++m) {
      mfma16_acc(g.acc[m & 1][m >> 1], bf[s & 1][m >> 1], af[s % PD][m & 1]); SBAR();
      if (s + 1 < NKS && m < NC) { bf[(s + 1) & 1][m] = rdw(((s + 1) % RING) * SSTRIDE + m * 1024); SBAR(); }
      if (s >= 1 && s - 1 + PD < NKS && (m == NC + 1 || m == NC + 3)) { af[(s - 1) % PD][m == NC + 3] = ldfrag(ra, m == NC + 3 ? va1 : va0, soff(s - 1 + PD)); SBAR(); }
    }
  }
  asm volatile("s_nop 7" ::: "memory"); SBAR();
}
template <int NC> DI void pair_reduce(GS<NC>& g, char* smem, int wid) {
  const int lane = threadIdx.x & 63;
  LAS f32x4* red = (LAS f32x4*)(smem + 143360) + lane;
#pragma unroll
  for (int j = 0; j < NC; ++j) red[(wid * NC + j) * 64] = g.acc[1][j];
  LGKM0(); BAR();
#pragma unroll
  for (int j = 0; j < NC; ++j) g.acc[0][j] += red[((wid ^ 2) * NC + j) * 64];
}
DI f32x4 unpack4(u32x2 v) { return f32x4{bflo(v[0]), bfhi(v[0]), bflo(v[1]), bfhi(v[1])}; }
DI u32x2 pack4(f32x4 v) { return u32x2{pk2(v[0], v[1]), pk2(v[2], v[3])}; }
DI unsigned piece_off(int rt, int gct, int nks, int lane) { return (rt * nks + (gct >> 1)) * 1024 + lane * 16 + (gct & 1) * 8; }
template <int NC> DI void store_tiles(rsrc_t r, int rt, int gct0, int nct, int nks, int lane, const u32x2 (&v)[NC]) {
  if (gct0 & 1) {
    st8(r, piece_off(rt, gct0, nks, lane), v[0]);
#pragma unroll
    for (int j = 1; j + 1 < NC; j += 2) {
      if (j + 1 < nct) st16(r, piece_off(rt, gct0 + j, nks, lane), u32x4{v[j][0], v[j][1], v[j + 1][0], v[j + 1][1]});
      else st8(r, piece_off(rt, gct0 + j, nks, lane), v[j]);
    }
  } else {
#pragma unroll
    for (int j = 0; j + 1 < NC; j += 2) st16(r, piece_off(rt, gct0 + j, nks, lane), u32x4{v[j][0], v[j][1], v[j + 1][0], v[j + 1][1]});
    if (nct == NC) st8(r, piece_off(rt, gct0 + NC - 1, nks, lane), v[NC - 1]);
  }
}

#define P4_PD 8
#define P5_PD 8
#define ROT3(c) 0
#define ROT4(c4) 0
#define ROT5(c) 0
#define PACE 1
constexpr int LDS_WO = 61440, LDS_CB = 162816, LDS_BYTES = 163840;
constexpr int CODE_BYTES = 45056;

DI void post_compute(const Args& a, char* smem, int wid, u64 tag, u64 t_start) {
  char* ws = a.ws;
  const int lane = threadIdx.x & 63, b = blockIdx.x;
  const int grp = (b & 7) * 4 + ((b >> 3) & 3), c = b >> 5, rh = wid & 1, kh = wid >> 1;
  const int rt = grp * 4 + rh * 2 + kh, tok = rt * 16 + (lane & 15);
  u32x2 hv[5];
  {
    GS<5> g;
#pragma unroll
    for (int j = 0; j < 5; ++j) { g.acc[0][j] = unpack4(*(const u32x2*)(a.x + (size_t)tok * D + c * 80 + j * 16 + (lane >> 4) * 4)); g.acc[1][j] = f32x4{}; }
#pragma unroll
    for (int j = 0; j < 5; ++j) asm volatile("" : "+v"(g.acc[0][j]), "+v"(g.acc[1][j]));
    const rsrc_t ra = mkrsrc(ws + WS_O, S * D * 2);
    const unsigned va0 = (rt * 20 + kh) * 1024 + lane * 16, va1 = ((rt ^ 1) * 20 + kh) * 1024 + lane * 16;
    await(ws, FL_O + grp * 32 + (lane & 31), tag);
    STAMPT(2, 0);
    gemm_run<5, 10, 10, 10240, 10, false>(g, (const LAS char*)smem + LDS_WO + kh * 5120 + lane * 16, ra, va0, va1, 2048, ROT3(c),
                                          [&](int s) { if (s == 9) BAR(); });
    STAMPT(2, 1);
    pair_reduce<5>(g, smem, wid);
#pragma unroll
    for (int j = 0; j < 5; ++j) hv[j] = pack4(g.acc[0][j]);
    store_tiles<5>(mkrsrc(ws + WS_H, S * D * 2), rt, c * 5, 5, 20, lane, hv);
    f32x4 s1 = g.acc[0][0], s2 = g.acc[0][0] * g.acc[0][0];
#pragma unroll
    for (int j = 1; j < 5; ++j) { s1 += g.acc[0][j]; s2 += g.acc[0][j] * g.acc[0][j]; }
    const float t1 = xq_sum((s1[0] + s1[1]) + (s1[2] + s1[3])), t2 = xq_sum((s2[0] + s2[1]) + (s2[2] + s2[3]));
    st8(mkrsrc(ws + WS_ST, S * 64), lane < 16 ? tok * 64 + c * 8 : S * 64, u32x2{__float_as_uint(t1), __float_as_uint(t2)});
    STAMPT(2, 2);
    WAITV(0);
    if (lane == 0) signal(ws, FL_H + rt * 8 + c, tag);
    STAMPT(2, 3);
  }
  {
    const int g4 = (b & 7) * 2 + ((b >> 4) & 1), c4 = ((b >> 5) << 1) | ((b >> 3) & 1);
    const int gct0 = (c4 * 27) >> 2, nct = (((c4 + 1) * 27) >> 2) - gct0;
    GS<7> g;
#pragma unroll
    for (int j = 0; j < 7; ++j) { g.acc[0][j] = f32x4{}; g.acc[1][j] = f32x4{}; asm volatile("" : "+v"(g.acc[0][j]), "+v"(g.acc[1][j])); }
    const rsrc_t ra = mkrsrc(ws + WS_H, S * D * 2);
    const int rt0 = g4 * 8 + wid * 2;
    const unsigned va0 = rt0 * 20480 + lane * 16;
    PADT(4, 70);
    await(ws, FL_H + rt0 * 8 + (lane & 15), tag);
    STAMPT(3, 0);
    u32x4 ps[2]; f32x4 cs[7], bv[7];
    const LAS f32x4* cb = (const LAS f32x4*)(smem + LDS_CB) + (lane >> 4);
    gemm_run<7, 20, P4_PD, 7168, 20, false>(g, (const LAS char*)smem + lane * 16, ra, va0, va0 + 20480, 1024, ROT4(c4), [&](int s) {
      if (s == -1) {
#pragma unroll
        for (int i = 0; i < 2; ++i) ps[i] = __builtin_amdgcn_raw_buffer_load_b128(mkrsrc(ws + WS_ST, S * 64), ((rt0 + i) * 16 + (lane & 15)) * 64 + (lane >> 4) * 16, 0, XAUX);
        BAR();
      }
      if (s == 0) BAR();
      if (s == 18) {
#pragma unroll
        for (int j = 0; j < 7; ++j) { cs[j] = cb[j * 4]; bv[j] = cb[28 + j * 4]; }
      }
      if (s == 19) BAR();
    });
    STAMPT(3, 1);
    const rsrc_t ro = mkrsrc(ws + WS_H1, S * F * 2);
#pragma unroll
    for (int i = 0; i < 2; ++i) {
      const float m = xq_sum(__uint_as_float(ps[i][0]) + __uint_as_float(ps[i][2])) * (1.f / D);
      const float var = fmaxf(xq_sum(__uint_as_float(ps[i][1]) + __uint_as_float(ps[i][3])) * (1.f / D) - m * m, 0.f);
      const float r1 = __builtin_amdgcn_rsqf(var + 1e-5f);
      const float rstd = r1 * __builtin_amdgcn_rsqf(var * r1 * r1 + 1e-5f);
      const float mr = -m * rstd;
      f32x4 v[7], t[7];
#pragma unroll
      for (int j = 0; j < 7; ++j) t[j] = cs[j] * mr + bv[j];
      SBAR();
#pragma unroll
      for (int j = 0; j < 7; ++j) v[j] = g.acc[i][j] * rstd + t[j];
      SBAR();
#pragma unroll
      for (int j = 0; j < 7; ++j) t[j] = v[j] * v[j];
      SBAR();
#pragma unroll
      for (int j = 0; j < 7; ++j) t[j] = t[j] * -0.1029432397f + -2.302208198f;
      SBAR();
#pragma unroll
      for (int j = 0; j < 7; ++j) t[j] = t[j] * v[j];
      SBAR();
#pragma unroll
      for (int j = 0; j < 7; ++j) t[j] = f32x4{EX(t[j][0]), EX(t[j][1]), EX(t[j][2]), EX(t[j][3])};
      SBAR();
#pragma unroll
      for (int j = 0; j < 7; ++j) t[j] = t[j] + 1.f;
      SBAR();
#pragma unroll
      for (int j = 0; j < 7; ++j) t[j] = f32x4{__builtin_amdgcn_rcpf(t[j][0]), __builtin_amdgcn_rcpf(t[j][1]), __builtin_amdgcn_rcpf(t[j][2]), __builtin_amdgcn_rcpf(t[j][3])};
      SBAR();
#pragma unroll
      for (int j = 0; j < 7; ++j) t[j] = t[j] * v[j];
      SBAR();
      u32x2 h1[7];
#pragma unroll
      for (int j = 0; j < 7; ++j) h1[j] = pack4(t[j]);
      store_tiles<7>(ro, rt0 + i, gct0, nct, 54, lane, h1);
      SBAR();
    }
    STAMPT(3, 2);
    WAITV(0);
    if (lane == 0) signal(ws, FL_H1 + (rt0 >> 1) * 16 + c4, tag);
    STAMPT(3, 3);
  }
  {
    GS<5> g;
#pragma unroll
    for (int j = 0; j < 5; ++j) { g.acc[0][j] = unpack4(*(const u32x2*)(a.b2 + c * 80 + j * 16 + (lane >> 4) * 4)) + unpack4(hv[j]); g.acc[1][j] = f32x4{}; }
#pragma unroll
    for (int j = 0; j < 5; ++j) asm volatile("" : "+v"(g.acc[0][j]), "+v"(g.acc[1][j]));
    const rsrc_t ra = mkrsrc(ws + WS_H1, S * F * 2);
    const unsigned va0 = (rt * 54 + kh) * 1024 + lane * 16, va1 = ((rt ^ 1) * 54 + kh) * 1024 + lane * 16;
    PADT(5, 80);
    await(ws, FL_H1 + (grp * 2 + rh) * 16 + (lane & 15), tag);
    STAMPT(4, 0);
    gemm_run<5, 27, P5_PD, 10240, 16, true>(g, (const LAS char*)smem + kh * 5120 + lane * 16, ra, va0, va1, 2048, ROT5(c), [&](int s) { if (s == -1) BAR(); });
    STAMPT(4, 1);
    pair_reduce<5>(g, smem, wid);
#pragma unroll
    for (int j = 0; j < 5; ++j) *(u32x2*)(a.out + (size_t)tok * D + c * 80 + j * 16 + (lane >> 4) * 4) = pack4(g.acc[0][j]);
    STAMPT(4, 2);
  }
}

DI void main_helper(const Args& a, char* smem, int head, int hw, u64 tag) {
  char* ws = a.ws;
  const int lane = threadIdx.x & 63, b = blockIdx.x;
  const char* kv = ws + WS_KV + (size_t)head * 64 * KVT;
  const unsigned lds0 = (unsigned)(uintptr_t)smem, v16 = lane * 16;
  auto issue = [&](int c) {
#pragma unroll
    for (int i = 0; i < 6; ++i) { const int pc = hw + 4 * i; if (pc < CH_PIECES) glds16x(v16, kv + (size_t)c * CH_BYTES + pc * 1024, lds0 + (c & 1) * CH_BYTES + pc * 1024); }
  };
  const unsigned ctl = lds0 + CTL2;
  auto refill = [&](int c) { lds_wait<NAP2>((const LAS char*)smem + CTL2 + 64 + (c - 2) * 4); STAMPH(6, c); issue(c); WAITV(0); lds_post(ctl + c * 4, 1); STAMPH(5, c); };
  __builtin_amdgcn_s_setprio(3);
  issue(0); issue(1);
  WAITV(5); lds_post(ctl, 1); STAMPH(5, 0);
  WAITV(0); lds_post(ctl + 4, 1); STAMPH(5, 1);
  const int c = b >> 5, c4 = ((b >> 5) << 1) | ((b >> 3) & 1), gct0 = (c4 * 27) >> 2;
  retile_store(ws, (const LAS char*)smem + LDS_RT, hw);
  WAITV(0);
  if (lane == 0 && __hip_atomic_fetch_add((LAS unsigned*)(smem + CTL2 + 60), 1u, __ATOMIC_RELAXED, __HIP_MEMORY_SCOPE_WORKGROUP) == 3u) signal(ws, FL_W + b, tag);
  refill(2);
  refill(3);
  {
    gu64* p = flagp(ws, FL_W + lane);
#pragma unroll 1
    for (unsigned sp = 0; sp < SPIN_CAP; ++sp) {
      const u64 f0 = __hip_atomic_load(p, RLX_AGENT), f1 = __hip_atomic_load(p + 64, RLX_AGENT), f2 = __hip_atomic_load(p + 128, RLX_AGENT), f3 = __hip_atomic_load(p + 192, RLX_AGENT);
      if (__all(f0 == tag && f1 == tag && f2 == tag && f3 == tag)) break;
      __builtin_amdgcn_s_sleep(4);
    }
    asm volatile("" ::: "memory");
  }
  if (b == 0 && hw == 0 && lane == 0) signal(ws, FL_EPOCH, tag);
  f32x4 cst;
  {
    const int n = gct0 * 16 + (lane < 28 ? lane : (lane < 56 ? lane - 28 : 0)) * 4;
    const rsrc_t rc = mkrsrc(ws + WS_CS1, 4 * 1792 * 4);
    f32x4 t[4];
#pragma unroll
    for (int k = 0; k < 4; ++k) t[k] = __builtin_bit_cast(f32x4, __builtin_amdgcn_raw_buffer_load_b128(rc, n * 4, k * 7168, XAUX));
    const f32x4 bv = unpack4(*(const u32x2*)(a.b1 + n));
    cst = lane < 28 ? (t[0] + t[1]) + (t[2] + t[3]) : bv;
    asm volatile("" : "+v"(cst));
  }
  const char* wo = ws + WS_WO + (size_t)c * 102400;
  auto wo_dma = [&](int i0, int i1) {
#pragma unroll 1
    for (int i = i0; i < i1; ++i) { const int p = i * 4 + hw; int q = p + ROT3(c) * 10; if (q >= 100) q -= 100; glds16x(v16, wo + q * 1024, lds0 + LDS_WO + p * 1024); }
  };
  bf16x8 wa[35], wb[20];
  const rsrc_t r1 = mkrsrc(ws + WS_W1 + (size_t)gct0 * 20480, 7 * 20480), r2 = mkrsrc(ws + WS_W2 + (size_t)c * 276480, 276480);
  auto ld1 = [&](int i) { int k = (i / 7) * 4 + hw + ROT4(c4); if (k >= 20) k -= 20; return ldfrag(r1, v16, ((i % 7) * 20 + k) * 1024); };
  auto ld2 = [&](int i) { int p = i * 4 + hw + ROT5(c) * 10; if (p >= 270) p -= 270; return ldfrag(r2, v16, p * 1024); };
  refill(4);
  wo_dma(0, 25);
  refill(5);
  lds_post(ctl + 32, 1);
#pragma unroll
  for (int i = 0; i < 20; ++i) wa[i] = ld1(i);
  SBAR();
  refill(6);
#pragma unroll
  for (int i = 20; i < 35; ++i) wa[i] = ld1(i);
  SBAR();
  refill(7);
#pragma unroll
  for (int i = 0; i < 20; ++i) wb[i] = ld2(i);
  SBAR();
  lds_wait<NAP2>((const LAS char*)smem + CTL2 + 64 + 28);
  __builtin_amdgcn_s_setprio(0);
  LAS bf16x8* lw = (LAS bf16x8*)smem + hw * 7 * 64 + lane;
#pragma unroll
  for (int i = 0; i < 14; ++i) lw[((i / 7) * 28 + i % 7) * 64] = wa[i];
  BAR();
#pragma unroll
  for (int i = 14; i < 21; ++i) lw[((i / 7) * 28 + i % 7) * 64] = wa[i];
  BAR();
#pragma unroll
  for (int i = 21; i < 35; ++i) lw[((i / 7) * 28 + i % 7) * 64] = wa[i];
  LGKM0();
  BAR();
  if (hw == 0 && lane < 56) *((LAS f32x4*)(smem + LDS_CB) + lane) = cst;
  LGKM0(); BAR();
#pragma unroll
  for (int i = 0; i < 35; ++i) { wa[i] = ld2(20 + i); SBAR(); __builtin_amdgcn_s_sleep(PACE); SBAR(); }
  BAR();
  LAS bf16x8* l2 = (LAS bf16x8*)smem + hw * 64 + lane;
#pragma unroll
  for (int i = 0; i < 20; ++i) l2[i * 256] = wb[i];
  LGKM0(); SBAR();
#pragma unroll
  for (int i = 0; i < 13; ++i) wb[i] = ld2(55 + i);
  SBAR();
#pragma unroll
  for (int i = 0; i < 20; ++i) l2[(20 + i) * 256] = wa[i];
  LGKM0(); BAR();
  BAR();
#pragma unroll
  for (int i = 0; i < 10; ++i) l2[i * 256] = wa[20 + i];
  BAR();
#pragma unroll
  for (int i = 0; i < 10; ++i) l2[(10 + i) * 256] = i < 5 ? wa[30 + i] : wb[i - 5];
  BAR();
#pragma unroll
  for (int i = 0; i < 8; ++i) l2[(20 + i) * 256] = wb[5 + i];
  LGKM0(); BAR();
  BAR(); BAR();
  BAR();
}

__global__ __launch_bounds__(512, 2) void fused_block(Args a) {
  extern __shared__ __attribute__((aligned(16))) char smem[];
  char* ws = a.ws;
  const int wid = __builtin_amdgcn_readfirstlane(threadIdx.x >> 6), lane = threadIdx.x & 63;
  const int b = blockIdx.x, xg = b & 7, j = b >> 3;
  STAMPT(0, 0);
  const u64 t_start = PADAT >= 0 ? __builtin_amdgcn_s_memrealtime() : 0;
  asm volatile("" :: "s"(a.x), "s"(a.wqkv), "s"(a.ws));
  const int bm = (xg >> 1) * 4 + (j & 3), bn = (xg & 1) * 8 + (j >> 2);
#define K1_ROT (j % 10)
  const int head = (b & 7) * 4 + ((b >> 3) & 3), qc = b >> 5;
  u64 tag = 0;
  if (bn >= 15) {
    tag = get_tag(ws);
    if (wid < 4) { XRow xr; xrow_load(xr, a.x, b * 8 + wid * 2); xrow_publish(xr, ws, b * 8 + wid * 2, tag); }
    k1_colsum(a.x, a.wqkv, ws, smem, bm, wid, tag);
    if (wid >= 4) retile_dma(a, (unsigned)(uintptr_t)smem + LDS_RT, wid - 4);
  }
  if (wid >= 4) {
    if (bn < 15) k1_helper(a, a.x, a.wqkv, ws, smem, bm, bn, K1_ROT, wid - 4, tag);
    WAITV(0); BAR(); BAR();
    main_helper(a, smem, head, wid - 4, tag);
  } else {
    if (bn < 15) {
      XRow xr;
      xrow_load(xr, a.x, b * 8 + wid * 2);
      {
        const char* pc = (const char*)(__builtin_amdgcn_s_getpc() & ~127ll);
        const unsigned off = min((((j & 1) * 4 + wid) * 64 + lane) * 128, CODE_BYTES);
        asm volatile("s_mov_b32 m0, %2\n\ts_nop 0\n\tglobal_load_lds_dword %0, %1" :: "v"(off), "s"(pc), "s"(__builtin_amdgcn_readfirstlane((unsigned)(uintptr_t)smem + 140000 + wid * 256)) : "memory");
      }
      tag = get_tag(ws);
      if (bn < 10) k1_compute<false>(ws, smem, bm, bn, wid, xr, tag, t_start); else k1_compute<true>(ws, smem, bm, bn, wid, xr, tag, t_start);
    }
    STAMPT(0, 3);
    if (wid == 3 && lane < 32) { ((LAS unsigned*)(smem + CTL2))[lane] = 0; LGKM0(); }
    WAITV(0); BAR();
    if (threadIdx.x == 0) signal(ws, FL_QKV + bn * 16 + bm, tag);
    STAMPT(0, 4);
    const int t0 = (head * 20) >> 7, t1 = (head * 20 + 19) >> 7;
    if (wid == 1) await(ws, FL_QKV + (5 + (lane >> 5) * 5 + ((lane & 16) ? t1 : t0)) * 16 + (lane & 15), tag);
    if (wid == 2) await(ws, FL_QKV + ((lane & 2) ? t1 : t0) * 16 + qc * 2 + (lane & 1), tag);
    PADT(1, 30);
    BAR();
    STAMPT(1, 0);
    attn_compute(ws, smem, head, qc, wid, tag); STAMPT(1, 4); PADT(2, 60); post_compute(a, smem, wid, tag, t_start); PADT(0, 100);
  }
}

extern "C" void kernel_launch(void* const* d_in, const int* in_sizes, int n_in, void* d_out, int out_size, void* d_ws, size_t ws_size, hipStream_t stream) {
  Args a;
  a.x = (const u16*)d_in[0]; a.wqkv = (const u16*)d_in[1]; a.wo = (const u16*)d_in[2];
  a.w1 = (const u16*)d_in[4]; a.b1 = (const u16*)d_in[5]; a.w2 = (const u16*)d_in[6]; a.b2 = (const u16*)d_in[7];
  a.out = (u16*)d_out; a.ws = (char*)d_ws;
  (void)hipFuncSetAttribute((const void*)fused_block, hipFuncAttributeMaxDynamicSharedMemorySize, LDS_BYTES);
  fused_block<<<256, 512, LDS_BYTES, stream>>>(a);
}
```

```cpp
#define TOUCH 1
#include <hip/hip_runtime.h>
#include <hip/hip_bf16.h>
typedef unsigned short u16;
typedef __attribute__((ext_vector_type(8))) short bf16x8;
typedef __attribute__((ext_vector_type(8))) __bf16 bf8;
typedef __attribute__((ext_vector_type(4))) short s16x4;
typedef __attribute__((ext_vector_type(16))) float f32x16;
typedef __attribute__((ext_vector_type(4))) float f32x4;
typedef __attribute__((ext_vector_type(2))) float f32x2;
typedef __attribute__((ext_vector_type(4))) unsigned u32x4;
typedef __attribute__((ext_vector_type(2))) unsigned u32x2;
typedef __attribute__((ext_vector_type(2))) __bf16 bf2;
#define LAS __attribute__((address_space(3)))
#define DI __device__ __forceinline__
#define SBAR() __builtin_amdgcn_sched_barrier(0)
#define LGKM0() __builtin_amdgcn_s_waitcnt(0xC07F)
#define BAR() asm volatile("s_barrier" ::: "memory")
#define WAITV(n) asm volatile("s_waitcnt vmcnt(%0)" :: "i"(n) : "memory")

constexpr int S = 2048, D = 640, H = 32, DH = 20, F = 1728;
constexpr int QT = 1280, KVT = 2688;
constexpr size_t WS_Q = 0, WS_KV = WS_Q + (size_t)H * 64 * QT, WS_O = WS_KV + (size_t)H * 64 * KVT,
                 WS_H = WS_O + (size_t)S * D * 2, WS_H1 = WS_H + (size_t)S * D * 2,
                 WS_WO = WS_H1 + (size_t)S * F * 2, WS_W1 = WS_WO + (size_t)D * D * 2, WS_W2 = WS_W1 + (size_t)D * F * 2 + 20480,
                 WS_CS1 = WS_W2 + (size_t)D * F * 2, WS_ST = WS_CS1 + 4 * 1792 * 4, WS_XST = WS_ST + (size_t)S * 8 * 8, WS_CSQ = WS_XST + (size_t)S * 16, WS_FL = WS_CSQ + 16384, WS_END = WS_FL + 65536;
#define STAMPT(k, s) do {} while (0)
#define STAMPH(k, s) do {} while (0)
#define PADAT -1
#define PADT(k, us) do { if (PADAT == (k)) { while (__builtin_amdgcn_s_memrealtime() - t_start < (us) * 100ull) __builtin_amdgcn_s_sleep(2); } } while (0)
constexpr int KSKEW = 1;
constexpr float QSCALE = 0.22360679774997896f * 1.4426950408889634f;

DI float bf2f(u16 v) { return __uint_as_float((unsigned)v << 16); }
DI float bflo(unsigned u) { return __uint_as_float(u << 16); }
DI float bfhi(unsigned u) { return __uint_as_float(u & 0xffff0000u); }
DI unsigned pk2(float lo, float hi) { f32x2 v = {lo, hi}; return __builtin_bit_cast(unsigned, __builtin_convertvector(v, bf2)); }
DI void glds16(unsigned voff, const void* sbase, unsigned lds) {
  asm volatile("s_mov_b32 m0, %2\n\ts_nop 0\n\tglobal_load_lds_dwordx4 %0, %1" :: "v"(voff), "s"(sbase), "s"(__builtin_amdgcn_readfirstlane(lds)) : "memory"); }
DI void glds16x(unsigned voff, const void* sbase, unsigned lds) {
  asm volatile("s_mov_b32 m0, %2\n\ts_nop 0\n\tglobal_load_lds_dwordx4 %0, %1 sc1" :: "v"(voff), "s"(sbase), "s"(__builtin_amdgcn_readfirstlane(lds)) : "memory"); }
DI s16x4 trread(const LAS char* p) { return __builtin_amdgcn_ds_read_tr16_b64_v4i16((LAS s16x4*)p); }
DI bf16x8 cat8(s16x4 a, s16x4 b) { return __builtin_shufflevector(a, b, 0, 1, 2, 3, 4, 5, 6, 7); }
#define MFMA32(a, b, c) __builtin_amdgcn_mfma_f32_32x32x16_bf16(__builtin_bit_cast(bf8, a), __builtin_bit_cast(bf8, b), c, 0, 0, 0)
#define MFMA32K8(a, b, c) __builtin_amdgcn_mfma_f32_32x32x8bf16_1k(a, b, c, 0, 0, 0)
#define MFMA16(a, b, c) __builtin_amdgcn_mfma_f32_16x16x32_bf16(__builtin_bit_cast(bf8, a), __builtin_bit_cast(bf8, b), c, 0, 0, 0)
DI void mfma16_acc(f32x4& c, bf16x8 a, bf16x8 b) { asm volatile("v_mfma_f32_16x16x32_bf16 %0, %1, %2, %0" : "+v"(c) : "v"(a), "v"(b)); }
DI float xhalf_sum(float v) { auto r = __builtin_amdgcn_permlane32_swap(__float_as_uint(v), __float_as_uint(v), false, false); return __uint_as_float(r[0]) + __uint_as_float(r[1]); }
DI float xhalf_max(float v) { auto r = __builtin_amdgcn_permlane32_swap(__float_as_uint(v), __float_as_uint(v), false, false); return fmaxf(__uint_as_float(r[0]), __uint_as_float(r[1])); }
DI float xq_sum(float v) { auto r = __builtin_amdgcn_permlane16_swap(__float_as_uint(v), __float_as_uint(v), false, false); return xhalf_sum(__uint_as_float(r[0]) + __uint_as_float(r[1])); }
typedef __amdgpu_buffer_rsrc_t rsrc_t;
DI rsrc_t mkrsrc(const void* p, unsigned bytes) { return __builtin_amdgcn_make_buffer_rsrc((void*)p, 0, bytes, 0x00020000); }
constexpr int XAUX = 16;
DI bf16x8 ldfrag(rsrc_t r, unsigned voff, unsigned soff) { return __builtin_bit_cast(bf16x8, __builtin_amdgcn_raw_buffer_load_b128(r, voff, soff, XAUX)); }
DI void st16(rsrc_t r, unsigned voff, u32x4 v) { __builtin_amdgcn_raw_buffer_store_b128(v, r, voff, 0, XAUX); }
DI void st8(rsrc_t r, unsigned voff, u32x2 v) { __builtin_amdgcn_raw_buffer_store_b64(v, r, voff, 0, XAUX); }

DI s16x4 ldhalf(rsrc_t r, unsigned voff, unsigned soff) { return __builtin_bit_cast(s16x4, __builtin_amdgcn_raw_buffer_load_b64(r, voff, soff, XAUX)); }
DI void st16s(rsrc_t r, unsigned voff, unsigned soff, u32x4 v) { __builtin_amdgcn_raw_buffer_store_b128(v, r, voff + soff, 0, XAUX); }
DI void st8s(rsrc_t r, unsigned voff, unsigned soff, u32x2 v) { __builtin_amdgcn_raw_buffer_store_b64(v, r, voff, soff, XAUX); }
typedef unsigned long long u64;
typedef __attribute__((address_space(1))) u64 gu64;
#define RLX_AGENT __ATOMIC_RELAXED, __HIP_MEMORY_SCOPE_AGENT
constexpr int FL_EPOCH = 0, FL_QKV = 64  , FL_W = FL_QKV + 256  , FL_O = FL_W + 256  ,
              FL_H = FL_O + 1024  , FL_H1 = FL_H + 1024  ;
constexpr unsigned SPIN_CAP = 1u << 17;
DI u64 get_tag(char* ws) {
  u64 e;
  asm volatile("s_load_dwordx2 %0, %1, 0x0\n\ts_waitcnt lgkmcnt(0)" : "=s"(e) : "s"(ws + WS_FL + FL_EPOCH * 8) : "memory");
  return e * 6364136223846793005ull + 1442695040888963407ull;
}
DI gu64* flagp(char* ws, int i) { return (gu64*)(ws + WS_FL) + i; }
DI void signal(char* ws, int i, u64 tag) { __hip_atomic_store(flagp(ws, i), tag, RLX_AGENT); }
DI void await(char* ws, int i  , u64 tag) {
  gu64* p = flagp(ws, i);
#pragma unroll 1
  for (unsigned sp = 0; sp < SPIN_CAP; ++sp) {
    if (__all(__hip_atomic_load(p, RLX_AGENT) == tag)) break;
    __builtin_amdgcn_s_sleep(1);
  }
  asm volatile("" ::: "memory");
}

DI void lds_post(unsigned addr, unsigned one) { asm volatile("s_mov_b64 exec, 1\n\tds_add_u32 %0, %1\n\ts_mov_b64 exec, -1" :: "v"(addr), "v"(one) : "memory"); }
template <int NAP = 1> DI void lds_wait(const LAS char* p) {
#pragma unroll 1
  for (unsigned sp = 0; sp < (1u << 22) && __builtin_amdgcn_readfirstlane(*(const volatile LAS unsigned*)p) < 4u; ++sp) __builtin_amdgcn_s_sleep(NAP);
  asm volatile("" ::: "memory");
}
#define NAP1 3
#define NAP2 12
constexpr int CTL1 = 133120  , CTL2 = 57344  ;

constexpr int K1_NSLOT = 4, K1_STAGE = 32768;
#define K1_PRE 2
template <int CTRL> DI float dpp_add(float v) { return v + __builtin_bit_cast(float, __builtin_amdgcn_update_dpp(0, __builtin_bit_cast(int, v), CTRL, 0xf, 0xf, true)); }
DI float sum32(float v) {
  v = dpp_add<0x128>(v); v = dpp_add<0x124>(v); v = dpp_add<0x122>(v); v = dpp_add<0x121>(v);
  auto r = __builtin_amdgcn_permlane16_swap(__float_as_uint(v), __float_as_uint(v), false, false); return __uint_as_float(r[0]) + __uint_as_float(r[1]);
}
struct XRow { u32x4 a, b; u32x2 c; };
DI void xrow_load(XRow& v, const u16* x, int row0) {
  const int lane = threadIdx.x & 63;
  const char* p = (const char*)x + (size_t)(row0 + (lane >> 5)) * 1280;
  v.a = *(const u32x4*)(p + (lane & 31) * 16); v.b = *(const u32x4*)(p + 512 + (lane & 31) * 16); v.c = *(const u32x2*)(p + 1024 + (lane & 31) * 8);
}
DI void xrow_publish(const XRow& v, char* ws, int row0, u64 tag) {
  const int lane = threadIdx.x & 63;
  const u32x4 va = v.a, vb = v.b; const u32x2 vc = v.c;
  float e[20];
#pragma unroll
  for (int i = 0; i < 4; ++i) { e[2 * i] = bflo(va[i]); e[2 * i + 1] = bfhi(va[i]); e[8 + 2 * i] = bflo(vb[i]); e[9 + 2 * i] = bfhi(vb[i]); }
  e[16] = bflo(vc[0]); e[17] = bfhi(vc[0]); e[18] = bflo(vc[1]); e[19] = bfhi(vc[1]);
  f32x2 s = {0.f, 0.f};
#pragma unroll
  for (int i = 0; i < 20; i += 2) s += f32x2{e[i], e[i + 1]};
  const float m = sum32(s[0] + s[1]) * (1.f / D);
  f32x2 q = {0.f, 0.f};
#pragma unroll
  for (int i = 0; i < 20; i += 2) { const f32x2 d = f32x2{e[i], e[i + 1]} - m; q += d * d; }
  const float rstd = __builtin_amdgcn_rsqf(sum32(q[0] + q[1]) * (1.f / D) + 1e-5f);
  if ((lane & 31) == 0) st16(mkrsrc(ws + WS_XST, S * 16), (row0 + (lane >> 5)) * 16, u32x4{__float_as_uint(m), (unsigned)tag, __float_as_uint(rstd), (unsigned)(tag >> 32)});
}
struct F4 { bf16x8 a0, a1, b0, b1; };
template <bool isV> DI void k1_compute(char* ws, char* smem, int bm, int bn, int wid, XRow& xr, u64 tag, u64 t_start) {
  const int lane = threadIdx.x & 63, r = lane & 31, hh = lane >> 5;
  const int wm = wid >> 1, wn = wid & 1;
  f32x16 c00 = f32x16{}, c01 = f32x16{}, c10 = f32x16{}, c11 = f32x16{};
  const int fA = (r >> 1) & 7;
  const int gi = lane >> 4, q = (lane & 15) >> 2, p = lane & 3;
  const LAS char* sm = (const LAS char*)smem;
  const LAS char* pa[2][4]; const LAS char* pb[2][2];
#pragma unroll
  for (int w = 0; w < 2; ++w) {
#pragma unroll
    for (int s = 0; s < 4; ++s) { pa[w][s] = sm + w * 65536 + (wm * 64 + r) * 128 + (((2 * s + hh) ^ fA) << 4); asm volatile("" : "+v"(pa[w][s])); }
#pragma unroll
    for (int jn = 0; jn < 2; ++jn) { pb[w][jn] = sm + w * 65536 + 16384 + (8 * (gi >> 1) + q) * 256 + 32 * (gi & 1) + 8 * p + (((2 * wn + jn) ^ q) << 6); asm volatile("" : "+v"(pb[w][jn])); }
  }
  auto lda = [&](int i, int t) { const int sl = (i >> 2) % K1_NSLOT; return *(const LAS bf16x8*)(pa[sl >> 1][i & 3] + (sl & 1) * K1_STAGE + t * 4096); };
  auto ldb = [&](int i, int jn) { const int sl = (i >> 2) % K1_NSLOT; const LAS char* b = pb[sl >> 1][jn] + (sl & 1) * K1_STAGE + (i & 3) * 4096; return cat8(trread(b), trread(b + 1024)); };
#define K1_MM(c, a, b) asm volatile("v_mfma_f32_32x32x16_bf16 %0, %1, %2, %0" : "+v"(c) : "v"(isV ? a : b), "v"(isV ? b : a))
  const bool isQ = bn < 5; const int tstride = isQ ? QT : KVT;
  const rsrc_t rimg = mkrsrc(ws + (isQ ? WS_Q : WS_KV), H * 64 * KVT);
  unsigned soff[8];
  if (!isV) {
#pragma unroll
    for (int u = 0; u < 8; ++u) {
      const int n = (bn % 5) * 128 + wn * 64 + (u >> 2) * 32 + 8 * (u & 3) + 4 * hh, head = n / 20, d = n - head * 20;
      soff[u] = head * 64 * tstride + (d < 16 ? (d >> 3) * 512 + r * 16 + (d & 7) * 2 : 1024 + r * 8);
      asm volatile("" : "+v"(soff[u]));
    }
  } else {
#pragma unroll
    for (int jn = 0; jn < 2; ++jn) {
      const int n = (bn - 10) * 128 + wn * 64 + jn * 32 + r, head = n / 20, d = n - head * 20;
      soff[jn] = head * 64 * KVT + 1280 + hh * 352 + d * 16;
      {
        const unsigned c1 = d == 18 ? 0x3F803F80u : 0u, o = d >= 18 ? soff[jn] + 32 : H * 64 * KVT;
#pragma unroll
        for (int u = 0; u < 4; ++u) st16s(rimg, o + (u & 1) * 704, (bm * 4 + wm * 2 + (u >> 1)) * KVT, u32x4{c1, c1, c1, c1});
      }
      asm volatile("" : "+v"(soff[jn]));
    }
  }
  asm volatile("" : "+v"(c00), "+v"(c01), "+v"(c10), "+v"(c11));
  SBAR();
  xrow_publish(xr, ws, blockIdx.x * 8 + wid * 2, tag);
  BAR(); SBAR();
  PADT(3, 15);
  STAMPT(0, 1);
  F4 f[3];
#pragma unroll
  for (int i = 0; i < 2; ++i) { f[i].a0 = lda(i, 0); f[i].b0 = ldb(i, 0); f[i].b1 = ldb(i, 1); f[i].a1 = lda(i, 1); }
  SBAR();
#pragma unroll
  for (int i = 0; i < 40; ++i) {
    F4& x = f[i % 3]; F4& y = f[(i + 2) % 3];
    const bool ld = i + 2 < 40;
    if (ld && (i & 3) == 2) { BAR(); SBAR(); }
    K1_MM(c00, x.a0, x.b0); SBAR(); if (ld) { y.a0 = lda(i + 2, 0); y.b0 = ldb(i + 2, 0); } SBAR();
    K1_MM(c01, x.a0, x.b1); SBAR(); if (ld) { y.b1 = ldb(i + 2, 1); } SBAR();
    K1_MM(c10, x.a1, x.b0); SBAR(); if (ld) { y.a1 = lda(i + 2, 1); } SBAR();
    K1_MM(c11, x.a1, x.b1); SBAR();
  }
  asm volatile("s_nop 11" ::: "memory"); SBAR();
  PADT(6, 22);
  STAMPT(0, 2);
  BAR();
  const LAS float* sMean = (const LAS float*)(smem + K1_NSLOT * K1_STAGE); const LAS float* sRstd = sMean + 128; const LAS float* sCs = sMean + 256;
  if (!isV) {
#pragma unroll
    for (int i = 0; i < 2; ++i) {
      const int tl = wm * 64 + i * 32;
      const float m = sMean[tl + r], rstd = sRstd[tl + r] * (isQ ? QSCALE : 1.f);
      int tile = bm * 4 + wm * 2 + i; if (!isQ) tile = (tile + 64 - KSKEW) & 63;
#pragma unroll
      for (int jn = 0; jn < 2; ++jn) {
        const f32x16& c = i ? (jn ? c11 : c10) : (jn ? c01 : c00);
#pragma unroll
        for (int g = 0; g < 4; ++g) {
          const f32x4 c4 = *(const LAS f32x4*)(sCs + wn * 64 + jn * 32 + 8 * g + 4 * hh);
          u32x2 v;
          v[0] = pk2((c[4 * g] - m * c4[0]) * rstd, (c[4 * g + 1] - m * c4[1]) * rstd);
          v[1] = pk2((c[4 * g + 2] - m * c4[2]) * rstd, (c[4 * g + 3] - m * c4[3]) * rstd);
          st8s(rimg, soff[jn * 4 + g], tile * tstride, v);
        }
      }
    }
  } else {
#pragma unroll
    for (int i = 0; i < 2; ++i) {
      const int tile = bm * 4 + wm * 2 + i;
#pragma unroll
      for (int s = 0; s < 2; ++s) {
        f32x4 m4[2], r4[2];
#pragma unroll
        for (int g = 0; g < 2; ++g) { const int tl = wm * 64 + i * 32 + 16 * s + 8 * g + 4 * hh; m4[g] = *(const LAS f32x4*)(sMean + tl); r4[g] = *(const LAS f32x4*)(sRstd + tl); }
#pragma unroll
        for (int jn = 0; jn < 2; ++jn) {
          const f32x16& c = i ? (jn ? c11 : c10) : (jn ? c01 : c00);
          const float cs = sCs[wn * 64 + jn * 32 + r];
          u32x4 v;
#pragma unroll
          for (int g = 0; g < 2; ++g) {
            const int e = 8 * s + 4 * g;
            v[2 * g] = pk2((c[e] - m4[g][0] * cs) * r4[g][0], (c[e + 1] - m4[g][1] * cs) * r4[g][1]);
            v[2 * g + 1] = pk2((c[e + 2] - m4[g][2] * cs) * r4[g][2], (c[e + 3] - m4[g][3] * cs) * r4[g][3]);
          }
          st16s(rimg, soff[jn] + s * 704, tile * KVT, v);
        }
      }
    }
  }
}
struct Args { const u16 *x, *wqkv, *wo, *w1, *b1, *w2, *b2; u16* out; char* ws; };
constexpr int LDS_RT = 143360;
DI void retile_dma(const Args& a, unsigned lds, int hw) {
  const int lane = threadIdx.x & 63, b = blockIdx.x;
  if (b >= 40 && b < 148) {
    const int jb = b - 40;
    const char* src = (const char*)a.w1 + (size_t)((jb & 3) * 160) * (F * 2) + (jb >> 2) * 128;
#pragma unroll
    for (int i = 0; i < 5; ++i) glds16((lane >> 3) * (F * 2) + (lane & 7) * 16, src + (size_t)(hw * 5 + i) * 8 * (F * 2), lds + (hw * 5 + i) * 1024);
  } else {
    const int jb = b < 40 ? b : b - 148;
    const char* src = (const char*)(b < 40 ? a.wo : a.w2) + (size_t)((jb >> 1) * 32 + hw * 8) * (D * 2) + (jb & 1) * 640;
#pragma unroll
    for (int i = 0; i < 5; ++i) { const int idx = i * 64 + lane, row = idx / 40; glds16(row * (D * 2) + (idx - row * 40) * 16, src, lds + (hw * 5 + i) * 1024); }
  }
}
DI void retile_store(char* ws, const LAS char* img, int hw) {
  const int lane = threadIdx.x & 63, b = blockIdx.x;
  const bool is1 = b >= 40 && b < 148;
  const int rs = is1 ? 128 : 640;
  const LAS char* p = img + (lane >> 2) * rs + (lane & 3) * 8 + (is1 ? hw * 32 : hw * 160);
  const rsrc_t rw = mkrsrc(ws + (b < 40 ? WS_WO : (is1 ? WS_W1 : WS_W2)), D * F * 2);
  const int jb = b < 40 ? b : (is1 ? b - 40 : b - 148);
  const int d0 = is1 ? ((jb >> 2) * 4 + hw) * 20 + (jb & 3) * 5 : (((jb & 1) * 4 + hw) * (b < 40 ? 20 : 54) + (jb >> 1)) * 5;
  float cs = 0.f;
#pragma unroll
  for (int k = 0; k < 5; ++k) {
    const LAS char* q = p + k * (is1 ? 4096 : 32);
    const u32x4 v = __builtin_bit_cast(u32x4, cat8(trread(q), trread(q + 16 * rs)));
    st16(rw, (d0 + k) * 1024 + lane * 16, v);
#pragma unroll
    for (int j = 0; j < 4; ++j) { asm volatile("v_add_f32 %0, %0, %1" : "+v"(cs) : "v"(bflo(v[j]))); asm volatile("v_add_f32 %0, %0, %1" : "+v"(cs) : "v"(bfhi(v[j]))); }
  }
  if (is1) { cs = xq_sum(cs); if (lane < 16) __builtin_amdgcn_raw_buffer_store_b32(__float_as_uint(cs), mkrsrc(ws + WS_CS1, 4 * 1792 * 4), ((jb & 3) * 1792 + 16 * ((jb >> 2) * 4 + hw) + lane) * 4, 0, XAUX); }
}
DI void k1_helper(const Args& a, const u16* __restrict__ x, const u16* __restrict__ w, char* ws, char* smem, int bm, int bn, int rot, int hw, u64& tag) {
  const int lane = threadIdx.x & 63;
  const unsigned lds0 = (unsigned)(uintptr_t)smem;
  const unsigned voffA0 = (lane >> 3) * 1280 + (((lane & 7) ^ (lane >> 4)) << 4);
  const unsigned voffA1 = (lane >> 3) * 1280 + (((lane & 7) ^ (4 + (lane >> 4))) << 4);
  const unsigned voffB = (lane >> 4) * 3840 + (((((lane & 15) >> 2) ^ (lane >> 4)) * 4 + (lane & 3)) << 4);
  const char* xa = (const char*)x + (size_t)(bm * 128 + hw * 32) * 1280;
  const char* wb = (const char*)w + (size_t)(hw * 16) * 3840 + bn * 256;
  auto issue = [&](int st) {
    int ks = st + rot; if (ks >= 10) ks -= 10;
    const unsigned dst = lds0 + (st % K1_NSLOT) * K1_STAGE + hw * 4096;
#pragma unroll
    for (int i = 0; i < 4; ++i) glds16((i & 1) ? voffA1 : voffA0, xa + i * 8 * 1280 + ks * 128, dst + i * 1024);
#pragma unroll
    for (int i = 0; i < 4; ++i) glds16(voffB, wb + (size_t)(ks * 64 + i * 4) * 3840, dst + 16384 + i * 1024);
  };
  issue(0); issue(1);
  if (K1_PRE == 3) issue(2);
  WAITV(8 * (K1_PRE - 1)); BAR();
  if (K1_PRE == 2) issue(2);
  WAITV(8); BAR();
  issue(3);
  tag = get_tag(ws);
#pragma unroll 1
  for (int st = 1; st < 7; ++st) { WAITV(8); BAR(); issue(st + 3); }
  retile_dma(a, lds0 + LDS_RT, hw);
  WAITV(13); BAR();
  WAITV(5); BAR();
  const rsrc_t rs = mkrsrc(ws + WS_XST, S * 16 + 16384);
  const unsigned go = lane < 32 ? (bm * 128 + hw * 32 + lane) * 16 : S * 16 + (bn * 64 + hw * 16 + (lane & 15)) * 16;
  u32x4 rec;
#pragma unroll 1
  for (unsigned sp = 0; sp < SPIN_CAP; ++sp) {
    asm volatile("" ::: "memory");
    rec = __builtin_amdgcn_raw_buffer_load_b128(rs, go, 0, XAUX);
    if (__all(rec[1] == (unsigned)tag && rec[3] == (unsigned)(tag >> 32))) break;
    __builtin_amdgcn_s_sleep(1);
  }
  LAS unsigned* st = (LAS unsigned*)(smem + K1_NSLOT * K1_STAGE);
  if (lane < 32) { st[hw * 32 + lane] = rec[0]; st[128 + hw * 32 + lane] = rec[2]; }
  else if (lane < 48) { st[256 + hw * 32 + 2 * (lane & 15)] = rec[0]; st[257 + hw * 32 + 2 * (lane & 15)] = rec[2]; }
  LGKM0(); BAR();
}
DI void k1_colsum(const u16* __restrict__ x, const u16* __restrict__ w, char* ws, char* smem, int u, int wid, u64 tag) {
  const int lane = threadIdx.x & 63;
  if (u < 15) {
    const char* p = (const char*)w + (size_t)(wid * 80 + (lane >> 4)) * 3840 + u * 256 + (lane & 15) * 16;
    u32x4 v[20];
#pragma unroll
    for (int i = 0; i < 20; ++i) v[i] = *(const u32x4*)(p + (size_t)i * 4 * 3840);
    float a[8];
#pragma unroll
    for (int e = 0; e < 8; ++e) a[e] = 0.f;
#pragma unroll
    for (int i = 0; i < 20; ++i)
#pragma unroll
      for (int e = 0; e < 4; ++e) { a[2 * e] += bflo(v[i][e]); a[2 * e + 1] += bfhi(v[i][e]); }
    LAS float* part = (LAS float*)smem;
#pragma unroll
    for (int e = 0; e < 8; ++e) { a[e] = xq_sum(a[e]); if (lane < 16) part[wid * 128 + lane * 8 + e] = a[e]; }
    LGKM0(); BAR();
    if (wid == 0) {
      float c0 = 0.f, c1 = 0.f;
#pragma unroll
      for (int k = 0; k < 8; ++k) { c0 += part[k * 128 + 2 * lane]; c1 += part[k * 128 + 2 * lane + 1]; }
      st16(mkrsrc(ws + WS_CSQ, 16384), (u * 64 + lane) * 16, u32x4{__float_as_uint(c0), (unsigned)tag, __float_as_uint(c1), (unsigned)(tag >> 32)});
    }
  }
}

constexpr int CH_TILES = 8, CH_BYTES = CH_TILES * KVT, NCH = 64 / CH_TILES, CH_PIECES = CH_BYTES / 1024;
#define EX(v) __builtin_amdgcn_exp2f(v)
#define PF(p) __builtin_bit_cast(bf16x8, p)
DI float rowmax16(const f32x16& s) {
  float m = s[0];
#pragma unroll
  for (int e = 1; e < 16; ++e) m = fmaxf(m, s[e]);
  return xhalf_max(m);
}
DI s16x4 shift_from(const f32x16& s, s16x4 q2, int hh) {
  const float m = -rowmax16(s);
  const unsigned mh = pk2(m, 0.f) & 0xffffu, ml = pk2(m - bflo(mh), 0.f) & 0xffffu;
  return hh ? s16x4{(short)mh, (short)ml, 0, 0} : q2;
}
DI bool attn_unsafe(const f32x16& o, int hh) {
  const float l = hh ? o[8] : o[12];
  bool bad = !(l >= 1e-20f && l <= 1e30f);
#pragma unroll
  for (int e = 0; e < 12; ++e) bad |= !(fabsf(o[e]) <= 3e38f);
  return __any(bad);
}
DI f32x16 attn_online(rsrc_t kv, bf16x8 q1, s16x4 q2, int lane, int k2off, int voff) {
  if (lane >= 32) q2 = s16x4{0, 0, 0, 0};
  float m = -INFINITY; f32x16 o = f32x16{};
#pragma unroll 1
  for (int t = 0; t < 64; ++t) {
    const int kt = ((t + 64 - KSKEW) & 63) * KVT, vt = t * KVT;
    f32x16 s = MFMA32(ldfrag(kv, lane * 16, kt), q1, f32x16{}); s = MFMA32K8(ldhalf(kv, k2off, kt), q2, s);
    const float mn = fmaxf(m, rowmax16(s)), al = EX(m - mn); m = mn;
#pragma unroll
    for (int e = 0; e < 16; ++e) { o[e] *= al; s[e] = EX(s[e] - mn); }
    const u32x4 p0 = {pk2(s[0], s[1]), pk2(s[2], s[3]), pk2(s[4], s[5]), pk2(s[6], s[7])};
    const u32x4 p1 = {pk2(s[8], s[9]), pk2(s[10], s[11]), pk2(s[12], s[13]), pk2(s[14], s[15])};
    o = MFMA32(ldfrag(kv, voff, vt), PF(p0), o); o = MFMA32(ldfrag(kv, voff + 704, vt), PF(p1), o);
  }
  return o;
}
DI void attn_compute(char* ws, char* smem, int head, int qc, int wid, u64 tag) {
  const int lane = threadIdx.x & 63, r = lane & 31, hh = lane >> 5;
  const rsrc_t kv = mkrsrc(ws + WS_KV + (size_t)head * 64 * KVT, 64 * KVT);
  const int qt = qc * 8 + wid * 2;
  const rsrc_t rq = mkrsrc(ws + WS_Q + (size_t)(head * 64 + qt) * QT, 2 * QT);
  const int k2off = hh ? 1600 : 1024 + r * 8;
  const int voff = 1280 + hh * 352 + (r < 21 ? r : (r == 24 ? 20 : 21)) * 16;
  const bf16x8 qa1 = ldfrag(rq, lane * 16, 0), qb1 = ldfrag(rq, lane * 16, QT);
  s16x4 qa2 = ldhalf(rq, 1024 + r * 8, 0), qb2 = ldhalf(rq, 1024 + r * 8, QT);
  if (hh) { qa2 = s16x4{0, 0, 0, 0}; qb2 = s16x4{0, 0, 0, 0}; }
  unsigned ooff[3];
#pragma unroll
  for (int g = 0; g < 3; ++g) {
    const int k = head * 20 + 8 * g + 4 * hh;
    ooff[g] = (g == 2 && hh) ? S * D * 2 : ((qt * 2 + (r >> 4)) * 20 + (k >> 5)) * 1024 + (((k >> 2) & 3) * 16 + (r & 15)) * 16 + ((k >> 4) & 1) * 8;
    asm volatile("" : "+v"(ooff[g]));
  }
  bf16x8 k1 = ldfrag(kv, lane * 16, 63 * KVT); s16x4 k2 = ldhalf(kv, k2off, 63 * KVT);
  f32x16 oa, ob;
  { f32x16 s = MFMA32(k1, qa1, f32x16{}); s = MFMA32K8(k2, qa2, s); qa2 = shift_from(s, qa2, hh);
    s = MFMA32(k1, qb1, f32x16{}); s = MFMA32K8(k2, qb2, s); qb2 = shift_from(s, qb2, hh); }
  const unsigned lds0 = (unsigned)(uintptr_t)smem;
  unsigned ctl = lds0 + CTL2;
  STAMPT(1, 1);
  asm volatile(
    ".macro AT_E r\n v_exp_f32 v[\\r], v[\\r]\n v_exp_f32 v[\\r+1], v[\\r+1]\n.endm\n"
    ".macro AT_C d, r\n v_cvt_pk_bf16_f32 v[\\d], v[\\r], v[\\r+1]\n.endm\n"
    ".macro AT_H sc, sn, pc, pp, o, x, xk2, q1, q2, y, yk2, off, mode\n"
    " v_mfma_f32_32x32x16_bf16 v[\\o:\\o+15], v[\\x+4:\\x+7], v[\\pp:\\pp+3], v[\\o:\\o+15]\n"
    " .if \\mode & 1\n ds_read_b128 v[\\y+4:\\y+7], v130 offset:\\off\n .endif\n"
    " AT_C \\pp+6, \\sn+12\n AT_C \\pp+7, \\sn+14\n AT_E \\sc\n AT_E \\sc+2\n"
    " .if (\\mode & 16) == 0\n v_mfma_f32_32x32x16_bf16 v[\\sn:\\sn+15], v[\\x:\\x+3], v[\\q1:\\q1+3], 0\n .endif\n"
    " .if \\mode & 1\n ds_read_b128 v[\\y:\\y+3], v128 offset:\\off\n .endif\n"
    " .if \\mode & 4\n ds_read_b32 v133, v131 offset:\\off\n .endif\n"
    " AT_E \\sc+4\n AT_C \\pc, \\sc\n AT_E \\sc+6\n AT_C \\pc+1, \\sc+2\n"
    " .if (\\mode & 16) == 0\n v_mfma_f32_32x32x8_bf16 v[\\sn:\\sn+15], v[\\xk2:\\xk2+1], v[\\q2:\\q2+1], v[\\sn:\\sn+15]\n .endif\n"
    " AT_E \\sc+8\n AT_C \\pc+2, \\sc+4\n AT_E \\sc+10\n AT_C \\pc+3, \\sc+6\n"
    " v_mfma_f32_32x32x16_bf16 v[\\o:\\o+15], v[\\x+8:\\x+11], v[\\pp+4:\\pp+7], v[\\o:\\o+15]\n"
    " .if \\mode & 1\n ds_read_b64 v[\\yk2:\\yk2+1], v129 offset:\\off\n ds_read_b128 v[\\y+8:\\y+11], v130 offset:\\off+704\n .endif\n"
    " .if \\mode & 2\n s_waitcnt lgkmcnt(0)\n .endif\n"
    " .if \\mode & 4\n s_mov_b64 exec, 1\n ds_add_u32 v131, v132 offset:\\off+60\n s_mov_b64 exec, -1\n v_cmp_gt_u32 vcc, 4, v133\n .endif\n"
    " AT_E \\sc+12\n AT_C \\pc+4, \\sc+8\n AT_E \\sc+14\n AT_C \\pc+5, \\sc+10\n"
    ".endm\n"
    ".macro AT_A x, xk2, y, yk2, t\n AT_H 56, 40, 80, 72, 8, \\x, \\xk2, 116, 124, \\y, \\yk2, (\\t)*2688, 1\n.endm\n"
    ".macro AT_B x, xk2, off, mode\n AT_H 40, 56, 72, 80, 24, \\x, \\xk2, 120, 126, 0, 0, \\off, \\mode\n.endm\n"
    ".macro AT_2 t\n AT_A 104, 102, 88, 100, \\t\n AT_B 104, 102, 0, 2\n AT_A 88, 100, 104, 102, \\t+1\n AT_B 88, 100, 0, 2\n.endm\n"
    ".macro AT_SPIN off, back\n"
    "1: s_cmp_eq_u32 s41, 0\n s_cbranch_scc1 \\back\n s_sub_u32 s41, s41, 1\n s_sleep 1\n ds_read_b32 v133, v131 offset:\\off\n s_waitcnt lgkmcnt(0)\n v_cmp_gt_u32 vcc, 4, v133\n s_nop 4\n s_cbranch_vccnz 1b\n s_branch \\back\n"
    ".endm\n"
    " s_nop 1\n"
    " v_mfma_f32_32x32x16_bf16 v[40:55], v[88:91], v[116:119], 0\n"
    " v_mfma_f32_32x32x8_bf16 v[40:55], v[100:101], v[124:125], v[40:55]\n"
    " v_mov_b32 v132, 1\n"
    " .irp r, 8,9,10,11,12,13,14,15,16,17,18,19,20,21,22,23,24,25,26,27,28,29,30,31,32,33,34,35,36,37,38,39,68,69,70,71,80,81,82,83,84,85,92,93,94,95,96,97,98,99\n v_mov_b32 v[\\r], 0\n .endr\n"
    " s_mov_b32 s40, 4\n s_mov_b32 s41, 0x400000\n"
    ".Lat_w0: s_sub_u32 s41, s41, 1\n s_cbranch_scc1 .Lat_w1\n ds_read_b32 v133, v131\n s_waitcnt lgkmcnt(0)\n v_cmp_gt_u32 vcc, 4, v133\n s_nop 4\n s_cbranch_vccnz .Lat_w0\n"
    ".Lat_w1: s_mov_b32 s41, 0x400000\n"
    " ds_read_b128 v[108:111], v130\n ds_read_b128 v[104:107], v128\n ds_read_b64 v[102:103], v129\n ds_read_b128 v[112:115], v130 offset:704\n"
    " s_branch .Lat_mid\n"
    ".Lat_s1: AT_SPIN 4, .Lat_b1\n"
    ".Lat_s2: AT_SPIN 8, .Lat_b2\n"
    " .p2align 6\n"
    ".Lat_loop:\n"
    " AT_A 88, 100, 104, 102, 0\n"
    ".Lat_mid:\n"
    " AT_B 88, 100, 0, 2\n"
    " AT_2 1\n AT_2 3\n AT_2 5\n"
    " AT_A 104, 102, 88, 100, 7\n AT_B 104, 102, 4, 6\n"
    " s_cbranch_vccnz .Lat_s1\n"
    ".Lat_b1:\n"
    " AT_A 88, 100, 104, 102, 8\n AT_B 88, 100, 0, 2\n"
    " AT_2 9\n AT_2 11\n AT_2 13\n"
    " AT_A 104, 102, 88, 100, 15\n AT_B 104, 102, 8, 6\n"
    " s_cbranch_vccnz .Lat_s2\n"
    ".Lat_b2:\n"
    " v_add_u32 v131, 8, v131\n"
    " s_sub_u32 s40, s40, 1\n s_cmp_lg_u32 s40, 0\n s_cbranch_scc1 .Lat_loop\n"
    " AT_H 56, 40, 80, 72, 8, 88, 100, 116, 124, 0, 0, 0, 16\n"
    " AT_C 86, 68\n AT_C 87, 70\n"
    " v_mfma_f32_32x32x16_bf16 v[24:39], v[92:95], v[80:83], v[24:39]\n s_nop 0\n"
    " v_mfma_f32_32x32x16_bf16 v[24:39], v[96:99], v[84:87], v[24:39]\n"
    " s_nop 12\n"
    : "=&{v[8:23]}"(oa), "=&{v[24:39]}"(ob), "+{v131}"(ctl), "+{v[88:91]}"(k1), "+{v[100:101]}"(k2)
    : "{v[116:119]}"(qa1), "{v[120:123]}"(qb1), "{v[124:125]}"(qa2), "{v[126:127]}"(qb2),
      "{v128}"(lds0 + lane * 16), "{v129}"(lds0 + k2off), "{v130}"(lds0 + voff)
    : "memory", "vcc", "scc", "s40", "s41", "v40","v41","v42","v43","v44","v45","v46","v47","v48","v49","v50","v51","v52","v53","v54","v55","v56","v57","v58","v59","v60","v61","v62","v63",
      "v64","v65","v66","v67","v68","v69","v70","v71","v72","v73","v74","v75","v76","v77","v78","v79","v80","v81","v82","v83","v84","v85","v86","v87","v92","v93","v94","v95","v96","v97","v98","v99",
      "v102","v103","v104","v105","v106","v107","v108","v109","v110","v111","v112","v113","v114","v115","v132","v133");
  STAMPT(1, 2);
  const rsrc_t ro = mkrsrc(ws + WS_O, S * D * 2);
  auto put = [&](const f32x16& o, int u) {
    const float il = __builtin_amdgcn_rcpf(hh ? o[8] : o[12]);
#pragma unroll
    for (int g = 0; g < 3; ++g) st8s(ro, ooff[g], u * 40960, u32x2{pk2(o[4 * g] * il, o[4 * g + 1] * il), pk2(o[4 * g + 2] * il, o[4 * g + 3] * il)});
  };
  put(oa, 0); put(ob, 1);
  const bool fa = attn_unsafe(oa, hh), fb = attn_unsafe(ob, hh);
  if (fa) { WAITV(0); put(attn_online(kv, qa1, qa2, lane, k2off, voff), 0); }
  if (fb) { WAITV(0); put(attn_online(kv, qb1, qb2, lane, k2off, voff), 1); }
  STAMPT(1, 3);
  WAITV(0);
  if (lane == 0) signal(ws, FL_O + (qc * 4 + wid) * 32 + head, tag);
}

template <int NC> struct GS { f32x4 acc[2][NC]; };
template <int NC, int NKS, int PD, int SSTRIDE, int RING, bool BARS, class Mid> DI void gemm_run(GS<NC>& g, const LAS char* wl, rsrc_t ra, unsigned va0, unsigned va1, unsigned astep, int rot, Mid mid) {
  bf16x8 af[PD][2], bf[2][NC];
  auto soff = [&](int s) { int k = s + rot; if (k >= NKS) k -= NKS; return (unsigned)k * astep; };
#pragma unroll
  for (int s = 0; s < PD; ++s) { const unsigned so = soff(s); af[s][0] = ldfrag(ra, va0, so); af[s][1] = ldfrag(ra, va1, so); }
  SBAR();
  const LAS char* wb[3] = {wl, wl + 61440, wl + 122880};
  asm volatile("" : "+v"(wb[0]), "+v"(wb[1]), "+v"(wb[2]));
  auto rdw = [&](int off) { return *(const LAS bf16x8*)(wb[off / 61440] + off % 61440); };
  mid(-1);
#pragma unroll
  for (int j = 0; j < NC; ++j) bf[0][j] = rdw(j * 1024);
#pragma unroll
  for (int s = 0; s < NKS; ++s) {
    LGKM0();
    if (BARS && (s + 1) % 4 == 0 && s + 1 < NKS) BAR();
    mid(s);
    SBAR();
#pragma unroll
    for (int m = 0; m < 2 * NC; ++m) {
      mfma16_acc(g.acc[m & 1][m >> 1], bf[s & 1][m >> 1], af[s % PD][m & 1]); SBAR();
      if (s + 1 < NKS && m < NC) { bf[(s + 1) & 1][m] = rdw(((s + 1) % RING) * SSTRIDE + m * 1024); SBAR(); }
      if (s >= 1 && s - 1 + PD < NKS && (m == NC + 1 || m == NC + 3)) { af[(s - 1) % PD][m == NC + 3] = ldfrag(ra, m == NC + 3 ? va1 : va0, soff(s - 1 + PD)); SBAR(); }
    }
  }
  asm volatile("s_nop 7" ::: "memory"); SBAR();
}
template <int NC> DI void pair_reduce(GS<NC>& g, char* smem, int wid) {
  const int lane = threadIdx.x & 63;
  LAS f32x4* red = (LAS f32x4*)(smem + 143360) + lane;
#pragma unroll
  for (int j = 0; j < NC; ++j) red[(wid * NC + j) * 64] = g.acc[1][j];
  LGKM0(); BAR();
#pragma unroll
  for (int j = 0; j < NC; ++j) g.acc[0][j] += red[((wid ^ 2) * NC + j) * 64];
}
DI f32x4 unpack4(u32x2 v) { return f32x4{bflo(v[0]), bfhi(v[0]), bflo(v[1]), bfhi(v[1])}; }
DI u32x2 pack4(f32x4 v) { return u32x2{pk2(v[0], v[1]), pk2(v[2], v[3])}; }
DI unsigned piece_off(int rt, int gct, int nks, int lane) { return (rt * nks + (gct >> 1)) * 1024 + lane * 16 + (gct & 1) * 8; }
template <int NC> DI void store_tiles(rsrc_t r, int rt, int gct0, int nct, int nks, int lane, const u32x2 (&v)[NC]) {
  if (gct0 & 1) {
    st8(r, piece_off(rt, gct0, nks, lane), v[0]);
#pragma unroll
    for (int j = 1; j + 1 < NC; j += 2) {
      if (j + 1 < nct) st16(r, piece_off(rt, gct0 + j, nks, lane), u32x4{v[j][0], v[j][1], v[j + 1][0], v[j + 1][1]});
      else st8(r, piece_off(rt, gct0 + j, nks, lane), v[j]);
    }
  } else {
#pragma unroll
    for (int j = 0; j + 1 < NC; j += 2) st16(r, piece_off(rt, gct0 + j, nks, lane), u32x4{v[j][0], v[j][1], v[j + 1][0], v[j + 1][1]});
    if (nct == NC) st8(r, piece_off(rt, gct0 + NC - 1, nks, lane), v[NC - 1]);
  }
}

#define P4_PD 8
#define P5_PD 8
#define ROT3(c) 0
#define ROT4(c4) 0
#define ROT5(c) 0
#define PACE 1
constexpr int LDS_WO = 61440, LDS_CB = 162816, LDS_BYTES = 163840;
constexpr int CODE_BYTES = 45056;

DI void post_compute(const Args& a, char* smem, int wid, u64 tag, u64 t_start) {
  char* ws = a.ws;
  const int lane = threadIdx.x & 63, b = blockIdx.x;
  const int grp = (b & 7) * 4 + ((b >> 3) & 3), c = b >> 5, rh = wid & 1, kh = wid >> 1;
  const int rt = grp * 4 + rh * 2 + kh, tok = rt * 16 + (lane & 15);
  u32x2 hv[5];
  {
    GS<5> g;
#pragma unroll
    for (int j = 0; j < 5; ++j) { g.acc[0][j] = unpack4(*(const u32x2*)(a.x + (size_t)tok * D + c * 80 + j * 16 + (lane >> 4) * 4)); g.acc[1][j] = f32x4{}; }
#pragma unroll
    for (int j = 0; j < 5; ++j) asm volatile("" : "+v"(g.acc[0][j]), "+v"(g.acc[1][j]));
    const rsrc_t ra = mkrsrc(ws + WS_O, S * D * 2);
    const unsigned va0 = (rt * 20 + kh) * 1024 + lane * 16, va1 = ((rt ^ 1) * 20 + kh) * 1024 + lane * 16;
    await(ws, FL_O + grp * 32 + (lane & 31), tag);
    STAMPT(2, 0);
    gemm_run<5, 10, 10, 10240, 10, false>(g, (const LAS char*)smem + LDS_WO + kh * 5120 + lane * 16, ra, va0, va1, 2048, ROT3(c),
                                          [&](int s) { if (s == 9) BAR(); });
    STAMPT(2, 1);
    pair_reduce<5>(g, smem, wid);
#pragma unroll
    for (int j = 0; j < 5; ++j) hv[j] = pack4(g.acc[0][j]);
    store_tiles<5>(mkrsrc(ws + WS_H, S * D * 2), rt, c * 5, 5, 20, lane, hv);
    f32x4 s1 = g.acc[0][0], s2 = g.acc[0][0] * g.acc[0][0];
#pragma unroll
    for (int j = 1; j < 5; ++j) { s1 += g.acc[0][j]; s2 += g.acc[0][j] * g.acc[0][j]; }
    const float t1 = xq_sum((s1[0] + s1[1]) + (s1[2] + s1[3])), t2 = xq_sum((s2[0] + s2[1]) + (s2[2] + s2[3]));
    st8(mkrsrc(ws + WS_ST, S * 64), lane < 16 ? tok * 64 + c * 8 : S * 64, u32x2{__float_as_uint(t1), __float_as_uint(t2)});
    STAMPT(2, 2);
    WAITV(0);
    if (lane == 0) signal(ws, FL_H + rt * 8 + c, tag);
    STAMPT(2, 3);
  }
  {
    const int g4 = (b & 7) * 2 + ((b >> 4) & 1), c4 = ((b >> 5) << 1) | ((b >> 3) & 1);
    const int gct0 = (c4 * 27) >> 2, nct = (((c4 + 1) * 27) >> 2) - gct0;
    GS<7> g;
#pragma unroll
    for (int j = 0; j < 7; ++j) { g.acc[0][j] = f32x4{}; g.acc[1][j] = f32x4{}; asm volatile("" : "+v"(g.acc[0][j]), "+v"(g.acc[1][j])); }
    const rsrc_t ra = mkrsrc(ws + WS_H, S * D * 2);
    const int rt0 = g4 * 8 + wid * 2;
    const unsigned va0 = rt0 * 20480 + lane * 16;
    PADT(4, 70);
    await(ws, FL_H + rt0 * 8 + (lane & 15), tag);
    STAMPT(3, 0);
    u32x4 ps[2]; f32x4 cs[7], bv[7];
    const LAS f32x4* cb = (const LAS f32x4*)(smem + LDS_CB) + (lane >> 4);
    gemm_run<7, 20, P4_PD, 7168, 20, false>(g, (const LAS char*)smem + lane * 16, ra, va0, va0 + 20480, 1024, ROT4(c4), [&](int s) {
      if (s == -1) {
#pragma unroll
        for (int i = 0; i < 2; ++i) ps[i] = __builtin_amdgcn_raw_buffer_load_b128(mkrsrc(ws + WS_ST, S * 64), ((rt0 + i) * 16 + (lane & 15)) * 64 + (lane >> 4) * 16, 0, XAUX);
        BAR();
      }
      if (s == 0) BAR();
      if (s == 18) {
#pragma unroll
        for (int j = 0; j < 7; ++j) { cs[j] = cb[j * 4]; bv[j] = cb[28 + j * 4]; }
      }
      if (s == 19) BAR();
    });
    STAMPT(3, 1);
    const rsrc_t ro = mkrsrc(ws + WS_H1, S * F * 2);
#pragma unroll
    for (int i = 0; i < 2; ++i) {
      const float m = xq_sum(__uint_as_float(ps[i][0]) + __uint_as_float(ps[i][2])) * (1.f / D);
      const float var = fmaxf(xq_sum(__uint_as_float(ps[i][1]) + __uint_as_float(ps[i][3])) * (1.f / D) - m * m, 0.f);
      const float r1 = __builtin_amdgcn_rsqf(var + 1e-5f);
      const float rstd = r1 * __builtin_amdgcn_rsqf(var * r1 * r1 + 1e-5f);
      const float mr = -m * rstd;
      f32x4 v[7], t[7];
#pragma unroll
      for (int j = 0; j < 7; ++j) t[j] = cs[j] * mr + bv[j];
      SBAR();
#pragma unroll
      for (int j = 0; j < 7; ++j) v[j] = g.acc[i][j] * rstd + t[j];
      SBAR();
#pragma unroll
      for (int j = 0; j < 7; ++j) t[j] = v[j] * v[j];
      SBAR();
#pragma unroll
      for (int j = 0; j < 7; ++j) t[j] = t[j] * -0.1029432397f + -2.302208198f;
      SBAR();
#pragma unroll
      for (int j = 0; j < 7; ++j) t[j] = t[j] * v[j];
      SBAR();
#pragma unroll
      for (int j = 0; j < 7; ++j) t[j] = f32x4{EX(t[j][0]), EX(t[j][1]), EX(t[j][2]), EX(t[j][3])};
      SBAR();
#pragma unroll
      for (int j = 0; j < 7; ++j) t[j] = t[j] + 1.f;
      SBAR();
#pragma unroll
      for (int j = 0; j < 7; ++j) t[j] = f32x4{__builtin_amdgcn_rcpf(t[j][0]), __builtin_amdgcn_rcpf(t[j][1]), __builtin_amdgcn_rcpf(t[j][2]), __builtin_amdgcn_rcpf(t[j][3])};
      SBAR();
#pragma unroll
      for (int j = 0; j < 7; ++j) t[j] = t[j] * v[j];
      SBAR();
      u32x2 h1[7];
#pragma unroll
      for (int j = 0; j < 7; ++j) h1[j] = pack4(t[j]);
      store_tiles<7>(ro, rt0 + i, gct0, nct, 54, lane, h1);
      SBAR();
    }
    STAMPT(3, 2);
    WAITV(0);
    if (lane == 0) signal(ws, FL_H1 + (rt0 >> 1) * 16 + c4, tag);
    STAMPT(3, 3);
  }
  {
    GS<5> g;
#pragma unroll
    for (int j = 0; j < 5; ++j) { g.acc[0][j] = unpack4(*(const u32x2*)(a.b2 + c * 80 + j * 16 + (lane >> 4) * 4)) + unpack4(hv[j]); g.acc[1][j] = f32x4{}; }
#pragma unroll
    for (int j = 0; j < 5; ++j) asm volatile("" : "+v"(g.acc[0][j]), "+v"(g.acc[1][j]));
    const rsrc_t ra = mkrsrc(ws + WS_H1, S * F * 2);
    const unsigned va0 = (rt * 54 + kh) * 1024 + lane * 16, va1 = ((rt ^ 1) * 54 + kh) * 1024 + lane * 16;
    PADT(5, 80);
    await(ws, FL_H1 + (grp * 2 + rh) * 16 + (lane & 15), tag);
    STAMPT(4, 0);
    gemm_run<5, 27, P5_PD, 10240, 16, true>(g, (const LAS char*)smem + kh * 5120 + lane * 16, ra, va0, va1, 2048, ROT5(c), [&](int s) { if (s == -1) BAR(); });
    STAMPT(4, 1);
    pair_reduce<5>(g, smem, wid);
#pragma unroll
    for (int j = 0; j < 5; ++j) *(u32x2*)(a.out + (size_t)tok * D + c * 80 + j * 16 + (lane >> 4) * 4) = pack4(g.acc[0][j]);
    STAMPT(4, 2);
  }
}

DI void main_helper(const Args& a, char* smem, int head, int hw, u64 tag) {
  char* ws = a.ws;
  const int lane = threadIdx.x & 63, b = blockIdx.x;
  const char* kv = ws + WS_KV + (size_t)head * 64 * KVT;
  const unsigned lds0 = (unsigned)(uintptr_t)smem, v16 = lane * 16;
  auto issue = [&](int c) {
#pragma unroll
    for (int i = 0; i < 6; ++i) { const int pc = hw + 4 * i; if (pc < CH_PIECES) glds16x(v16, kv + (size_t)c * CH_BYTES + pc * 1024, lds0 + (c & 1) * CH_BYTES + pc * 1024); }
  };
  const unsigned ctl = lds0 + CTL2;
  auto refill = [&](int c) { lds_wait<NAP2>((const LAS char*)smem + CTL2 + 64 + (c - 2) * 4); STAMPH(6, c); issue(c); WAITV(0); lds_post(ctl + c * 4, 1); STAMPH(5, c); };
  __builtin_amdgcn_s_setprio(3);
  issue(0); issue(1);
  WAITV(5); lds_post(ctl, 1); STAMPH(5, 0);
  WAITV(0); lds_post(ctl + 4, 1); STAMPH(5, 1);
  const int c = b >> 5, c4 = ((b >> 5) << 1) | ((b >> 3) & 1), gct0 = (c4 * 27) >> 2;
  retile_store(ws, (const LAS char*)smem + LDS_RT, hw);
  WAITV(0);
  if (lane == 0 && __hip_atomic_fetch_add((LAS unsigned*)(smem + CTL2 + 60), 1u, __ATOMIC_RELAXED, __HIP_MEMORY_SCOPE_WORKGROUP) == 3u) signal(ws, FL_W + b, tag);
  refill(2);
  refill(3);
  {
    gu64* p = flagp(ws, FL_W + lane);
#pragma unroll 1
    for (unsigned sp = 0; sp < SPIN_CAP; ++sp) {
      const u64 f0 = __hip_atomic_load(p, RLX_AGENT), f1 = __hip_atomic_load(p + 64, RLX_AGENT), f2 = __hip_atomic_load(p + 128, RLX_AGENT), f3 = __hip_atomic_load(p + 192, RLX_AGENT);
      if (__all(f0 == tag && f1 == tag && f2 == tag && f3 == tag)) break;
      __builtin_amdgcn_s_sleep(4);
    }
    asm volatile("" ::: "memory");
  }
  if (b == 0 && hw == 0 && lane == 0) signal(ws, FL_EPOCH, tag);
  f32x4 cst;
  {
    const int n = gct0 * 16 + (lane < 28 ? lane : (lane < 56 ? lane - 28 : 0)) * 4;
    const rsrc_t rc = mkrsrc(ws + WS_CS1, 4 * 1792 * 4);
    f32x4 t[4];
#pragma unroll
    for (int k = 0; k < 4; ++k) t[k] = __builtin_bit_cast(f32x4, __builtin_amdgcn_raw_buffer_load_b128(rc, n * 4, k * 7168, XAUX));
    const f32x4 bv = unpack4(*(const u32x2*)(a.b1 + n));
    cst = lane < 28 ? (t[0] + t[1]) + (t[2] + t[3]) : bv;
    asm volatile("" : "+v"(cst));
  }
  const char* wo = ws + WS_WO + (size_t)c * 102400;
  auto wo_dma = [&](int i0, int i1) {
#pragma unroll 1
    for (int i = i0; i < i1; ++i) { const int p = i * 4 + hw; int q = p + ROT3(c) * 10; if (q >= 100) q -= 100; glds16x(v16, wo + q * 1024, lds0 + LDS_WO + p * 1024); }
  };
  bf16x8 wa[35], wb[20];
  const rsrc_t r1 = mkrsrc(ws + WS_W1 + (size_t)gct0 * 20480, 7 * 20480), r2 = mkrsrc(ws + WS_W2 + (size_t)c * 276480, 276480);
  auto ld1 = [&](int i) { int k = (i / 7) * 4 + hw + ROT4(c4); if (k >= 20) k -= 20; return ldfrag(r1, v16, ((i % 7) * 20 + k) * 1024); };
  auto ld2 = [&](int i) { int p = i * 4 + hw + ROT5(c) * 10; if (p >= 270) p -= 270; return ldfrag(r2, v16, p * 1024); };
  refill(4);
  wo_dma(0, 25);
  refill(5);
  lds_post(ctl + 32, 1);
#pragma unroll
  for (int i = 0; i < 20; ++i) wa[i] = ld1(i);
  SBAR();
  refill(6);
#pragma unroll
  for (int i = 20; i < 35; ++i) wa[i] = ld1(i);
  SBAR();
  refill(7);
#pragma unroll
  for (int i = 0; i < 20; ++i) wb[i] = ld2(i);
  SBAR();
  lds_wait<NAP2>((const LAS char*)smem + CTL2 + 64 + 28);
  __builtin_amdgcn_s_setprio(0);
  LAS bf16x8* lw = (LAS bf16x8*)smem + hw * 7 * 64 + lane;
#pragma unroll
  for (int i = 0; i < 14; ++i) lw[((i / 7) * 28 + i % 7) * 64] = wa[i];
  BAR();
#pragma unroll
  for (int i = 14; i < 21; ++i) lw[((i / 7) * 28 + i % 7) * 64] = wa[i];
  BAR();
#pragma unroll
  for (int i = 21; i < 35; ++i) lw[((i / 7) * 28 + i % 7) * 64] = wa[i];
  LGKM0();
  BAR();
  if (hw == 0 && lane < 56) *((LAS f32x4*)(smem + LDS_CB) + lane) = cst;
  LGKM0(); BAR();
#pragma unroll
  for (int i = 0; i < 35; ++i) { wa[i] = ld2(20 + i); SBAR(); __builtin_amdgcn_s_sleep(PACE); SBAR(); }
  BAR();
  LAS bf16x8* l2 = (LAS bf16x8*)smem + hw * 64 + lane;
#pragma unroll
  for (int i = 0; i < 20; ++i) l2[i * 256] = wb[i];
  LGKM0(); SBAR();
#pragma unroll
  for (int i = 0; i < 13; ++i) wb[i] = ld2(55 + i);
  SBAR();
#pragma unroll
  for (int i = 0; i < 20; ++i) l2[(20 + i) * 256] = wa[i];
  LGKM0(); BAR();
  BAR();
#pragma unroll
  for (int i = 0; i < 10; ++i) l2[i * 256] = wa[20 + i];
  BAR();
#pragma unroll
  for (int i = 0; i < 10; ++i) l2[(10 + i) * 256] = i < 5 ? wa[30 + i] : wb[i - 5];
  BAR();
#pragma unroll
  for (int i = 0; i < 8; ++i) l2[(20 + i) * 256] = wb[5 + i];
  LGKM0(); BAR();
  BAR(); BAR();
  BAR();
}

__global__ __launch_bounds__(512, 2) void fused_block(Args a) {
  extern __shared__ __attribute__((aligned(16))) char smem[];
  char* ws = a.ws;
  const int wid = __builtin_amdgcn_readfirstlane(threadIdx.x >> 6), lane = threadIdx.x & 63;
  const int b = blockIdx.x, xg = b & 7, j = b >> 3;
  STAMPT(0, 0);
  const u64 t_start = PADAT >= 0 ? __builtin_amdgcn_s_memrealtime() : 0;
  asm volatile("" :: "s"(a.x), "s"(a.wqkv), "s"(a.ws));
  const int bm = (xg >> 1) * 4 + (j & 3), bn = (xg & 1) * 8 + (j >> 2);
#define K1_ROT (j % 10)
  const int head = (b & 7) * 4 + ((b >> 3) & 3), qc = b >> 5;
  u64 tag = 0;
  if (bn >= 15) {
    tag = get_tag(ws);
    if (wid < 4) { XRow xr; xrow_load(xr, a.x, b * 8 + wid * 2); xrow_publish(xr, ws, b * 8 + wid * 2, tag); }
    k1_colsum(a.x, a.wqkv, ws, smem, bm, wid, tag);
    if (wid >= 4) retile_dma(a, (unsigned)(uintptr_t)smem + LDS_RT, wid - 4);
  }
  if (wid >= 4) {
    if (bn < 15) k1_helper(a, a.x, a.wqkv, ws, smem, bm, bn, K1_ROT, wid - 4, tag);
    WAITV(0); BAR(); BAR();
    main_helper(a, smem, head, wid - 4, tag);
  } else {
    if (bn < 15) {
      XRow xr;
      xrow_load(xr, a.x, b * 8 + wid * 2);
      {
        const char* pc = (const char*)(__builtin_amdgcn_s_getpc() & ~127ll);
        const unsigned off = min((((j & 1) * 4 + wid) * 64 + lane) * 128, CODE_BYTES);
        asm volatile("s_mov_b32 m0, %2\n\ts_nop 0\n\tglobal_load_lds_dword %0, %1" :: "v"(off), "s"(pc), "s"(__builtin_amdgcn_readfirstlane((unsigned)(uintptr_t)smem + 140000 + wid * 256)) : "memory");
      }
      {
        const char* base = wid == 0 ? ws : (wid == 1 ? (const char*)a.out : (wid == 2 ? (const char*)a.w1 : (const char*)a.w2));
        const unsigned off = wid == 0 ? min(lane * 524288u, (unsigned)WS_FL) : min(lane * 65536u, 2200000u & ~63u);
        asm volatile("s_mov_b32 m0, %2\n\ts_nop 0\n\tglobal_load_lds_dword %0, %1 sc1" :: "v"(off), "s"(base), "s"(__builtin_amdgcn_readfirstlane((unsigned)(uintptr_t)smem + 141312 + wid * 256)) : "memory");
      }
      tag = get_tag(ws);
      if (bn < 10) k1_compute<false>(ws, smem, bm, bn, wid, xr, tag, t_start); else k1_compute<true>(ws, smem, bm, bn, wid, xr, tag, t_start);
    }
    STAMPT(0, 3);
    if (wid == 3 && lane < 32) { ((LAS unsigned*)(smem + CTL2))[lane] = 0; LGKM0(); }
    WAITV(0); BAR();
    if (threadIdx.x == 0) signal(ws, FL_QKV + bn * 16 + bm, tag);
    STAMPT(0, 4);
    const int t0 = (head * 20) >> 7, t1 = (head * 20 + 19) >> 7;
    if (wid == 1) await(ws, FL_QKV + (5 + (lane >> 5) * 5 + ((lane & 16) ? t1 : t0)) * 16 + (lane & 15), tag);
    if (wid == 2) await(ws, FL_QKV + ((lane & 2) ? t1 : t0) * 16 + qc * 2 + (lane & 1), tag);
    PADT(1, 30);
    BAR();
    STAMPT(1, 0);
    attn_compute(ws, smem, head, qc, wid, tag); STAMPT(1, 4); PADT(2, 60); post_compute(a, smem, wid, tag, t_start); PADT(0, 100);
  }
}

extern "C" void kernel_launch(void* const* d_in, const int* in_sizes, int n_in, void* d_out, int out_size, void* d_ws, size_t ws_size, hipStream_t stream) {
  Args a;
  a.x = (const u16*)d_in[0]; a.wqkv = (const u16*)d_in[1]; a.wo = (const u16*)d_in[2];
  a.w1 = (const u16*)d_in[4]; a.b1 = (const u16*)d_in[5]; a.w2 = (const u16*)d_in[6]; a.b2 = (const u16*)d_in[7];
  a.out = (u16*)d_out; a.ws = (char*)d_ws;
  (void)hipFuncSetAttribute((const void*)fused_block, hipFuncAttributeMaxDynamicSharedMemorySize, LDS_BYTES);
  fused_block<<<256, 512, LDS_BYTES, stream>>>(a);
}
```

```cpp
#include <hip/hip_runtime.h>
#include <hip/hip_bf16.h>
typedef unsigned short u16;
typedef __attribute__((ext_vector_type(8))) short bf16x8;
typedef __attribute__((ext_vector_type(8))) __bf16 bf8;
typedef __attribute__((ext_vector_type(4))) short s16x4;
typedef __attribute__((ext_vector_type(16))) float f32x16;
typedef __attribute__((ext_vector_type(4))) float f32x4;
typedef __attribute__((ext_vector_type(2))) float f32x2;
typedef __attribute__((ext_vector_type(4))) unsigned u32x4;
typedef __attribute__((ext_vector_type(2))) unsigned u32x2;
typedef __attribute__((ext_vector_type(2))) __bf16 bf2;
#define LAS __attribute__((address_space(3)))
#define DI __device__ __forceinline__
#define SBAR() __builtin_amdgcn_sched_barrier(0)
#define LGKM0() __builtin_amdgcn_s_waitcnt(0xC07F)
#define BAR() asm volatile("s_barrier" ::: "memory")
#define WAITV(n) asm volatile("s_waitcnt vmcnt(%0)" :: "i"(n) : "memory")

constexpr int S = 2048, D = 640, H = 32, DH = 20, F = 1728;
constexpr int QT = 1280, KVT = 2688;
constexpr size_t WS_Q = 0, WS_KV = WS_Q + (size_t)H * 64 * QT, WS_O = WS_KV + (size_t)H * 64 * KVT,
                 WS_H = WS_O + (size_t)S * D * 2, WS_H1 = WS_H + (size_t)S * D * 2,
                 WS_ST = WS_H1 + (size_t)S * F * 2, WS_WO = WS_ST + (size_t)S * 64, WS_W1 = WS_WO + (size_t)D * D * 2, WS_W2 = WS_W1 + (size_t)D * F * 2 + 20480,
                 WS_CS1 = WS_W2 + (size_t)D * F * 2, WS_XST = WS_CS1 + 4 * 1792 * 4, WS_CSQ = WS_XST + (size_t)S * 16, WS_FL = WS_CSQ + 16384, WS_END = WS_FL + 65536;
#define STAMPT(k, s) do {} while (0)
#define STAMPH(k, s) do {} while (0)
#define PADAT -1
#define PADT(k, us) do { if (PADAT == (k)) { while (__builtin_amdgcn_s_memrealtime() - t_start < (us) * 100ull) __builtin_amdgcn_s_sleep(2); } } while (0)
constexpr int KSKEW = 1;
constexpr float QSCALE = 0.22360679774997896f * 1.4426950408889634f;

DI float bf2f(u16 v) { return __uint_as_float((unsigned)v << 16); }
DI float bflo(unsigned u) { return __uint_as_float(u << 16); }
DI float bfhi(unsigned u) { return __uint_as_float(u & 0xffff0000u); }
DI unsigned pk2(float lo, float hi) { f32x2 v = {lo, hi}; return __builtin_bit_cast(unsigned, __builtin_convertvector(v, bf2)); }
DI float rbf(float v) { return bflo(pk2(v, 0.f)); }
DI void glds16(unsigned voff, const void* sbase, unsigned lds) {
  asm volatile("s_mov_b32 m0, %2\n\ts_nop 0\n\tglobal_load_lds_dwordx4 %0, %1" :: "v"(voff), "s"(sbase), "s"(__builtin_amdgcn_readfirstlane(lds)) : "memory"); }
DI void glds16x(unsigned voff, const void* sbase, unsigned lds) {
  asm volatile("s_mov_b32 m0, %2\n\ts_nop 0\n\tglobal_load_lds_dwordx4 %0, %1 sc1" :: "v"(voff), "s"(sbase), "s"(__builtin_amdgcn_readfirstlane(lds)) : "memory"); }
DI s16x4 trread(const LAS char* p) { return __builtin_amdgcn_ds_read_tr16_b64_v4i16((LAS s16x4*)p); }
DI bf16x8 cat8(s16x4 a, s16x4 b) { return __builtin_shufflevector(a, b, 0, 1, 2, 3, 4, 5, 6, 7); }
#define MFMA32(a, b, c) __builtin_amdgcn_mfma_f32_32x32x16_bf16(__builtin_bit_cast(bf8, a), __builtin_bit_cast(bf8, b), c, 0, 0, 0)
#define MFMA32K8(a, b, c) __builtin_amdgcn_mfma_f32_32x32x8bf16_1k(a, b, c, 0, 0, 0)
#define MFMA16(a, b, c) __builtin_amdgcn_mfma_f32_16x16x32_bf16(__builtin_bit_cast(bf8, a), __builtin_bit_cast(bf8, b), c, 0, 0, 0)
DI void mfma16_acc(f32x4& c, bf16x8 a, bf16x8 b) { asm volatile("v_mfma_f32_16x16x32_bf16 %0, %1, %2, %0" : "+v"(c) : "v"(a), "v"(b)); }
DI float xhalf_sum(float v) { auto r = __builtin_amdgcn_permlane32_swap(__float_as_uint(v), __float_as_uint(v), false, false); return __uint_as_float(r[0]) + __uint_as_float(r[1]); }
DI float xhalf_max(float v) { auto r = __builtin_amdgcn_permlane32_swap(__float_as_uint(v), __float_as_uint(v), false, false); return fmaxf(__uint_as_float(r[0]), __uint_as_float(r[1])); }
DI float xq_sum(float v) { auto r = __builtin_amdgcn_permlane16_swap(__float_as_uint(v), __float_as_uint(v), false, false); return xhalf_sum(__uint_as_float(r[0]) + __uint_as_float(r[1])); }
typedef __amdgpu_buffer_rsrc_t rsrc_t;
DI rsrc_t mkrsrc(const void* p, unsigned bytes) { return __builtin_amdgcn_make_buffer_rsrc((void*)p, 0, bytes, 0x00020000); }
constexpr int XAUX = 16;
DI bf16x8 ldfrag(rsrc_t r, unsigned voff, unsigned soff) { return __builtin_bit_cast(bf16x8, __builtin_amdgcn_raw_buffer_load_b128(r, voff, soff, XAUX)); }
DI void st16(rsrc_t r, unsigned voff, u32x4 v) { __builtin_amdgcn_raw_buffer_store_b128(v, r, voff, 0, XAUX); }
DI void st8(rsrc_t r, unsigned voff, u32x2 v) { __builtin_amdgcn_raw_buffer_store_b64(v, r, voff, 0, XAUX); }

DI s16x4 ldhalf(rsrc_t r, unsigned voff, unsigned soff) { return __builtin_bit_cast(s16x4, __builtin_amdgcn_raw_buffer_load_b64(r, voff, soff, XAUX)); }
DI void st16s(rsrc_t r, unsigned voff, unsigned soff, u32x4 v) { __builtin_amdgcn_raw_buffer_store_b128(v, r, voff + soff, 0, XAUX); }
DI void st8s(rsrc_t r, unsigned voff, unsigned soff, u32x2 v) { __builtin_amdgcn_raw_buffer_store_b64(v, r, voff, soff, XAUX); }
typedef unsigned long long u64;
typedef __attribute__((address_space(1))) u64 gu64;
#define RLX_AGENT __ATOMIC_RELAXED, __HIP_MEMORY_SCOPE_AGENT
constexpr int FL_EPOCH = 0, FL_W = 64  ;
constexpr int CT_BASE = 512, CT_QKV = 0  , CT_O = 128  , CT_H = 192  , CT_H1 = 256  ;
constexpr unsigned SPIN_CAP = 1u << 17;
DI u64 get_tag(char* ws) {
  u64 e;
  asm volatile("s_load_dwordx2 %0, %1, 0x0\n\ts_waitcnt lgkmcnt(0)" : "=s"(e) : "s"(ws + WS_FL + FL_EPOCH * 8) : "memory");
  return e * 6364136223846793005ull + 1442695040888963407ull;
}
DI gu64* flagp(char* ws, int i) { return (gu64*)(ws + WS_FL) + i; }
DI void signal(char* ws, int i, u64 tag) { __hip_atomic_store(flagp(ws, i), tag, RLX_AGENT); }
DI gu64* ctrp(char* ws, int i) { return (gu64*)(ws + WS_FL) + CT_BASE + i * 16; }
DI void arrive(char* ws, int i) { __hip_atomic_fetch_add(ctrp(ws, i), 1ull, RLX_AGENT); }
DI void await(char* ws, int i, int n, u64 want) {
  gu64* p = ctrp(ws, i);
  const bool on = (int)(threadIdx.x & 63) < n;
#pragma unroll 1
  for (unsigned sp = 0; sp < SPIN_CAP; ++sp) {
    u64 v = want;
    if (on) asm volatile("global_atomic_add_x2 %0, %1, %2, off sc0 sc1\n\ts_waitcnt vmcnt(0)" : "=v"(v) : "v"(p), "v"(0ull) : "memory");
    if (__all(v == want)) break;
  }
  asm volatile("" ::: "memory");
}

DI void lds_post(unsigned addr, unsigned one) { asm volatile("s_mov_b64 exec, 1\n\tds_add_u32 %0, %1\n\ts_mov_b64 exec, -1" :: "v"(addr), "v"(one) : "memory"); }
template <int NAP = 1> DI void lds_wait(const LAS char* p) {
#pragma unroll 1
  for (unsigned sp = 0; sp < (1u << 22) && __builtin_amdgcn_readfirstlane(*(const volatile LAS unsigned*)p) < 4u; ++sp) __builtin_amdgcn_s_sleep(NAP);
  asm volatile("" ::: "memory");
}
#define NAP1 3
#define NAP2 12
constexpr int CTL1 = 133120  , CTL2 = 57344  ;

constexpr int K1_NSLOT = 4, K1_STAGE = 32768;
#define K1_PRE 2
template <int CTRL> DI float dpp_add(float v) { return v + __builtin_bit_cast(float, __builtin_amdgcn_update_dpp(0, __builtin_bit_cast(int, v), CTRL, 0xf, 0xf, true)); }
DI float sum32(float v) {
  v = dpp_add<0x128>(v); v = dpp_add<0x124>(v); v = dpp_add<0x122>(v); v = dpp_add<0x121>(v);
  auto r = __builtin_amdgcn_permlane16_swap(__float_as_uint(v), __float_as_uint(v), false, false); return __uint_as_float(r[0]) + __uint_as_float(r[1]);
}
struct XRow { u32x4 a, b; u32x2 c; };
DI void xrow_load(XRow& v, const u16* x, int row0) {
  const int lane = threadIdx.x & 63;
  const char* p = (const char*)x + (size_t)(row0 + (lane >> 5)) * 1280;
  v.a = *(const u32x4*)(p + (lane & 31) * 16); v.b = *(const u32x4*)(p + 512 + (lane & 31) * 16); v.c = *(const u32x2*)(p + 1024 + (lane & 31) * 8);
}
DI void xrow_publish(const XRow& v, char* ws, int row0, u64 tag) {
  const int lane = threadIdx.x & 63;
  const u32x4 va = v.a, vb = v.b; const u32x2 vc = v.c;
  float e[20];
#pragma unroll
  for (int i = 0; i < 4; ++i) { e[2 * i] = bflo(va[i]); e[2 * i + 1] = bfhi(va[i]); e[8 + 2 * i] = bflo(vb[i]); e[9 + 2 * i] = bfhi(vb[i]); }
  e[16] = bflo(vc[0]); e[17] = bfhi(vc[0]); e[18] = bflo(vc[1]); e[19] = bfhi(vc[1]);
  f32x2 s = {0.f, 0.f};
#pragma unroll
  for (int i = 0; i < 20; i += 2) s += f32x2{e[i], e[i + 1]};
  const float m = sum32(s[0] + s[1]) * (1.f / D);
  f32x2 q = {0.f, 0.f};
#pragma unroll
  for (int i = 0; i < 20; i += 2) { const f32x2 d = f32x2{e[i], e[i + 1]} - m; q += d * d; }
  const float rstd = 1.f / rbf(__builtin_sqrtf(rbf(rbf(sum32(q[0] + q[1]) * (1.f / D)) + 1e-5f)));
  if ((lane & 31) == 0) st16(mkrsrc(ws + WS_XST, S * 16), (row0 + (lane >> 5)) * 16, u32x4{__float_as_uint(rbf(m)), (unsigned)tag, __float_as_uint(rstd), (unsigned)(tag >> 32)});
}
struct F4 { bf16x8 a0, a1, b0, b1; };
template <bool isV> DI void k1_compute(char* ws, char* smem, int bm, int bn, int wid, XRow& xr, u64 tag, u64 t_start) {
  const int lane = threadIdx.x & 63, r = lane & 31, hh = lane >> 5;
  const int wm = wid >> 1, wn = wid & 1;
  f32x16 c00 = f32x16{}, c01 = f32x16{}, c10 = f32x16{}, c11 = f32x16{};
  const int fA = (r >> 1) & 7;
  const int gi = lane >> 4, q = (lane & 15) >> 2, p = lane & 3;
  const LAS char* sm = (const LAS char*)smem;
  const LAS char* pa[2][4]; const LAS char* pb[2][2];
#pragma unroll
  for (int w = 0; w < 2; ++w) {
#pragma unroll
    for (int s = 0; s < 4; ++s) { pa[w][s] = sm + w * 65536 + (wm * 64 + r) * 128 + (((2 * s + hh) ^ fA) << 4); asm volatile("" : "+v"(pa[w][s])); }
#pragma unroll
    for (int jn = 0; jn < 2; ++jn) { pb[w][jn] = sm + w * 65536 + 16384 + (8 * (gi >> 1) + q) * 256 + 32 * (gi & 1) + 8 * p + (((2 * wn + jn) ^ q) << 6); asm volatile("" : "+v"(pb[w][jn])); }
  }
  auto lda = [&](int i, int t) { const int sl = (i >> 2) % K1_NSLOT; return *(const LAS bf16x8*)(pa[sl >> 1][i & 3] + (sl & 1) * K1_STAGE + t * 4096); };
  auto ldb = [&](int i, int jn) { const int sl = (i >> 2) % K1_NSLOT; const LAS char* b = pb[sl >> 1][jn] + (sl & 1) * K1_STAGE + (i & 3) * 4096; return cat8(trread(b), trread(b + 1024)); };
#define K1_MM(c, a, b) asm volatile("v_mfma_f32_32x32x16_bf16 %0, %1, %2, %0" : "+v"(c) : "v"(isV ? a : b), "v"(isV ? b : a))
  const bool isQ = bn < 5; const int tstride = isQ ? QT : KVT;
  const rsrc_t rimg = mkrsrc(ws + (isQ ? WS_Q : WS_KV), H * 64 * KVT);
  unsigned soff[8];
  if (!isV) {
#pragma unroll
    for (int u = 0; u < 8; ++u) {
      const int G = (bn % 5) * 16 + wn * 8 + u, pr = G / 5, g5 = G - pr * 5;
      soff[u] = g5 == 2 ? (2 * pr + hh) * 64 * tstride + 1024 + r * 8 : (2 * pr + (g5 > 2)) * 64 * tstride + (g5 > 2 ? g5 - 3 : g5) * 512 + r * 16 + hh * 8;
      asm volatile("" : "+v"(soff[u]));
    }
  } else {
#pragma unroll
    for (int u = 0; u < 4; ++u) {
      const int n = (bn - 10) * 128 + wn * 64 + u * 16 + (lane & 15), head = n / 20, d = n - head * 20;
      soff[u] = head * 64 * KVT + 1280 + d * 64 + (((lane >> 4) ^ (d >> 2)) & 3) * 16;
      const unsigned c1 = d == 18 ? 0x3F803F80u : 0u, o = d >= 18 ? soff[u] + 128 : H * 64 * KVT;
#pragma unroll
      for (int i = 0; i < 2; ++i) st16s(rimg, o, (bm * 4 + wm * 2 + i) * KVT, u32x4{c1, c1, c1, c1});
      asm volatile("" : "+v"(soff[u]));
    }
  }
  asm volatile("" : "+v"(c00), "+v"(c01), "+v"(c10), "+v"(c11));
  SBAR();
  xrow_publish(xr, ws, blockIdx.x * 8 + wid * 2, tag);
  BAR(); SBAR();
  PADT(3, 15);
  STAMPT(0, 1);
  F4 f[3];
#pragma unroll
  for (int i = 0; i < 2; ++i) { f[i].a0 = lda(i, 0); f[i].b0 = ldb(i, 0); f[i].b1 = ldb(i, 1); f[i].a1 = lda(i, 1); }
  SBAR();
#pragma unroll
  for (int i = 0; i < 40; ++i) {
    F4& x = f[i % 3]; F4& y = f[(i + 2) % 3];
    const bool ld = i + 2 < 40;
    if (ld && (i & 3) == 2) { BAR(); SBAR(); }
    K1_MM(c00, x.a0, x.b0); SBAR(); if (ld) { y.a0 = lda(i + 2, 0); y.b0 = ldb(i + 2, 0); } SBAR();
    K1_MM(c01, x.a0, x.b1); SBAR(); if (ld) { y.b1 = ldb(i + 2, 1); } SBAR();
    K1_MM(c10, x.a1, x.b0); SBAR(); if (ld) { y.a1 = lda(i + 2, 1); } SBAR();
    K1_MM(c11, x.a1, x.b1); SBAR();
  }
  asm volatile("s_nop 11" ::: "memory"); SBAR();
  PADT(6, 22);
  STAMPT(0, 2);
  BAR();
  const LAS float* sMean = (const LAS float*)(smem + K1_NSLOT * K1_STAGE); const LAS float* sRstd = sMean + 128; const LAS float* sCs = sMean + 256;
  if (!isV) {
#pragma unroll
    for (int i = 0; i < 2; ++i) {
      const int tl = wm * 64 + i * 32;
      const float m = sMean[tl + r], rstd = sRstd[tl + r] * (isQ ? QSCALE : 1.f);
      int tile = bm * 4 + wm * 2 + i; if (!isQ) tile = (tile + 64 - KSKEW) & 63;
#pragma unroll
      for (int jn = 0; jn < 2; ++jn) {
        const f32x16& c = i ? (jn ? c11 : c10) : (jn ? c01 : c00);
#pragma unroll
        for (int g = 0; g < 4; ++g) {
          const f32x4 c4 = *(const LAS f32x4*)(sCs + wn * 64 + jn * 32 + 8 * g + 4 * hh);
          u32x2 v;
          v[0] = pk2((c[4 * g] - m * c4[0]) * rstd, (c[4 * g + 1] - m * c4[1]) * rstd);
          v[1] = pk2((c[4 * g + 2] - m * c4[2]) * rstd, (c[4 * g + 3] - m * c4[3]) * rstd);
          st8s(rimg, soff[jn * 4 + g], tile * tstride, v);
        }
      }
    }
  } else {
#pragma unroll
    for (int i = 0; i < 2; ++i) {
      const int tile = bm * 4 + wm * 2 + i;
      f32x4 m4[4], r4[4];
#pragma unroll
      for (int g = 0; g < 4; ++g) { const int tl = wm * 64 + i * 32 + 8 * g + 4 * hh; m4[g] = *(const LAS f32x4*)(sMean + tl); r4[g] = *(const LAS f32x4*)(sRstd + tl); }
#pragma unroll
      for (int jn = 0; jn < 2; ++jn) {
        const f32x16& c = i ? (jn ? c11 : c10) : (jn ? c01 : c00);
        const float cs = sCs[wn * 64 + jn * 32 + r];
        unsigned v[8];
#pragma unroll
        for (int g = 0; g < 4; ++g) {
          v[2 * g] = pk2((c[4 * g] - m4[g][0] * cs) * r4[g][0], (c[4 * g + 1] - m4[g][1] * cs) * r4[g][1]);
          v[2 * g + 1] = pk2((c[4 * g + 2] - m4[g][2] * cs) * r4[g][2], (c[4 * g + 3] - m4[g][3] * cs) * r4[g][3]);
        }
        u32x4 lo, hi;
#pragma unroll
        for (int e = 0; e < 4; ++e) { const auto t = __builtin_amdgcn_permlane16_swap(v[e], v[4 + e], false, false); lo[e] = t[0]; hi[e] = t[1]; }
        st16s(rimg, soff[2 * jn], tile * KVT, lo); st16s(rimg, soff[2 * jn + 1], tile * KVT, hi);
      }
    }
  }
}
struct Args { const u16 *x, *wqkv, *wo, *w1, *b1, *w2, *b2; u16* out; char* ws; };
constexpr int LDS_RT = 143360;
DI int g0of(int c4) { return (c4 * 27) >> 2; }
DI void retile_dma(const Args& a, unsigned lds, int hw) {
  const int lane = threadIdx.x & 63, b = blockIdx.x;
  if (b >= 40 && b < 148) {
    const int jb = b - 40;
#pragma unroll
    for (int i = 0; i < 5; ++i) {
      const int p = hw * 5 + i, ks = (jb & 3) * 5 + (p >> 2), t = (p >> 1) & 1;
      const int tile = ks < 16 ? 5 * (ks >> 1) + 2 * (ks & 1) + t : 5 * (2 * (ks - 16) + t) + 4;
      glds16((lane >> 3) * (F * 2) + (lane & 7) * 16, (const char*)a.w1 + (size_t)(tile * 16 + (p & 1) * 8) * (F * 2) + (jb >> 2) * 128, lds + p * 1024);
    }
  } else {
    const int jb = b < 40 ? b : b - 148, ks = jb >> 1, t = hw >> 1;
    int row0, skip = 0;
    if (b < 40) { if (ks < 16) row0 = (2 * ks + (hw & 1)) * 20 + 8 * t; else { row0 = ((ks - 16) * 8 + 2 * hw) * 20 + 16; skip = 16; } }
    else { const int sg = 2 * (ks - 48) + t; row0 = (ks < 48 ? g0of(ks / 3) + 2 * (ks % 3) + t : g0of(sg + sg / 3 + 1) + 6) * 16 + (hw & 1) * 8; }
    const char* src = (const char*)(b < 40 ? a.wo : a.w2) + (size_t)row0 * (D * 2) + (jb & 1) * 640;
#pragma unroll
    for (int i = 0; i < 5; ++i) { const int idx = i * 64 + lane, row = idx / 40; glds16((row + (row >> 2) * skip) * (D * 2) + (idx - row * 40) * 16, src, lds + (hw * 5 + i) * 1024); }
  }
}
DI void retile_store(char* ws, const LAS char* img, int hw) {
  const int lane = threadIdx.x & 63, b = blockIdx.x;
  const bool is1 = b >= 40 && b < 148;
  const int rs = is1 ? 128 : 640;
  const LAS char* p = img + (lane >> 2) * rs + (lane & 3) * 8 + (is1 ? hw * 32 : hw * 160);
  const rsrc_t rw = mkrsrc(ws + (b < 40 ? WS_WO : (is1 ? WS_W1 : WS_W2)), D * F * 2);
  const int jb = b < 40 ? b : (is1 ? b - 40 : b - 148);
  const int d0 = is1 ? ((jb >> 2) * 4 + hw) * 20 + (jb & 3) * 5 : (((jb & 1) * 4 + hw) * (b < 40 ? 20 : 54) + (jb >> 1)) * 5;
  float cs = 0.f;
#pragma unroll
  for (int k = 0; k < 5; ++k) {
    const LAS char* q = p + k * (is1 ? 4096 : 32);
    const u32x4 v = __builtin_bit_cast(u32x4, cat8(trread(q), trread(q + 16 * rs)));
    st16(rw, (d0 + k) * 1024 + lane * 16, v);
#pragma unroll
    for (int j = 0; j < 4; ++j) { asm volatile("v_add_f32 %0, %0, %1" : "+v"(cs) : "v"(bflo(v[j]))); asm volatile("v_add_f32 %0, %0, %1" : "+v"(cs) : "v"(bfhi(v[j]))); }
  }
  if (is1) { cs = xq_sum(cs); if (lane < 16) __builtin_amdgcn_raw_buffer_store_b32(__float_as_uint(cs), mkrsrc(ws + WS_CS1, 4 * 1792 * 4), ((jb & 3) * 1792 + 16 * ((jb >> 2) * 4 + hw) + lane) * 4, 0, XAUX); }
}
DI void k1_helper(const Args& a, const u16* __restrict__ x, const u16* __restrict__ w, char* ws, char* smem, int bm, int bn, int rot, int hw, u64& tag) {
  const int lane = threadIdx.x & 63;
  const unsigned lds0 = (unsigned)(uintptr_t)smem;
  const unsigned voffA0 = (lane >> 3) * 1280 + (((lane & 7) ^ (lane >> 4)) << 4);
  const unsigned voffA1 = (lane >> 3) * 1280 + (((lane & 7) ^ (4 + (lane >> 4))) << 4);
  const unsigned voffB = (lane >> 4) * 3840 + (((((lane & 15) >> 2) ^ (lane >> 4)) * 4 + (lane & 3)) << 4);
  const char* xa = (const char*)x + (size_t)(bm * 128 + hw * 32) * 1280;
  const char* wb = (const char*)w + (size_t)(hw * 16) * 3840 + bn * 256;
  auto issue = [&](int st) {
    int ks = st + rot; if (ks >= 10) ks -= 10;
    const unsigned dst = lds0 + (st % K1_NSLOT) * K1_STAGE + hw * 4096;
#pragma unroll
    for (int i = 0; i < 4; ++i) glds16((i & 1) ? voffA1 : voffA0, xa + i * 8 * 1280 + ks * 128, dst + i * 1024);
#pragma unroll
    for (int i = 0; i < 4; ++i) glds16(voffB, wb + (size_t)(ks * 64 + i * 4) * 3840, dst + 16384 + i * 1024);
  };
  issue(0); issue(1);
  if (K1_PRE == 3) issue(2);
  WAITV(8 * (K1_PRE - 1)); BAR();
  if (K1_PRE == 2) issue(2);
  WAITV(8); BAR();
  issue(3);
  tag = get_tag(ws);
#pragma unroll 1
  for (int st = 1; st < 7; ++st) { WAITV(8); BAR(); issue(st + 3); }
  retile_dma(a, lds0 + LDS_RT, hw);
  WAITV(13); BAR();
  WAITV(5); BAR();
  const rsrc_t rs = mkrsrc(ws + WS_XST, S * 16 + 16384);
  const unsigned go = lane < 32 ? (bm * 128 + hw * 32 + lane) * 16 : S * 16 + (bn * 64 + hw * 16 + (lane & 15)) * 16;
  u32x4 rec;
#pragma unroll 1
  for (unsigned sp = 0; sp < SPIN_CAP; ++sp) {
    asm volatile("" ::: "memory");
    rec = __builtin_amdgcn_raw_buffer_load_b128(rs, go, 0, XAUX);
    if (__all(rec[1] == (unsigned)tag && rec[3] == (unsigned)(tag >> 32))) break;
    __builtin_amdgcn_s_sleep(1);
  }
  LAS unsigned* st = (LAS unsigned*)(smem + K1_NSLOT * K1_STAGE);
  if (lane < 32) { st[hw * 32 + lane] = rec[0]; st[128 + hw * 32 + lane] = rec[2]; }
  else if (lane < 48) { st[256 + hw * 32 + 2 * (lane & 15)] = rec[0]; st[257 + hw * 32 + 2 * (lane & 15)] = rec[2]; }
  LGKM0(); BAR();
}
DI void k1_colsum(const u16* __restrict__ x, const u16* __restrict__ w, char* ws, char* smem, int u, int wid, u64 tag) {
  const int lane = threadIdx.x & 63;
  if (u < 15) {
    if (wid == 0 && lane < 8) __hip_atomic_store(ctrp(ws, CT_QKV + u * 8 + lane), tag, RLX_AGENT);
    const char* p = (const char*)w + (size_t)(wid * 80 + (lane >> 4)) * 3840 + u * 256 + (lane & 15) * 16;
    u32x4 v[20];
#pragma unroll
    for (int i = 0; i < 20; ++i) v[i] = *(const u32x4*)(p + (size_t)i * 4 * 3840);
    float a[8];
#pragma unroll
    for (int e = 0; e < 8; ++e) a[e] = 0.f;
#pragma unroll
    for (int i = 0; i < 20; ++i)
#pragma unroll
      for (int e = 0; e < 4; ++e) { a[2 * e] += bflo(v[i][e]); a[2 * e + 1] += bfhi(v[i][e]); }
    LAS float* part = (LAS float*)smem;
#pragma unroll
    for (int e = 0; e < 8; ++e) { a[e] = xq_sum(a[e]); if (lane < 16) part[wid * 128 + lane * 8 + e] = a[e]; }
    LGKM0(); BAR();
    if (wid == 0) {
      WAITV(0);
      float c0 = 0.f, c1 = 0.f;
#pragma unroll
      for (int k = 0; k < 8; ++k) { c0 += part[k * 128 + 2 * lane]; c1 += part[k * 128 + 2 * lane + 1]; }
      st16(mkrsrc(ws + WS_CSQ, 16384), (u * 64 + lane) * 16, u32x4{__float_as_uint(c0), (unsigned)tag, __float_as_uint(c1), (unsigned)(tag >> 32)});
    }
  }
}

constexpr int CH_TILES = 8, CH_BYTES = CH_TILES * KVT, NCH = 64 / CH_TILES, CH_PIECES = CH_BYTES / 1024;
#define EX(v) __builtin_amdgcn_exp2f(v)
#define PF(p) __builtin_bit_cast(bf16x8, p)
DI float rowmax16(const f32x16& s) {
  float m = s[0];
#pragma unroll
  for (int e = 1; e < 16; ++e) m = fmaxf(m, s[e]);
  return xhalf_max(m);
}
DI s16x4 shift_from(const f32x16& s, s16x4 q2, int hh) {
  const float m = -rowmax16(s);
  const unsigned mh = pk2(m, 0.f) & 0xffffu, ml = pk2(m - bflo(mh), 0.f) & 0xffffu;
  return hh ? s16x4{(short)mh, (short)ml, 0, 0} : q2;
}
DI bool attn_unsafe(const f32x16& o, int hh) {
  const float l = hh ? o[8] : o[12];
  float t = 0.f;
#pragma unroll
  for (int e = 0; e < 12; e += 4) t += (fabsf(o[e]) + fabsf(o[e + 1])) + (fabsf(o[e + 2]) + fabsf(o[e + 3]));
  return __any(!(l >= 1e-20f && l <= 1e30f && t <= 3e38f));
}
DI f32x16 attn_online(rsrc_t kv, bf16x8 q1, s16x4 q2, int lane, int k2off, int voff) {
  if (lane >= 32) q2 = s16x4{0, 0, 0, 0};
  float m = -INFINITY; f32x16 o = f32x16{};
#pragma unroll 1
  for (int t = 0; t < 64; ++t) {
    const int kt = ((t + 64 - KSKEW) & 63) * KVT, vt = t * KVT;
    f32x16 s = MFMA32(ldfrag(kv, lane * 16, kt), q1, f32x16{}); s = MFMA32K8(ldhalf(kv, k2off, kt), q2, s);
    const float mn = fmaxf(m, rowmax16(s)), al = EX(m - mn); m = mn;
#pragma unroll
    for (int e = 0; e < 16; ++e) { o[e] *= al; s[e] = EX(s[e] - mn); }
    const u32x4 p0 = {pk2(s[0], s[1]), pk2(s[2], s[3]), pk2(s[4], s[5]), pk2(s[6], s[7])};
    const u32x4 p1 = {pk2(s[8], s[9]), pk2(s[10], s[11]), pk2(s[12], s[13]), pk2(s[14], s[15])};
    o = MFMA32(ldfrag(kv, voff, vt), PF(p0), o); o = MFMA32(ldfrag(kv, voff ^ 16, vt), PF(p1), o);
  }
  return o;
}
DI void attn_compute(char* ws, char* smem, int head, int qc, int wid) {
  const int lane = threadIdx.x & 63, r = lane & 31, hh = lane >> 5;
  const rsrc_t kv = mkrsrc(ws + WS_KV + (size_t)head * 64 * KVT, 64 * KVT);
  const int qt = qc * 8 + wid * 2;
  const rsrc_t rq = mkrsrc(ws + WS_Q + (size_t)(head * 64 + qt) * QT, 2 * QT);
  const int k2off = hh ? 2560 : 1024 + r * 8;
  const int vr = r < 21 ? r : (r == 24 ? 20 : 21), voff = 1280 + vr * 64 + (((2 * hh) ^ (vr >> 2)) & 3) * 16;
  const bf16x8 qa1 = ldfrag(rq, lane * 16, 0), qb1 = ldfrag(rq, lane * 16, QT);
  s16x4 qa2 = ldhalf(rq, 1024 + r * 8, 0), qb2 = ldhalf(rq, 1024 + r * 8, QT);
  if (hh) { qa2 = s16x4{0, 0, 0, 0}; qb2 = s16x4{0, 0, 0, 0}; }
  unsigned ooff0 = ((qt * 2 + (r >> 4)) * 20 + (head >> 1)) * 1024 + (((head & 1) * 2 + hh) * 16 + (r & 15)) * 16;
  unsigned ooff1 = hh ? S * D * 2 : ((qt * 2 + (r >> 4)) * 20 + 16 + (head >> 3)) * 1024 + ((head & 7) * 16 + (r & 15)) * 8;
  asm volatile("" : "+v"(ooff0), "+v"(ooff1));
  bf16x8 k1 = ldfrag(kv, lane * 16, 63 * KVT); s16x4 k2 = ldhalf(kv, k2off, 63 * KVT);
  f32x16 oa, ob;
  { f32x16 s = MFMA32(k1, qa1, f32x16{}); s = MFMA32K8(k2, qa2, s); qa2 = shift_from(s, qa2, hh);
    s = MFMA32(k1, qb1, f32x16{}); s = MFMA32K8(k2, qb2, s); qb2 = shift_from(s, qb2, hh); }
  const unsigned lds0 = (unsigned)(uintptr_t)smem;
  unsigned ctl = lds0 + CTL2;
  STAMPT(1, 1);
  asm volatile(
    ".macro AT_E r\n v_exp_f32 v[\\r], v[\\r]\n v_exp_f32 v[\\r+1], v[\\r+1]\n.endm\n"
    ".macro AT_C d, r\n v_cvt_pk_bf16_f32 v[\\d], v[\\r], v[\\r+1]\n.endm\n"
    ".macro AT_H sc, sn, pc, pp, o, x, xk2, q1, q2, y, yk2, off, mode\n"
    " v_mfma_f32_32x32x16_bf16 v[\\o:\\o+15], v[\\x+4:\\x+7], v[\\pp:\\pp+3], v[\\o:\\o+15]\n"
    " .if \\mode & 1\n ds_read_b128 v[\\y+4:\\y+7], v130 offset:\\off\n .endif\n"
    " AT_C \\pp+6, \\sn+12\n AT_C \\pp+7, \\sn+14\n AT_E \\sc\n AT_E \\sc+2\n"
    " .if (\\mode & 16) == 0\n v_mfma_f32_32x32x16_bf16 v[\\sn:\\sn+15], v[\\x:\\x+3], v[\\q1:\\q1+3], 0\n .endif\n"
    " .if \\mode & 1\n ds_read_b128 v[\\y:\\y+3], v128 offset:\\off\n .endif\n"
    " .if \\mode & 4\n ds_read_b32 v133, v131 offset:\\off\n .endif\n"
    " AT_E \\sc+4\n AT_C \\pc, \\sc\n AT_E \\sc+6\n AT_C \\pc+1, \\sc+2\n"
    " .if (\\mode & 16) == 0\n v_mfma_f32_32x32x8_bf16 v[\\sn:\\sn+15], v[\\xk2:\\xk2+1], v[\\q2:\\q2+1], v[\\sn:\\sn+15]\n .endif\n"
    " AT_E \\sc+8\n AT_C \\pc+2, \\sc+4\n AT_E \\sc+10\n AT_C \\pc+3, \\sc+6\n"
    " v_mfma_f32_32x32x16_bf16 v[\\o:\\o+15], v[\\x+8:\\x+11], v[\\pp+4:\\pp+7], v[\\o:\\o+15]\n"
    " .if \\mode & 1\n ds_read_b64 v[\\yk2:\\yk2+1], v129 offset:\\off\n ds_read_b128 v[\\y+8:\\y+11], v134 offset:\\off\n .endif\n"
    " .if \\mode & 2\n s_waitcnt lgkmcnt(0)\n .endif\n"
    " .if \\mode & 4\n s_mov_b64 exec, 1\n ds_add_u32 v131, v132 offset:\\off+60\n s_mov_b64 exec, -1\n v_cmp_gt_u32 vcc, 4, v133\n .endif\n"
    " AT_E \\sc+12\n AT_C \\pc+4, \\sc+8\n AT_E \\sc+14\n AT_C \\pc+5, \\sc+10\n"
    ".endm\n"
    ".macro AT_A x, xk2, y, yk2, t\n AT_H 56, 40, 80, 72, 8, \\x, \\xk2, 116, 124, \\y, \\yk2, (\\t)*2688, 1\n.endm\n"
    ".macro AT_B x, xk2, off, mode\n AT_H 40, 56, 72, 80, 24, \\x, \\xk2, 120, 126, 0, 0, \\off, \\mode\n.endm\n"
    ".macro AT_2 t\n AT_A 104, 102, 88, 100, \\t\n AT_B 104, 102, 0, 2\n AT_A 88, 100, 104, 102, \\t+1\n AT_B 88, 100, 0, 2\n.endm\n"
    ".macro AT_SPIN off, back\n"
    "1: s_cmp_eq_u32 s41, 0\n s_cbranch_scc1 \\back\n s_sub_u32 s41, s41, 1\n s_sleep 1\n ds_read_b32 v133, v131 offset:\\off\n s_waitcnt lgkmcnt(0)\n v_cmp_gt_u32 vcc, 4, v133\n s_nop 4\n s_cbranch_vccnz 1b\n s_branch \\back\n"
    ".endm\n"
    " s_nop 1\n"
    " v_mfma_f32_32x32x16_bf16 v[40:55], v[88:91], v[116:119], 0\n"
    " v_mfma_f32_32x32x8_bf16 v[40:55], v[100:101], v[124:125], v[40:55]\n"
    " v_mov_b32 v132, 1\n"
    " .irp r, 8,9,10,11,12,13,14,15,16,17,18,19,20,21,22,23,24,25,26,27,28,29,30,31,32,33,34,35,36,37,38,39,68,69,70,71,80,81,82,83,84,85,92,93,94,95,96,97,98,99\n v_mov_b32 v[\\r], 0\n .endr\n"
    " s_mov_b32 s40, 4\n s_mov_b32 s41, 0x400000\n"
    ".Lat_w0: s_sub_u32 s41, s41, 1\n s_cbranch_scc1 .Lat_w1\n ds_read_b32 v133, v131\n s_waitcnt lgkmcnt(0)\n v_cmp_gt_u32 vcc, 4, v133\n s_nop 4\n s_cbranch_vccnz .Lat_w0\n"
    ".Lat_w1: s_mov_b32 s41, 0x400000\n"
    " ds_read_b128 v[108:111], v130\n ds_read_b128 v[104:107], v128\n ds_read_b64 v[102:103], v129\n ds_read_b128 v[112:115], v134\n"
    " s_branch .Lat_mid\n"
    ".Lat_s1: AT_SPIN 4, .Lat_b1\n"
    ".Lat_s2: AT_SPIN 8, .Lat_b2\n"
    " .p2align 6\n"
    ".Lat_loop:\n"
    " AT_A 88, 100, 104, 102, 0\n"
    ".Lat_mid:\n"
    " AT_B 88, 100, 0, 2\n"
    " AT_2 1\n AT_2 3\n AT_2 5\n"
    " AT_A 104, 102, 88, 100, 7\n AT_B 104, 102, 4, 6\n"
    " s_cbranch_vccnz .Lat_s1\n"
    ".Lat_b1:\n"
    " AT_A 88, 100, 104, 102, 8\n AT_B 88, 100, 0, 2\n"
    " AT_2 9\n AT_2 11\n AT_2 13\n"
    " AT_A 104, 102, 88, 100, 15\n AT_B 104, 102, 8, 6\n"
    " s_cbranch_vccnz .Lat_s2\n"
    ".Lat_b2:\n"
    " v_add_u32 v131, 8, v131\n"
    " s_sub_u32 s40, s40, 1\n s_cmp_lg_u32 s40, 0\n s_cbranch_scc1 .Lat_loop\n"
    " AT_H 56, 40, 80, 72, 8, 88, 100, 116, 124, 0, 0, 0, 16\n"
    " AT_C 86, 68\n AT_C 87, 70\n"
    " v_mfma_f32_32x32x16_bf16 v[24:39], v[92:95], v[80:83], v[24:39]\n s_nop 0\n"
    " v_mfma_f32_32x32x16_bf16 v[24:39], v[96:99], v[84:87], v[24:39]\n"
    " s_nop 12\n"
    : "=&{v[8:23]}"(oa), "=&{v[24:39]}"(ob), "+{v131}"(ctl), "+{v[88:91]}"(k1), "+{v[100:101]}"(k2)
    : "{v[116:119]}"(qa1), "{v[120:123]}"(qb1), "{v[124:125]}"(qa2), "{v[126:127]}"(qb2),
      "{v128}"(lds0 + lane * 16), "{v129}"(lds0 + k2off), "{v130}"(lds0 + voff), "{v134}"(lds0 + (voff ^ 16))
    : "memory", "vcc", "scc", "s40", "s41", "v40","v41","v42","v43","v44","v45","v46","v47","v48","v49","v50","v51","v52","v53","v54","v55","v56","v57","v58","v59","v60","v61","v62","v63",
      "v64","v65","v66","v67","v68","v69","v70","v71","v72","v73","v74","v75","v76","v77","v78","v79","v80","v81","v82","v83","v84","v85","v86","v87","v92","v93","v94","v95","v96","v97","v98","v99",
      "v102","v103","v104","v105","v106","v107","v108","v109","v110","v111","v112","v113","v114","v115","v132","v133");
  STAMPT(1, 2);
  const bool fa = attn_unsafe(oa, hh), fb = attn_unsafe(ob, hh);
  if (__builtin_expect(fa, 0)) oa = attn_online(kv, qa1, qa2, lane, k2off, voff);
  if (__builtin_expect(fb, 0)) ob = attn_online(kv, qb1, qb2, lane, k2off, voff);
  const rsrc_t ro = mkrsrc(ws + WS_O, S * D * 2);
#pragma unroll
  for (int u = 0; u < 2; ++u) {
    const f32x16& o = u ? ob : oa;
    const float il = __builtin_amdgcn_rcpf(hh ? o[8] : o[12]);
    st16(ro, ooff0 + u * 40960, u32x4{pk2(o[0] * il, o[1] * il), pk2(o[2] * il, o[3] * il), pk2(o[4] * il, o[5] * il), pk2(o[6] * il, o[7] * il)});
    st8s(ro, ooff1, u * 40960, u32x2{pk2(o[8] * il, o[9] * il), pk2(o[10] * il, o[11] * il)});
  }
  STAMPT(1, 3);
  WAITV(0);
  if (lane < 2) arrive(ws, CT_O + (qc * 4 + wid) * 2 + lane);
  STAMPT(1, 4);
}

template <int NC> struct GS { f32x4 acc[2][NC]; };
template <int NC, int NKS, int NFULL, int PD, int G, int SSTRIDE, int RING, bool BARS, class Mid> DI void gemm_run(GS<NC>& g, const LAS char* wl, rsrc_t ra, unsigned va0, unsigned va1, unsigned astep, Mid mid) {
  bf16x8 af[PD][2], bf[2][NC];
  const unsigned l8 = (threadIdx.x & 63) * 8;
  auto lda = [&](int s, int i) { const unsigned v = i ? va1 : va0; return s < NFULL ? ldfrag(ra, v, s * astep) : cat8(ldhalf(ra, v - l8, s * astep), ldhalf(ra, v - l8 + 512, s * astep)); };
#pragma unroll
  for (int s = 0; s < G; ++s) { af[s][0] = lda(s, 0); af[s][1] = lda(s, 1); SBAR(); }
  const LAS char* wb[3] = {wl, wl + 61440, wl + 122880};
  asm volatile("" : "+v"(wb[0]), "+v"(wb[1]), "+v"(wb[2]));
  auto rdw = [&](int off) { return *(const LAS bf16x8*)(wb[off / 61440] + off % 61440); };
  mid(-1);
#pragma unroll
  for (int j = 0; j < NC; ++j) bf[0][j] = rdw(j * 1024);
  int nl = G;
#pragma unroll
  for (int s = 0; s < NKS; ++s) {
    LGKM0();
    if (BARS && (s + 1) % 4 == 0 && s + 1 < NKS) BAR();
    mid(s);
    SBAR();
    const int lim = s + PD < NKS ? s + PD : NKS;
    const int l0 = nl < lim ? nl : -1, l1 = (nl + 1 < lim) ? nl + 1 : -1;
    nl = l1 >= 0 ? nl + 2 : (l0 >= 0 ? nl + 1 : nl);
#pragma unroll
    for (int m = 0; m < 2 * NC; ++m) {
      mfma16_acc(g.acc[m & 1][m >> 1], bf[s & 1][m >> 1], af[s % PD][m & 1]); SBAR();
      if (s + 1 < NKS && m < NC) { bf[(s + 1) & 1][m] = rdw(((s + 1) % RING) * SSTRIDE + m * 1024); SBAR(); }
      if (l0 >= 0 && (m == NC || m == NC + 1)) { af[l0 % PD][m - NC] = lda(l0, m - NC); SBAR(); }
      if (l1 >= 0 && (m == NC + 2 || m == NC + 3)) { af[l1 % PD][m - NC - 2] = lda(l1, m - NC - 2); SBAR(); }
    }
  }
  asm volatile("s_nop 7" ::: "memory"); SBAR();
}
template <int NC> DI void pair_reduce(GS<NC>& g, char* smem, int wid) {
  const int lane = threadIdx.x & 63;
  LAS f32x4* red = (LAS f32x4*)(smem + 143360) + lane;
#pragma unroll
  for (int j = 0; j < NC; ++j) red[(wid * NC + j) * 64] = g.acc[1][j];
  LGKM0(); BAR();
#pragma unroll
  for (int j = 0; j < NC; ++j) g.acc[0][j] += red[((wid ^ 2) * NC + j) * 64];
}
DI f32x4 unpack4(u32x2 v) { return f32x4{bflo(v[0]), bfhi(v[0]), bflo(v[1]), bfhi(v[1])}; }
DI u32x2 pack4(f32x4 v) { return u32x2{pk2(v[0], v[1]), pk2(v[2], v[3])}; }
template <int NC> DI void store_tiles(rsrc_t r, unsigned o16, unsigned o8, const u32x2 (&v)[NC]) {
#pragma unroll
  for (int j = 0; j + 1 < NC; j += 2) st16(r, o16 + j * 512, u32x4{v[j][0], v[j][1], v[j + 1][0], v[j + 1][1]});
  st8(r, o8, v[NC - 1]);
}

#define P4_PD 8
#define P5_PD 8
#define GATE 3
#define PACE 1
constexpr int LDS_WO = 61440, LDS_CB = 162816, LDS_RS = 161792, LDS_BYTES = 163840;
constexpr int CODE_BYTES = 45056;

struct Pre { u32x2 x[5], b2[5]; };
DI void post_prefetch(Pre& p, const Args& a, int wid) {
  const int lane = threadIdx.x & 63, b = blockIdx.x, c = b >> 5;
  const int tok = (((b & 7) * 4 + ((b >> 3) & 3)) * 4 + (wid & 1) * 2 + (wid >> 1)) * 16 + (lane & 15);
#pragma unroll
  for (int j = 0; j < 5; ++j) { p.x[j] = *(const u32x2*)(a.x + (size_t)tok * D + c * 80 + j * 16 + (lane >> 4) * 4); p.b2[j] = *(const u32x2*)(a.b2 + c * 80 + j * 16 + (lane >> 4) * 4); }
}
DI void post_compute(const Args& a, char* smem, int wid, const Pre& pre, u64 tag, u64 t_start) {
  char* ws = a.ws;
  const int lane = threadIdx.x & 63, b = blockIdx.x;
  const int grp = (b & 7) * 4 + ((b >> 3) & 3), c = b >> 5, rh = wid & 1, kh = wid >> 1;
  const int rt = grp * 4 + rh * 2 + kh, tok = rt * 16 + (lane & 15);
  u32x2 hv[5];
  {
    GS<5> g;
#pragma unroll
    for (int j = 0; j < 5; ++j) { g.acc[0][j] = unpack4(pre.x[j]); g.acc[1][j] = f32x4{}; }
#pragma unroll
    for (int j = 0; j < 5; ++j) asm volatile("" : "+v"(g.acc[0][j]), "+v"(g.acc[1][j]));
    const rsrc_t ra = mkrsrc(ws + WS_O, S * D * 2);
    const unsigned va0 = (rt * 20 + kh) * 1024 + lane * 16, va1 = ((rt ^ 1) * 20 + kh) * 1024 + lane * 16;
    unsigned ho16 = (rt * 20 + 2 * c) * 1024 + lane * 16, ho8 = (rt * 20 + 16) * 1024 + c * 512 + lane * 8, so8 = lane < 16 ? (rt * 8 + c) * 128 + lane * 8 : S * 64;
    asm volatile("" : "+v"(ho16), "+v"(ho8), "+v"(so8));
    await(ws, CT_O + grp * 2 + rh, 1, tag + 32);
    STAMPT(2, 0);
    gemm_run<5, 10, 8, 10, GATE, 10240, 10, false>(g, (const LAS char*)smem + LDS_WO + kh * 5120 + lane * 16, ra, va0, va1, 2048,
                                                [&](int s) { if (s == 9) BAR(); });
    STAMPT(2, 1);
    pair_reduce<5>(g, smem, wid);
#pragma unroll
    for (int j = 0; j < 5; ++j) hv[j] = pack4(g.acc[0][j]);
    store_tiles<5>(mkrsrc(ws + WS_H, S * D * 2), ho16, ho8, hv);
    f32x4 s1 = g.acc[0][0], s2 = g.acc[0][0] * g.acc[0][0];
#pragma unroll
    for (int j = 1; j < 5; ++j) { s1 += g.acc[0][j]; s2 += g.acc[0][j] * g.acc[0][j]; }
    const float t1 = xq_sum((s1[0] + s1[1]) + (s1[2] + s1[3])), t2 = xq_sum((s2[0] + s2[1]) + (s2[2] + s2[3]));
    st8(mkrsrc(ws + WS_ST, S * 64), so8, u32x2{__float_as_uint(t1), __float_as_uint(t2)});
    STAMPT(2, 2);
    WAITV(0);
    if (lane == 0) arrive(ws, CT_H + (rt >> 1));
    STAMPT(2, 3);
  }
  {
    const int g4 = (b & 7) * 2 + ((b >> 4) & 1), c4 = ((b >> 5) << 1) | ((b >> 3) & 1);
    GS<7> g;
#pragma unroll
    for (int j = 0; j < 7; ++j) { g.acc[0][j] = f32x4{}; g.acc[1][j] = f32x4{}; asm volatile("" : "+v"(g.acc[0][j]), "+v"(g.acc[1][j])); }
    const rsrc_t ra = mkrsrc(ws + WS_H, S * D * 2);
    const int rt0 = g4 * 8 + wid * 2;
    const unsigned va0 = rt0 * 20480 + lane * 16;
    PADT(4, 70);
    f32x4 cs[7], bv[7]; f32x2 rs[2];
    const LAS f32x4* cb = (const LAS f32x4*)(smem + LDS_CB) + (lane >> 4);
    unsigned go16 = (rt0 * 54 + 3 * c4) * 1024 + lane * 16, go8 = (c4 & 3) ? (rt0 * 54 + 48) * 1024 + (c4 - 1 - (c4 >> 2)) * 512 + lane * 8 : S * F * 2;
    asm volatile("" : "+v"(go16), "+v"(go8));
    await(ws, CT_H + (rt0 >> 1), 1, tag + 16);
    STAMPT(3, 0);
    gemm_run<7, 20, 16, P4_PD, GATE, 7168, 20, false>(g, (const LAS char*)smem + lane * 16, ra, va0, va0 + 20480, 1024, [&](int s) {
      if (s == -1) BAR();
      if (s == 18) {
        BAR();
#pragma unroll
        for (int j = 0; j < 7; ++j) { cs[j] = cb[j * 4]; bv[j] = cb[28 + j * 4]; }
#pragma unroll
        for (int i = 0; i < 2; ++i) rs[i] = *(const LAS f32x2*)(smem + LDS_RS + (wid * 32 + i * 16 + (lane & 15)) * 8);
      }
      if (s == 19) BAR();
    });
    STAMPT(3, 1);
    const rsrc_t ro = mkrsrc(ws + WS_H1, S * F * 2);
#pragma unroll
    for (int i = 0; i < 2; ++i) {
      const float mr = rs[i][0], rstd = rs[i][1];
      f32x4 v[7], t[7];
#pragma unroll
      for (int j = 0; j < 7; ++j) t[j] = cs[j] * mr + bv[j];
      SBAR();
#pragma unroll
      for (int j = 0; j < 7; ++j) v[j] = g.acc[i][j] * rstd + t[j];
      SBAR();
#pragma unroll
      for (int j = 0; j < 7; ++j) t[j] = v[j] * v[j];
      SBAR();
#pragma unroll
      for (int j = 0; j < 7; ++j) t[j] = t[j] * -0.1029432397f + -2.302208198f;
      SBAR();
#pragma unroll
      for (int j = 0; j < 7; ++j) t[j] = t[j] * v[j];
      SBAR();
#pragma unroll
      for (int j = 0; j < 7; ++j) t[j] = f32x4{EX(t[j][0]), EX(t[j][1]), EX(t[j][2]), EX(t[j][3])};
      SBAR();
#pragma unroll
      for (int j = 0; j < 7; ++j) t[j] = t[j] + 1.f;
      SBAR();
#pragma unroll
      for (int j = 0; j < 7; ++j) t[j] = f32x4{__builtin_amdgcn_rcpf(t[j][0]), __builtin_amdgcn_rcpf(t[j][1]), __builtin_amdgcn_rcpf(t[j][2]), __builtin_amdgcn_rcpf(t[j][3])};
      SBAR();
#pragma unroll
      for (int j = 0; j < 7; ++j) t[j] = t[j] * v[j];
      SBAR();
      u32x2 h1[7];
#pragma unroll
      for (int j = 0; j < 7; ++j) h1[j] = pack4(t[j]);
      store_tiles<7>(ro, go16 + i * 55296, go8 + i * 55296, h1);
      SBAR();
    }
    STAMPT(3, 2);
    WAITV(0);
    if (lane == 0) arrive(ws, CT_H1 + (rt0 >> 1));
    STAMPT(3, 3);
  }
  {
    GS<5> g;
#pragma unroll
    for (int j = 0; j < 5; ++j) { g.acc[0][j] = unpack4(pre.b2[j]) + unpack4(hv[j]); g.acc[1][j] = f32x4{}; }
#pragma unroll
    for (int j = 0; j < 5; ++j) asm volatile("" : "+v"(g.acc[0][j]), "+v"(g.acc[1][j]));
    const rsrc_t ra = mkrsrc(ws + WS_H1, S * F * 2);
    const unsigned va0 = (rt * 54 + kh) * 1024 + lane * 16, va1 = ((rt ^ 1) * 54 + kh) * 1024 + lane * 16;
    PADT(5, 80);
    await(ws, CT_H1 + (rt >> 1), 1, tag + 16);
    STAMPT(4, 0);
    gemm_run<5, 27, 24, P5_PD, GATE, 10240, 16, true>(g, (const LAS char*)smem + kh * 5120 + lane * 16, ra, va0, va1, 2048, [&](int s) { if (s == -1) BAR(); });
    STAMPT(4, 1);
    pair_reduce<5>(g, smem, wid);
#pragma unroll
    for (int j = 0; j < 5; ++j) *(u32x2*)(a.out + (size_t)tok * D + c * 80 + j * 16 + (lane >> 4) * 4) = pack4(g.acc[0][j]);
    STAMPT(4, 2);
  }
}

DI void main_helper(const Args& a, char* smem, int head, int hw, u64 tag) {
  char* ws = a.ws;
  const int lane = threadIdx.x & 63, b = blockIdx.x;
  const char* kv = ws + WS_KV + (size_t)head * 64 * KVT;
  const unsigned lds0 = (unsigned)(uintptr_t)smem, v16 = lane * 16;
  auto issue = [&](int c) {
#pragma unroll
    for (int i = 0; i < 6; ++i) { const int pc = hw + 4 * i; if (pc < CH_PIECES) glds16x(v16, kv + (size_t)c * CH_BYTES + pc * 1024, lds0 + (c & 1) * CH_BYTES + pc * 1024); }
  };
  const unsigned ctl = lds0 + CTL2;
  auto refill = [&](int c) { lds_wait<NAP2>((const LAS char*)smem + CTL2 + 64 + (c - 2) * 4); STAMPH(6, c); issue(c); WAITV(0); lds_post(ctl + c * 4, 1); STAMPH(5, c); };
  __builtin_amdgcn_s_setprio(3);
  issue(0); issue(1);
  WAITV(5); lds_post(ctl, 1); STAMPH(5, 0);
  WAITV(0); lds_post(ctl + 4, 1); STAMPH(5, 1);
  const int c = b >> 5, c4 = ((b >> 5) << 1) | ((b >> 3) & 1), gct0 = (c4 * 27) >> 2;
  retile_store(ws, (const LAS char*)smem + LDS_RT, hw);
  if (hw == 0 && lane == 0 && b < 192) __hip_atomic_store(ctrp(ws, CT_O + b), tag, RLX_AGENT);
  WAITV(0);
  if (lane == 0 && __hip_atomic_fetch_add((LAS unsigned*)(smem + CTL2 + 60), 1u, __ATOMIC_RELAXED, __HIP_MEMORY_SCOPE_WORKGROUP) == 3u) signal(ws, FL_W + b, tag);
  refill(2);
  refill(3);
  {
    gu64* p = flagp(ws, FL_W + lane);
#pragma unroll 1
    for (unsigned sp = 0; sp < SPIN_CAP; ++sp) {
      const u64 f0 = __hip_atomic_load(p, RLX_AGENT), f1 = __hip_atomic_load(p + 64, RLX_AGENT), f2 = __hip_atomic_load(p + 128, RLX_AGENT), f3 = __hip_atomic_load(p + 192, RLX_AGENT);
      if (__all(f0 == tag && f1 == tag && f2 == tag && f3 == tag)) break;
      __builtin_amdgcn_s_sleep(4);
    }
    asm volatile("" ::: "memory");
  }
  if (b == 0 && hw == 0 && lane == 0) signal(ws, FL_EPOCH, tag);
  f32x4 cst;
  {
    const int n = gct0 * 16 + (lane < 28 ? lane : (lane < 56 ? lane - 28 : 0)) * 4;
    const rsrc_t rc = mkrsrc(ws + WS_CS1, 4 * 1792 * 4);
    f32x4 t[4];
#pragma unroll
    for (int k = 0; k < 4; ++k) t[k] = __builtin_bit_cast(f32x4, __builtin_amdgcn_raw_buffer_load_b128(rc, n * 4, k * 7168, XAUX));
    const f32x4 bv = unpack4(*(const u32x2*)(a.b1 + n));
    cst = lane < 28 ? (t[0] + t[1]) + (t[2] + t[3]) : bv;
    asm volatile("" : "+v"(cst));
  }
  const char* wo = ws + WS_WO + (size_t)c * 102400;
  auto wo_dma = [&](int i0, int i1) {
#pragma unroll 1
    for (int i = i0; i < i1; ++i) { const int p = i * 4 + hw; glds16x(v16, wo + p * 1024, lds0 + LDS_WO + p * 1024); }
  };
  bf16x8 wa[35], wb[20];
  const rsrc_t r1 = mkrsrc(ws + WS_W1 + (size_t)gct0 * 20480, 7 * 20480), r2 = mkrsrc(ws + WS_W2 + (size_t)c * 276480, 276480);
  auto ld1 = [&](int i) { return ldfrag(r1, v16 + hw * 1024, ((i % 7) * 20 + (i / 7) * 4) * 1024); };
  auto ld2 = [&](int i) { return ldfrag(r2, v16 + hw * 1024, i * 4096); };
  refill(4);
  wo_dma(0, 25);
  refill(5);
  lds_post(ctl + 32, 1);
#pragma unroll
  for (int i = 0; i < 20; ++i) wa[i] = ld1(i);
  SBAR();
  refill(6);
#pragma unroll
  for (int i = 20; i < 35; ++i) wa[i] = ld1(i);
  SBAR();
  refill(7);
#pragma unroll
  for (int i = 0; i < 20; ++i) wb[i] = ld2(i);
  SBAR();
  lds_wait<NAP2>((const LAS char*)smem + CTL2 + 64 + 28);
  __builtin_amdgcn_s_setprio(0);
  LAS bf16x8* lw = (LAS bf16x8*)smem + hw * 7 * 64 + lane;
#pragma unroll
  for (int i = 0; i < 14; ++i) lw[((i / 7) * 28 + i % 7) * 64] = wa[i];
  BAR();
#pragma unroll
  for (int i = 14; i < 21; ++i) lw[((i / 7) * 28 + i % 7) * 64] = wa[i];
  BAR();
#pragma unroll
  for (int i = 21; i < 35; ++i) lw[((i / 7) * 28 + i % 7) * 64] = wa[i];
  LGKM0();
  BAR();
  if (hw == 0 && lane < 56) *((LAS f32x4*)(smem + LDS_CB) + lane) = cst;
  {
    const rsrc_t rst = mkrsrc(ws + WS_ST, S * 64);
    const unsigned so = (((((b & 7) * 2 + ((b >> 4) & 1)) * 4 + hw) * 2 + ((lane >> 4) & 1)) * 8 + (lane >> 5) * 4) * 128 + (lane & 15) * 8;
    f32x2 p[4];
#pragma unroll
    for (int k = 0; k < 4; ++k) p[k] = __builtin_bit_cast(f32x2, __builtin_amdgcn_raw_buffer_load_b64(rst, so + k * 128, 0, XAUX));
    const float m = xhalf_sum((p[0][0] + p[1][0]) + (p[2][0] + p[3][0])) * (1.f / D);
    const float var = fmaxf(xhalf_sum((p[0][1] + p[1][1]) + (p[2][1] + p[3][1])) * (1.f / D) - m * m, 0.f);
    const float r1 = __builtin_amdgcn_rsqf(var + 1e-5f);
    const float rstd = r1 * __builtin_amdgcn_rsqf(var * r1 * r1 + 1e-5f);
    if (lane < 32) *(LAS f32x2*)(smem + LDS_RS + (hw * 32 + lane) * 8) = f32x2{-m * rstd, rstd};
  }
#pragma unroll
  for (int i = 0; i < 35; ++i) { wa[i] = ld2(20 + i); SBAR(); __builtin_amdgcn_s_sleep(PACE); SBAR(); }
  LGKM0(); BAR();
  BAR();
  LAS bf16x8* l2 = (LAS bf16x8*)smem + hw * 64 + lane;
#pragma unroll
  for (int i = 0; i < 20; ++i) l2[i * 256] = wb[i];
  LGKM0(); SBAR();
#pragma unroll
  for (int i = 0; i < 13; ++i) wb[i] = ld2(55 + i);
  SBAR();
#pragma unroll
  for (int i = 0; i < 20; ++i) l2[(20 + i) * 256] = wa[i];
  LGKM0(); BAR();
  BAR();
#pragma unroll
  for (int i = 0; i < 10; ++i) l2[i * 256] = wa[20 + i];
  BAR();
#pragma unroll
  for (int i = 0; i < 10; ++i) l2[(10 + i) * 256] = i < 5 ? wa[30 + i] : wb[i - 5];
  BAR();
#pragma unroll
  for (int i = 0; i < 8; ++i) l2[(20 + i) * 256] = wb[5 + i];
  LGKM0(); BAR();
  BAR(); BAR();
  BAR();
}

__global__ __launch_bounds__(512, 2) void fused_block(Args a) {
  extern __shared__ __attribute__((aligned(16))) char smem[];
  char* ws = a.ws;
  const int wid = __builtin_amdgcn_readfirstlane(threadIdx.x >> 6), lane = threadIdx.x & 63;
  const int b = blockIdx.x, xg = b & 7, j = b >> 3;
  STAMPT(0, 0);
  const u64 t_start = PADAT >= 0 ? __builtin_amdgcn_s_memrealtime() : 0;
  asm volatile("" :: "s"(a.x), "s"(a.wqkv), "s"(a.ws));
  const int bm = (xg >> 1) * 4 + (j & 3), bn = (xg & 1) * 8 + (j >> 2);
#define K1_ROT (j % 10)
  const int head = (b & 7) * 4 + ((b >> 3) & 3), qc = b >> 5;
  u64 tag = 0;
  if (bn >= 15) {
    tag = get_tag(ws);
    if (wid < 4) { XRow xr; xrow_load(xr, a.x, b * 8 + wid * 2); xrow_publish(xr, ws, b * 8 + wid * 2, tag); }
    k1_colsum(a.x, a.wqkv, ws, smem, bm, wid, tag);
    if (wid >= 4) retile_dma(a, (unsigned)(uintptr_t)smem + LDS_RT, wid - 4);
  }
  if (wid >= 4) {
    if (bn < 15) k1_helper(a, a.x, a.wqkv, ws, smem, bm, bn, K1_ROT, wid - 4, tag);
    WAITV(0); BAR(); BAR();
    main_helper(a, smem, head, wid - 4, tag);
  } else {
    if (bn < 15) {
      XRow xr;
      xrow_load(xr, a.x, b * 8 + wid * 2);
      {
        const char* pc = (const char*)(__builtin_amdgcn_s_getpc() & ~127ll);
        const unsigned off = min((((j & 1) * 4 + wid) * 64 + lane) * 128, CODE_BYTES);
        asm volatile("s_mov_b32 m0, %2\n\ts_nop 0\n\tglobal_load_lds_dword %0, %1" :: "v"(off), "s"(pc), "s"(__builtin_amdgcn_readfirstlane((unsigned)(uintptr_t)smem + 140000 + wid * 256)) : "memory");
      }
      tag = get_tag(ws);
      if (bn < 10) k1_compute<false>(ws, smem, bm, bn, wid, xr, tag, t_start); else k1_compute<true>(ws, smem, bm, bn, wid, xr, tag, t_start);
    }
    STAMPT(0, 3);
    if (wid == 3 && lane < 32) { ((LAS unsigned*)(smem + CTL2))[lane] = 0; LGKM0(); }
    WAITV(0); BAR();
    if (wid == 0 && bn < 15 && (bn < 5 ? lane == (bm >> 1) : lane < 8)) arrive(ws, CT_QKV + bn * 8 + lane);
    STAMPT(0, 4);
    Pre pre; post_prefetch(pre, a, wid);
    const int t0 = (head * 20) >> 7, t1 = (head * 20 + 19) >> 7;
    if (wid == 1) await(ws, CT_QKV + (min(lane >> 1, 2) * 5 + ((lane & 1) ? t1 : t0)) * 8 + qc, 6, tag + (lane < 2 ? 2 : 16));
    PADT(1, 30);
    BAR();
    STAMPT(1, 0);
    attn_compute(ws, smem, head, qc, wid); PADT(2, 60); post_compute(a, smem, wid, pre, tag, t_start); PADT(0, 100);
  }
}

extern "C" void kernel_launch(void* const* d_in, const int* in_sizes, int n_in, void* d_out, int out_size, void* d_ws, size_t ws_size, hipStream_t stream) {
  Args a;
  a.x = (const u16*)d_in[0]; a.wqkv = (const u16*)d_in[1]; a.wo = (const u16*)d_in[2];
  a.w1 = (const u16*)d_in[4]; a.b1 = (const u16*)d_in[5]; a.w2 = (const u16*)d_in[6]; a.b2 = (const u16*)d_in[7];
  a.out = (u16*)d_out; a.ws = (char*)d_ws;
  (void)hipFuncSetAttribute((const void*)fused_block, hipFuncAttributeMaxDynamicSharedMemorySize, LDS_BYTES);
  fused_block<<<256, 512, LDS_BYTES, stream>>>(a);
}
```

```cpp
#define K1_ROT ((j % 5) * 2)
#include <hip/hip_runtime.h>
#include <hip/hip_bf16.h>
typedef unsigned short u16;
typedef __attribute__((ext_vector_type(8))) short bf16x8;
typedef __attribute__((ext_vector_type(8))) __bf16 bf8;
typedef __attribute__((ext_vector_type(4))) short s16x4;
typedef __attribute__((ext_vector_type(16))) float f32x16;
typedef __attribute__((ext_vector_type(4))) float f32x4;
typedef __attribute__((ext_vector_type(2))) float f32x2;
typedef __attribute__((ext_vector_type(4))) unsigned u32x4;
typedef __attribute__((ext_vector_type(2))) unsigned u32x2;
typedef __attribute__((ext_vector_type(2))) __bf16 bf2;
#define LAS __attribute__((address_space(3)))
#define DI __device__ __forceinline__
#define SBAR() __builtin_amdgcn_sched_barrier(0)
#define LGKM0() __builtin_amdgcn_s_waitcnt(0xC07F)
#define BAR() asm volatile("s_barrier" ::: "memory")
#define WAITV(n) asm volatile("s_waitcnt vmcnt(%0)" :: "i"(n) : "memory")

constexpr int S = 2048, D = 640, H = 32, DH = 20, F = 1728;
constexpr int QT = 1280, KVT = 2688;
constexpr size_t WS_Q = 0, WS_KV = WS_Q + (size_t)H * 64 * QT, WS_O = WS_KV + (size_t)H * 64 * KVT,
                 WS_H = WS_O + (size_t)S * D * 2, WS_H1 = WS_H + (size_t)S * D * 2,
                 WS_ST = WS_H1 + (size_t)S * F * 2, WS_WO = WS_ST + (size_t)S * 64, WS_W1 = WS_WO + (size_t)D * D * 2, WS_W2 = WS_W1 + (size_t)D * F * 2 + 20480,
                 WS_CS1 = WS_W2 + (size_t)D * F * 2, WS_XST = WS_CS1 + 4 * 1792 * 4, WS_CSQ = WS_XST + (size_t)S * 16, WS_FL = WS_CSQ + 16384, WS_END = WS_FL + 65536;
#define STAMPT(k, s) do {} while (0)
#define STAMPH(k, s) do {} while (0)
#define PADAT -1
#define PADT(k, us) do { if (PADAT == (k)) { while (__builtin_amdgcn_s_memrealtime() - t_start < (us) * 100ull) __builtin_amdgcn_s_sleep(2); } } while (0)
constexpr int KSKEW = 1;
constexpr float QSCALE = 0.22360679774997896f * 1.4426950408889634f;

DI float bf2f(u16 v) { return __uint_as_float((unsigned)v << 16); }
DI float bflo(unsigned u) { return __uint_as_float(u << 16); }
DI float bfhi(unsigned u) { return __uint_as_float(u & 0xffff0000u); }
DI unsigned pk2(float lo, float hi) { f32x2 v = {lo, hi}; return __builtin_bit_cast(unsigned, __builtin_convertvector(v, bf2)); }
DI float rbf(float v) { return bflo(pk2(v, 0.f)); }
DI void glds16(unsigned voff, const void* sbase, unsigned lds) {
  asm volatile("s_mov_b32 m0, %2\n\ts_nop 0\n\tglobal_load_lds_dwordx4 %0, %1" :: "v"(voff), "s"(sbase), "s"(__builtin_amdgcn_readfirstlane(lds)) : "memory"); }
DI void glds16x(unsigned voff, const void* sbase, unsigned lds) {
  asm volatile("s_mov_b32 m0, %2\n\ts_nop 0\n\tglobal_load_lds_dwordx4 %0, %1 sc1" :: "v"(voff), "s"(sbase), "s"(__builtin_amdgcn_readfirstlane(lds)) : "memory"); }
DI s16x4 trread(const LAS char* p) { return __builtin_amdgcn_ds_read_tr16_b64_v4i16((LAS s16x4*)p); }
DI bf16x8 cat8(s16x4 a, s16x4 b) { return __builtin_shufflevector(a, b, 0, 1, 2, 3, 4, 5, 6, 7); }
#define MFMA32(a, b, c) __builtin_amdgcn_mfma_f32_32x32x16_bf16(__builtin_bit_cast(bf8, a), __builtin_bit_cast(bf8, b), c, 0, 0, 0)
#define MFMA32K8(a, b, c) __builtin_amdgcn_mfma_f32_32x32x8bf16_1k(a, b, c, 0, 0, 0)
#define MFMA16(a, b, c) __builtin_amdgcn_mfma_f32_16x16x32_bf16(__builtin_bit_cast(bf8, a), __builtin_bit_cast(bf8, b), c, 0, 0, 0)
DI void mfma16_acc(f32x4& c, bf16x8 a, bf16x8 b) { asm volatile("v_mfma_f32_16x16x32_bf16 %0, %1, %2, %0" : "+v"(c) : "v"(a), "v"(b)); }
DI float xhalf_sum(float v) { auto r = __builtin_amdgcn_permlane32_swap(__float_as_uint(v), __float_as_uint(v), false, false); return __uint_as_float(r[0]) + __uint_as_float(r[1]); }
DI float xhalf_max(float v) { auto r = __builtin_amdgcn_permlane32_swap(__float_as_uint(v), __float_as_uint(v), false, false); return fmaxf(__uint_as_float(r[0]), __uint_as_float(r[1])); }
DI float xq_sum(float v) { auto r = __builtin_amdgcn_permlane16_swap(__float_as_uint(v), __float_as_uint(v), false, false); return xhalf_sum(__uint_as_float(r[0]) + __uint_as_float(r[1])); }
typedef __amdgpu_buffer_rsrc_t rsrc_t;
DI rsrc_t mkrsrc(const void* p, unsigned bytes) { return __builtin_amdgcn_make_buffer_rsrc((void*)p, 0, bytes, 0x00020000); }
constexpr int XAUX = 16;
DI bf16x8 ldfrag(rsrc_t r, unsigned voff, unsigned soff) { return __builtin_bit_cast(bf16x8, __builtin_amdgcn_raw_buffer_load_b128(r, voff, soff, XAUX)); }
DI void st16(rsrc_t r, unsigned voff, u32x4 v) { __builtin_amdgcn_raw_buffer_store_b128(v, r, voff, 0, XAUX); }
DI void st8(rsrc_t r, unsigned voff, u32x2 v) { __builtin_amdgcn_raw_buffer_store_b64(v, r, voff, 0, XAUX); }

DI s16x4 ldhalf(rsrc_t r, unsigned voff, unsigned soff) { return __builtin_bit_cast(s16x4, __builtin_amdgcn_raw_buffer_load_b64(r, voff, soff, XAUX)); }
DI void st16s(rsrc_t r, unsigned voff, unsigned soff, u32x4 v) { __builtin_amdgcn_raw_buffer_store_b128(v, r, voff + soff, 0, XAUX); }
DI void st8s(rsrc_t r, unsigned voff, unsigned soff, u32x2 v) { __builtin_amdgcn_raw_buffer_store_b64(v, r, voff, soff, XAUX); }
typedef unsigned long long u64;
typedef __attribute__((address_space(1))) u64 gu64;
#define RLX_AGENT __ATOMIC_RELAXED, __HIP_MEMORY_SCOPE_AGENT
constexpr int FL_EPOCH = 0, FL_W = 64  ;
constexpr int CT_BASE = 512, CT_QKV = 0  , CT_O = 128  , CT_H = 192  , CT_H1 = 256  ;
constexpr unsigned SPIN_CAP = 1u << 17;
DI u64 get_tag(char* ws) {
  u64 e;
  asm volatile("s_load_dwordx2 %0, %1, 0x0\n\ts_waitcnt lgkmcnt(0)" : "=s"(e) : "s"(ws + WS_FL + FL_EPOCH * 8) : "memory");
  return e * 6364136223846793005ull + 1442695040888963407ull;
}
DI gu64* flagp(char* ws, int i) { return (gu64*)(ws + WS_FL) + i; }
DI void signal(char* ws, int i, u64 tag) { __hip_atomic_store(flagp(ws, i), tag, RLX_AGENT); }
DI gu64* ctrp(char* ws, int i) { return (gu64*)(ws + WS_FL) + CT_BASE + i * 16; }
DI void arrive(char* ws, int i) { __hip_atomic_fetch_add(ctrp(ws, i), 1ull, RLX_AGENT); }
DI void await(char* ws, int i, int n, u64 want) {
  gu64* p = ctrp(ws, i);
  const bool on = (int)(threadIdx.x & 63) < n;
#pragma unroll 1
  for (unsigned sp = 0; sp < SPIN_CAP; ++sp) {
    u64 v = want;
    if (on) asm volatile("global_atomic_add_x2 %0, %1, %2, off sc0 sc1\n\ts_waitcnt vmcnt(0)" : "=v"(v) : "v"(p), "v"(0ull) : "memory");
    if (__all(v == want)) break;
  }
  asm volatile("" ::: "memory");
}

DI void lds_post(unsigned addr, unsigned one) { asm volatile("s_mov_b64 exec, 1\n\tds_add_u32 %0, %1\n\ts_mov_b64 exec, -1" :: "v"(addr), "v"(one) : "memory"); }
template <int NAP = 1> DI void lds_wait(const LAS char* p) {
#pragma unroll 1
  for (unsigned sp = 0; sp < (1u << 22) && __builtin_amdgcn_readfirstlane(*(const volatile LAS unsigned*)p) < 4u; ++sp) __builtin_amdgcn_s_sleep(NAP);
  asm volatile("" ::: "memory");
}
#define NAP1 3
#define NAP2 12
constexpr int CTL1 = 133120  , CTL2 = 57344  ;

constexpr int K1_NSLOT = 4, K1_STAGE = 32768, CODE_BYTES = 49152;
#define K1_PRE 2
DI float dot2(unsigned a, unsigned b, float c) { return __builtin_amdgcn_fdot2_f32_bf16(__builtin_bit_cast(bf2, a), __builtin_bit_cast(bf2, b), c, false); }
template <int CTRL> DI float dpp_add(float v) { return v + __builtin_bit_cast(float, __builtin_amdgcn_update_dpp(0, __builtin_bit_cast(int, v), CTRL, 0xf, 0xf, true)); }
DI float sum32(float v) {
  v = dpp_add<0x128>(v); v = dpp_add<0x124>(v); v = dpp_add<0x122>(v); v = dpp_add<0x121>(v);
  auto r = __builtin_amdgcn_permlane16_swap(__float_as_uint(v), __float_as_uint(v), false, false); return __uint_as_float(r[0]) + __uint_as_float(r[1]);
}
struct XRow { u32x4 a, b; u32x2 c; };
DI void xrow_publish(const XRow& v, char* ws, int row0, u64 tag) {
  const int lane = threadIdx.x & 63;
  const unsigned e[10] = {v.a[0], v.a[1], v.a[2], v.a[3], v.b[0], v.b[1], v.b[2], v.b[3], v.c[0], v.c[1]};
  float s = 0.f, q = 0.f;
#pragma unroll
  for (int i = 0; i < 10; ++i) { s = dot2(e[i], 0x3F803F80u, s); q = dot2(e[i], e[i], q); }
  const float m = sum32(s) * (1.f / D), var = fmaxf(sum32(q) * (1.f / D) - m * m, 0.f);
  const float rstd = __builtin_amdgcn_rcpf(rbf(__builtin_amdgcn_sqrtf(rbf(rbf(var) + 1e-5f))));
  if ((lane & 31) == 0) st16(mkrsrc(ws + WS_XST, S * 16), (row0 + (lane >> 5)) * 16, u32x4{__float_as_uint(rbf(m)), (unsigned)tag, __float_as_uint(rstd), (unsigned)(tag >> 32)});
}
struct F4 { bf16x8 a0, a1, b0, b1; };
template <bool isV> DI void k1_compute(char* ws, char* smem, int bm, int bn, int wid, const XRow& xr, u64 tag, const char* code, u64 t_start) {
  const int lane = threadIdx.x & 63, r = lane & 31, hh = lane >> 5;
  const int wm = wid >> 1, wn = wid & 1;
  f32x16 c00 = f32x16{}, c01 = f32x16{}, c10 = f32x16{}, c11 = f32x16{};
  const int fA = (r >> 1) & 7;
  const int gi = lane >> 4, q = (lane & 15) >> 2, p = lane & 3;
  const LAS char* sm = (const LAS char*)smem;
  const LAS char* pa[2][4]; const LAS char* pb[2][2];
#pragma unroll
  for (int w = 0; w < 2; ++w) {
#pragma unroll
    for (int s = 0; s < 4; ++s) { pa[w][s] = sm + w * 65536 + (wm * 64 + r) * 128 + (((2 * s + hh) ^ fA) << 4); asm volatile("" : "+v"(pa[w][s])); }
#pragma unroll
    for (int jn = 0; jn < 2; ++jn) { pb[w][jn] = sm + w * 65536 + 16384 + (8 * (gi >> 1) + q) * 256 + 32 * (gi & 1) + 8 * p + (((2 * wn + jn) ^ q) << 6); asm volatile("" : "+v"(pb[w][jn])); }
  }
  auto lda = [&](int i, int t) { const int sl = (i >> 2) % K1_NSLOT; return *(const LAS bf16x8*)(pa[sl >> 1][i & 3] + (sl & 1) * K1_STAGE + t * 4096); };
  auto ldb = [&](int i, int jn) { const int sl = (i >> 2) % K1_NSLOT; const LAS char* b = pb[sl >> 1][jn] + (sl & 1) * K1_STAGE + (i & 3) * 4096; return cat8(trread(b), trread(b + 1024)); };
#define K1_MM(c, a, b) asm volatile("v_mfma_f32_32x32x16_bf16 %0, %1, %2, %0" : "+v"(c) : "v"(isV ? a : b), "v"(isV ? b : a))
  const bool isQ = bn < 5; const int tstride = isQ ? QT : KVT;
  const rsrc_t rimg = mkrsrc(ws + (isQ ? WS_Q : WS_KV), H * 64 * KVT);
  unsigned soff[8];
  if (!isV) {
#pragma unroll
    for (int u = 0; u < 8; ++u) {
      const int G = (bn % 5) * 16 + wn * 8 + u, pr = G / 5, g5 = G - pr * 5;
      soff[u] = g5 == 2 ? (2 * pr + hh) * 64 * tstride + 1024 + r * 8 : (2 * pr + (g5 > 2)) * 64 * tstride + (g5 > 2 ? g5 - 3 : g5) * 512 + r * 16 + hh * 8;
      asm volatile("" : "+v"(soff[u]));
    }
  } else {
#pragma unroll
    for (int u = 0; u < 4; ++u) {
      const int n = (bn - 10) * 128 + wn * 64 + u * 16 + (lane & 15), head = n / 20, d = n - head * 20;
      soff[u] = head * 64 * KVT + 1280 + d * 64 + (((lane >> 4) ^ (d >> 2)) & 3) * 16;
      const unsigned c1 = d == 18 ? 0x3F803F80u : 0u, o = d >= 18 ? soff[u] + 128 : H * 64 * KVT;
#pragma unroll
      for (int i = 0; i < 2; ++i) st16s(rimg, o, (bm * 4 + wm * 2 + i) * KVT, u32x4{c1, c1, c1, c1});
      asm volatile("" : "+v"(soff[u]));
    }
  }
  asm volatile("" : "+v"(c00), "+v"(c01), "+v"(c10), "+v"(c11));
  SBAR();
  xrow_publish(xr, ws, blockIdx.x * 8 + wid * 2, tag);
  WAITV(isV ? 9 : 1); BAR(); SBAR();
  if (wid == 0) {
    const unsigned off = min((int)((blockIdx.x >> 3) * 16 + (lane & 15)) * 128, CODE_BYTES);
    asm volatile("s_mov_b32 m0, %2\n\ts_nop 0\n\tglobal_load_lds_dword %0, %1" :: "v"(off), "s"(code), "s"(__builtin_amdgcn_readfirstlane((unsigned)(uintptr_t)smem + 140032 + wid * 256)) : "memory");
  }
  PADT(3, 15);
  STAMPT(0, 1);
  F4 f[3];
#pragma unroll
  for (int i = 0; i < 2; ++i) { f[i].a0 = lda(i, 0); f[i].b0 = ldb(i, 0); f[i].b1 = ldb(i, 1); f[i].a1 = lda(i, 1); }
  SBAR();
#pragma unroll
  for (int i = 0; i < 40; ++i) {
    F4& x = f[i % 3]; F4& y = f[(i + 2) % 3];
    const bool ld = i + 2 < 40;
    if (ld && (i & 3) == 2) { BAR(); SBAR(); }
    K1_MM(c00, x.a0, x.b0); SBAR(); if (ld) { y.a0 = lda(i + 2, 0); y.b0 = ldb(i + 2, 0); } SBAR();
    K1_MM(c01, x.a0, x.b1); SBAR(); if (ld) { y.b1 = ldb(i + 2, 1); } SBAR();
    K1_MM(c10, x.a1, x.b0); SBAR(); if (ld) { y.a1 = lda(i + 2, 1); } SBAR();
    K1_MM(c11, x.a1, x.b1); SBAR();
  }
  asm volatile("s_nop 11" ::: "memory"); SBAR();
  PADT(6, 22);
  STAMPT(0, 2);
  BAR();
  const LAS float* sMean = (const LAS float*)(smem + K1_NSLOT * K1_STAGE); const LAS float* sRstd = sMean + 128; const LAS float* sCs = sMean + 256;
  if (!isV) {
#pragma unroll
    for (int i = 0; i < 2; ++i) {
      const int tl = wm * 64 + i * 32;
      const float m = sMean[tl + r], rstd = sRstd[tl + r] * (isQ ? QSCALE : 1.f);
      int tile = bm * 4 + wm * 2 + i; if (!isQ) tile = (tile + 64 - KSKEW) & 63;
#pragma unroll
      for (int jn = 0; jn < 2; ++jn) {
        const f32x16& c = i ? (jn ? c11 : c10) : (jn ? c01 : c00);
#pragma unroll
        for (int g = 0; g < 4; ++g) {
          const f32x4 c4 = *(const LAS f32x4*)(sCs + wn * 64 + jn * 32 + 8 * g + 4 * hh);
          u32x2 v;
          v[0] = pk2((c[4 * g] - m * c4[0]) * rstd, (c[4 * g + 1] - m * c4[1]) * rstd);
          v[1] = pk2((c[4 * g + 2] - m * c4[2]) * rstd, (c[4 * g + 3] - m * c4[3]) * rstd);
          st8s(rimg, soff[jn * 4 + g], tile * tstride, v);
        }
      }
    }
  } else {
#pragma unroll
    for (int i = 0; i < 2; ++i) {
      const int tile = bm * 4 + wm * 2 + i;
      f32x4 m4[4], r4[4];
#pragma unroll
      for (int g = 0; g < 4; ++g) { const int tl = wm * 64 + i * 32 + 8 * g + 4 * hh; m4[g] = *(const LAS f32x4*)(sMean + tl); r4[g] = *(const LAS f32x4*)(sRstd + tl); }
#pragma unroll
      for (int jn = 0; jn < 2; ++jn) {
        const f32x16& c = i ? (jn ? c11 : c10) : (jn ? c01 : c00);
        const float cs = sCs[wn * 64 + jn * 32 + r];
        unsigned v[8];
#pragma unroll
        for (int g = 0; g < 4; ++g) {
          v[2 * g] = pk2((c[4 * g] - m4[g][0] * cs) * r4[g][0], (c[4 * g + 1] - m4[g][1] * cs) * r4[g][1]);
          v[2 * g + 1] = pk2((c[4 * g + 2] - m4[g][2] * cs) * r4[g][2], (c[4 * g + 3] - m4[g][3] * cs) * r4[g][3]);
        }
        u32x4 lo, hi;
#pragma unroll
        for (int e = 0; e < 4; ++e) { const auto t = __builtin_amdgcn_permlane16_swap(v[e], v[4 + e], false, false); lo[e] = t[0]; hi[e] = t[1]; }
        st16s(rimg, soff[2 * jn], tile * KVT, lo); st16s(rimg, soff[2 * jn + 1], tile * KVT, hi);
      }
    }
  }
}
struct Args { const u16 *x, *wqkv, *wo, *w1, *b1, *w2, *b2; u16* out; char* ws; };
constexpr int LDS_RT = 143360;
DI int g0of(int c4) { return (c4 * 27) >> 2; }
DI void retile_dma(const Args& a, unsigned lds, int hw) {
  const int lane = threadIdx.x & 63, b = blockIdx.x;
  if (b >= 40 && b < 148) {
    const int jb = b - 40;
#pragma unroll
    for (int i = 0; i < 5; ++i) {
      const int p = hw * 5 + i, ks = (jb & 3) * 5 + (p >> 2), t = (p >> 1) & 1;
      const int tile = ks < 16 ? 5 * (ks >> 1) + 2 * (ks & 1) + t : 5 * (2 * (ks - 16) + t) + 4;
      glds16((lane >> 3) * (F * 2) + (lane & 7) * 16, (const char*)a.w1 + (size_t)(tile * 16 + (p & 1) * 8) * (F * 2) + (jb >> 2) * 128, lds + p * 1024);
    }
  } else {
    const int jb = b < 40 ? b : b - 148, ks = jb >> 1, t = hw >> 1;
    int row0, skip = 0;
    if (b < 40) { if (ks < 16) row0 = (2 * ks + (hw & 1)) * 20 + 8 * t; else { row0 = ((ks - 16) * 8 + 2 * hw) * 20 + 16; skip = 16; } }
    else { const int sg = 2 * (ks - 48) + t; row0 = (ks < 48 ? g0of(ks / 3) + 2 * (ks % 3) + t : g0of(sg + sg / 3 + 1) + 6) * 16 + (hw & 1) * 8; }
    const char* src = (const char*)(b < 40 ? a.wo : a.w2) + (size_t)row0 * (D * 2) + (jb & 1) * 640;
#pragma unroll
    for (int i = 0; i < 5; ++i) { const int idx = i * 64 + lane, row = idx / 40; glds16((row + (row >> 2) * skip) * (D * 2) + (idx - row * 40) * 16, src, lds + (hw * 5 + i) * 1024); }
  }
}
DI void retile_store(char* ws, const LAS char* img, int hw) {
  const int lane = threadIdx.x & 63, b = blockIdx.x;
  const bool is1 = b >= 40 && b < 148;
  const int rs = is1 ? 128 : 640;
  const LAS char* p = img + (lane >> 2) * rs + (lane & 3) * 8 + (is1 ? hw * 32 : hw * 160);
  const rsrc_t rw = mkrsrc(ws + (b < 40 ? WS_WO : (is1 ? WS_W1 : WS_W2)), D * F * 2);
  const int jb = b < 40 ? b : (is1 ? b - 40 : b - 148);
  const int d0 = is1 ? ((jb >> 2) * 4 + hw) * 20 + (jb & 3) * 5 : (((jb & 1) * 4 + hw) * (b < 40 ? 20 : 54) + (jb >> 1)) * 5;
  float cs = 0.f;
#pragma unroll
  for (int k = 0; k < 5; ++k) {
    const LAS char* q = p + k * (is1 ? 4096 : 32);
    const u32x4 v = __builtin_bit_cast(u32x4, cat8(trread(q), trread(q + 16 * rs)));
    st16(rw, (d0 + k) * 1024 + lane * 16, v);
#pragma unroll
    for (int j = 0; j < 4; ++j) { asm volatile("v_add_f32 %0, %0, %1" : "+v"(cs) : "v"(bflo(v[j]))); asm volatile("v_add_f32 %0, %0, %1" : "+v"(cs) : "v"(bfhi(v[j]))); }
  }
  if (is1) { cs = xq_sum(cs); if (lane < 16) __builtin_amdgcn_raw_buffer_store_b32(__float_as_uint(cs), mkrsrc(ws + WS_CS1, 4 * 1792 * 4), ((jb & 3) * 1792 + 16 * ((jb >> 2) * 4 + hw) + lane) * 4, 0, XAUX); }
}
DI void k1_helper(const Args& a, const u16* __restrict__ x, const u16* __restrict__ w, char* ws, char* smem, int bm, int bn, int rot, int hw, u64& tag) {
  const int lane = threadIdx.x & 63;
  const unsigned lds0 = (unsigned)(uintptr_t)smem;
  const unsigned voffA0 = (lane >> 3) * 1280 + (((lane & 7) ^ (lane >> 4)) << 4);
  const unsigned voffA1 = (lane >> 3) * 1280 + (((lane & 7) ^ (4 + (lane >> 4))) << 4);
  const unsigned voffB = (lane >> 4) * 3840 + (((((lane & 15) >> 2) ^ (lane >> 4)) * 4 + (lane & 3)) << 4);
  const char* xa = (const char*)x + (size_t)(bm * 128 + hw * 32) * 1280;
  const char* wb = (const char*)w + (size_t)(hw * 16) * 3840 + bn * 256;
  auto issue = [&](int st) {
    int ks = st + rot; if (ks >= 10) ks -= 10;
    const unsigned dst = lds0 + (st % K1_NSLOT) * K1_STAGE + hw * 4096;
#pragma unroll
    for (int i = 0; i < 4; ++i) glds16((i & 1) ? voffA1 : voffA0, xa + i * 8 * 1280 + ks * 128, dst + i * 1024);
#pragma unroll
    for (int i = 0; i < 4; ++i) glds16(voffB, wb + (size_t)(ks * 64 + i * 4) * 3840, dst + 16384 + i * 1024);
  };
  issue(1);
  if (K1_PRE == 3) issue(2);
  WAITV(8 * (K1_PRE - 1)); BAR();
  if (K1_PRE == 2) issue(2);
  WAITV(8); BAR();
  issue(3);
  tag = get_tag(ws);
#pragma unroll 1
  for (int st = 1; st < 7; ++st) { WAITV(8); BAR(); issue(st + 3); }
  WAITV(8); BAR();
  WAITV(0); BAR();
  STAMPH(7, 0);
  const rsrc_t rs = mkrsrc(ws + WS_XST, S * 16 + 16384);
  const unsigned go = lane < 32 ? (bm * 128 + hw * 32 + lane) * 16 : S * 16 + (bn * 64 + hw * 16 + (lane & 15)) * 16;
  u32x4 rec;
#pragma unroll 1
  for (unsigned sp = 0; sp < SPIN_CAP; ++sp) {
    asm volatile("" ::: "memory");
    rec = __builtin_amdgcn_raw_buffer_load_b128(rs, go, 0, XAUX);
    if (__all(rec[1] == (unsigned)tag && rec[3] == (unsigned)(tag >> 32))) break;
    __builtin_amdgcn_s_sleep(1);
  }
  STAMPH(7, 1);
  LAS unsigned* st = (LAS unsigned*)(smem + K1_NSLOT * K1_STAGE);
  if (lane < 32) { st[hw * 32 + lane] = rec[0]; st[128 + hw * 32 + lane] = rec[2]; }
  else if (lane < 48) { st[256 + hw * 32 + 2 * (lane & 15)] = rec[0]; st[257 + hw * 32 + 2 * (lane & 15)] = rec[2]; }
  LGKM0(); BAR();
}
DI u64 k1_colsum(const u16* __restrict__ w, char* ws, char* smem, int u, int wid, const XRow& xr) {
  const int lane = threadIdx.x & 63;
  const char* p = (const char*)w + (size_t)(wid * 80 + (lane >> 4)) * 3840 + min(u, 14) * 256 + (lane & 15) * 16;
  bf16x8 v[20];
#pragma unroll
  for (int i = 0; i < 20; ++i) v[i] = *(const bf16x8*)(p + (size_t)i * 4 * 3840);
  SBAR();
  const u64 tag = get_tag(ws);
  if (wid == 0 && lane < 8 && u < 15) __hip_atomic_store(ctrp(ws, CT_QKV + u * 8 + lane), tag, RLX_AGENT);
  if (wid < 4) xrow_publish(xr, ws, blockIdx.x * 8 + wid * 2, tag);
  bf16x8 sel = bf16x8{};
#pragma unroll
  for (int j = 0; j < 8; ++j) sel[j] = (lane & 15) == j ? (short)0x3F80 : (short)0;
  f32x4 acc = f32x4{};
#pragma unroll
  for (int i = 0; i < 20; ++i) acc = MFMA16(v[i], sel, acc);
  LAS float* part = (LAS float*)smem;
  if ((lane & 15) < 8) {
#pragma unroll
    for (int i = 0; i < 4; ++i) part[wid * 128 + ((lane >> 4) * 4 + i) * 8 + (lane & 15)] = acc[i];
  }
  LGKM0(); BAR();
  if (wid == 0 && u < 15) {
    float c0 = 0.f, c1 = 0.f;
#pragma unroll
    for (int k = 0; k < 8; ++k) { c0 += part[k * 128 + 2 * lane]; c1 += part[k * 128 + 2 * lane + 1]; }
    WAITV(0);
    st16(mkrsrc(ws + WS_CSQ, 16384), (u * 64 + lane) * 16, u32x4{__float_as_uint(c0), (unsigned)tag, __float_as_uint(c1), (unsigned)(tag >> 32)});
  }
  STAMPT(5, 0);
  return tag;
}

constexpr int CH_TILES = 8, CH_BYTES = CH_TILES * KVT, NCH = 64 / CH_TILES, CH_PIECES = CH_BYTES / 1024;
#define EX(v) __builtin_amdgcn_exp2f(v)
#define PF(p) __builtin_bit_cast(bf16x8, p)
DI float rowmax16(const f32x16& s) {
  float m = s[0];
#pragma unroll
  for (int e = 1; e < 16; ++e) m = fmaxf(m, s[e]);
  return xhalf_max(m);
}
DI s16x4 shift_from(const f32x16& s, s16x4 q2, int hh) {
  float m = s[0];
#pragma unroll
  for (int e = 1; e < 15; e += 2) asm("v_max3_f32 %0, %0, %1, %2" : "+v"(m) : "v"(s[e]), "v"(s[e + 1]));
  m = -fmaxf(m, s[15]);
  const unsigned mh = pk2(m, 0.f) & 0xffffu, ml = pk2(m - bflo(mh), 0.f) & 0xffffu;
  return hh ? s16x4{(short)mh, (short)ml, 0, 0} : q2;
}
DI bool attn_unsafe(const f32x16& o, int hh) {
  const float l = hh ? o[8] : o[12];
  float t = 0.f;
#pragma unroll
  for (int e = 0; e < 12; e += 4) t += (fabsf(o[e]) + fabsf(o[e + 1])) + (fabsf(o[e + 2]) + fabsf(o[e + 3]));
  return __any(!(l >= 1e-20f && l <= 1e30f && t <= 3e38f));
}
DI f32x16 attn_online(rsrc_t kv, bf16x8 q1, s16x4 q2, int lane, int k2off, int voff) {
  if (lane >= 32) q2 = s16x4{0, 0, 0, 0};
  float m = -INFINITY; f32x16 o = f32x16{};
#pragma unroll 1
  for (int t = 0; t < 64; ++t) {
    const int kt = ((t + 64 - KSKEW) & 63) * KVT, vt = t * KVT;
    f32x16 s = MFMA32(ldfrag(kv, lane * 16, kt), q1, f32x16{}); s = MFMA32K8(ldhalf(kv, k2off, kt), q2, s);
    const float mn = fmaxf(m, rowmax16(s)), al = EX(m - mn); m = mn;
#pragma unroll
    for (int e = 0; e < 16; ++e) { o[e] *= al; s[e] = EX(s[e] - mn); }
    const u32x4 p0 = {pk2(s[0], s[1]), pk2(s[2], s[3]), pk2(s[4], s[5]), pk2(s[6], s[7])};
    const u32x4 p1 = {pk2(s[8], s[9]), pk2(s[10], s[11]), pk2(s[12], s[13]), pk2(s[14], s[15])};
    o = MFMA32(ldfrag(kv, voff, vt), PF(p0), o); o = MFMA32(ldfrag(kv, voff ^ 16, vt), PF(p1), o);
  }
  return o;
}
DI void attn_compute(char* ws, char* smem, int head, int qc, int wid) {
  const int lane = threadIdx.x & 63, r = lane & 31, hh = lane >> 5;
  const rsrc_t kv = mkrsrc(ws + WS_KV + (size_t)head * 64 * KVT, 64 * KVT);
  const int qt = qc * 8 + wid * 2;
  const rsrc_t rq = mkrsrc(ws + WS_Q + (size_t)(head * 64 + qt) * QT, 2 * QT);
  const int k2off = hh ? 2560 : 1024 + r * 8;
  const int vr = r < 21 ? r : (r == 24 ? 20 : 21), voff = 1280 + vr * 64 + (((2 * hh) ^ (vr >> 2)) & 3) * 16;
  const bf16x8 qa1 = ldfrag(rq, lane * 16, 0), qb1 = ldfrag(rq, lane * 16, QT);
  s16x4 qa2 = ldhalf(rq, 1024 + r * 8, 0), qb2 = ldhalf(rq, 1024 + r * 8, QT);
  if (hh) { qa2 = s16x4{0, 0, 0, 0}; qb2 = s16x4{0, 0, 0, 0}; }
  unsigned ooff0 = ((qt * 2 + (r >> 4)) * 20 + (head >> 1)) * 1024 + (((head & 1) * 2 + hh) * 16 + (r & 15)) * 16;
  unsigned ooff1 = hh ? S * D * 2 : ((qt * 2 + (r >> 4)) * 20 + 16 + (head >> 3)) * 1024 + ((head & 7) * 16 + (r & 15)) * 8;
  asm volatile("" : "+v"(ooff0), "+v"(ooff1));
  bf16x8 k1 = ldfrag(kv, lane * 16, 63 * KVT); s16x4 k2 = ldhalf(kv, k2off, 63 * KVT);
  f32x16 oa, ob;
  { f32x16 s = MFMA32(k1, qa1, f32x16{}), t = MFMA32(k1, qb1, f32x16{}); s = MFMA32K8(k2, qa2, s); t = MFMA32K8(k2, qb2, t);
    asm volatile("s_nop 15" : "+v"(s), "+v"(t));
    qa2 = shift_from(s, qa2, hh); qb2 = shift_from(t, qb2, hh); }
  const unsigned lds0 = (unsigned)(uintptr_t)smem;
  unsigned ctl = lds0 + CTL2;
  STAMPT(1, 1);
  asm volatile(
    ".macro AT_E r\n v_exp_f32 v[\\r], v[\\r]\n v_exp_f32 v[\\r+1], v[\\r+1]\n.endm\n"
    ".macro AT_C d, r\n v_cvt_pk_bf16_f32 v[\\d], v[\\r], v[\\r+1]\n.endm\n"
    ".macro AT_H sc, sn, pc, pp, o, x, xk2, q1, q2, y, yk2, off, mode\n"
    " v_mfma_f32_32x32x16_bf16 v[\\o:\\o+15], v[\\x+4:\\x+7], v[\\pp:\\pp+3], v[\\o:\\o+15]\n"
    " .if \\mode & 1\n ds_read_b128 v[\\y+4:\\y+7], v130 offset:\\off\n .endif\n"
    " AT_C \\pp+6, \\sn+12\n AT_C \\pp+7, \\sn+14\n AT_E \\sc\n AT_E \\sc+2\n"
    " .if (\\mode & 16) == 0\n v_mfma_f32_32x32x16_bf16 v[\\sn:\\sn+15], v[\\x:\\x+3], v[\\q1:\\q1+3], 0\n .endif\n"
    " .if \\mode & 1\n ds_read_b128 v[\\y:\\y+3], v128 offset:\\off\n .endif\n"
    " .if \\mode & 4\n ds_read_b32 v133, v131 offset:\\off\n .endif\n"
    " AT_E \\sc+4\n AT_C \\pc, \\sc\n AT_E \\sc+6\n AT_C \\pc+1, \\sc+2\n"
    " .if (\\mode & 16) == 0\n v_mfma_f32_32x32x8_bf16 v[\\sn:\\sn+15], v[\\xk2:\\xk2+1], v[\\q2:\\q2+1], v[\\sn:\\sn+15]\n .endif\n"
    " AT_E \\sc+8\n AT_C \\pc+2, \\sc+4\n AT_E \\sc+10\n AT_C \\pc+3, \\sc+6\n"
    " v_mfma_f32_32x32x16_bf16 v[\\o:\\o+15], v[\\x+8:\\x+11], v[\\pp+4:\\pp+7], v[\\o:\\o+15]\n"
    " .if \\mode & 1\n ds_read_b64 v[\\yk2:\\yk2+1], v129 offset:\\off\n ds_read_b128 v[\\y+8:\\y+11], v134 offset:\\off\n .endif\n"
    " .if \\mode & 2\n s_waitcnt lgkmcnt(0)\n .endif\n"
    " .if \\mode & 4\n s_mov_b64 exec, 1\n ds_add_u32 v131, v132 offset:\\off+60\n s_mov_b64 exec, -1\n v_cmp_gt_u32 vcc, 4, v133\n .endif\n"
    " AT_E \\sc+12\n AT_C \\pc+4, \\sc+8\n AT_E \\sc+14\n AT_C \\pc+5, \\sc+10\n"
    ".endm\n"
    ".macro AT_A x, xk2, y, yk2, t\n AT_H 56, 40, 80, 72, 8, \\x, \\xk2, 116, 124, \\y, \\yk2, (\\t)*2688, 1\n.endm\n"
    ".macro AT_B x, xk2, off, mode\n AT_H 40, 56, 72, 80, 24, \\x, \\xk2, 120, 126, 0, 0, \\off, \\mode\n.endm\n"
    ".macro AT_2 t\n AT_A 104, 102, 88, 100, \\t\n AT_B 104, 102, 0, 2\n AT_A 88, 100, 104, 102, \\t+1\n AT_B 88, 100, 0, 2\n.endm\n"
    ".macro AT_SPIN off, back\n"
    "1: s_cmp_eq_u32 s41, 0\n s_cbranch_scc1 \\back\n s_sub_u32 s41, s41, 1\n s_sleep 1\n ds_read_b32 v133, v131 offset:\\off\n s_waitcnt lgkmcnt(0)\n v_cmp_gt_u32 vcc, 4, v133\n s_nop 4\n s_cbranch_vccnz 1b\n s_branch \\back\n"
    ".endm\n"
    " s_nop 1\n"
    " v_mfma_f32_32x32x16_bf16 v[40:55], v[88:91], v[116:119], 0\n"
    " v_mfma_f32_32x32x8_bf16 v[40:55], v[100:101], v[124:125], v[40:55]\n"
    " v_mov_b32 v132, 1\n"
    " .irp r, 8,9,10,11,12,13,14,15,16,17,18,19,20,21,22,23,24,25,26,27,28,29,30,31,32,33,34,35,36,37,38,39,68,69,70,71,80,81,82,83,84,85,92,93,94,95,96,97,98,99\n v_mov_b32 v[\\r], 0\n .endr\n"
    " s_mov_b32 s40, 4\n s_mov_b32 s41, 0x400000\n"
    ".Lat_w0: s_sub_u32 s41, s41, 1\n s_cbranch_scc1 .Lat_w1\n ds_read_b32 v133, v131\n s_waitcnt lgkmcnt(0)\n v_cmp_gt_u32 vcc, 4, v133\n s_nop 4\n s_cbranch_vccnz .Lat_w0\n"
    ".Lat_w1: s_mov_b32 s41, 0x400000\n"
    " ds_read_b128 v[108:111], v130\n ds_read_b128 v[104:107], v128\n ds_read_b64 v[102:103], v129\n ds_read_b128 v[112:115], v134\n"
    " s_branch .Lat_mid\n"
    ".Lat_s1: AT_SPIN 4, .Lat_b1\n"
    ".Lat_s2: AT_SPIN 8, .Lat_b2\n"
    " .p2align 6\n"
    ".Lat_loop:\n"
    " AT_A 88, 100, 104, 102, 0\n"
    ".Lat_mid:\n"
    " AT_B 88, 100, 0, 2\n"
    " AT_2 1\n AT_2 3\n AT_2 5\n"
    " AT_A 104, 102, 88, 100, 7\n AT_B 104, 102, 4, 6\n"
    " s_cbranch_vccnz .Lat_s1\n"
    ".Lat_b1:\n"
    " AT_A 88, 100, 104, 102, 8\n AT_B 88, 100, 0, 2\n"
    " AT_2 9\n AT_2 11\n AT_2 13\n"
    " AT_A 104, 102, 88, 100, 15\n AT_B 104, 102, 8, 6\n"
    " s_cbranch_vccnz .Lat_s2\n"
    ".Lat_b2:\n"
    " v_add_u32 v131, 8, v131\n"
    " s_sub_u32 s40, s40, 1\n s_cmp_lg_u32 s40, 0\n s_cbranch_scc1 .Lat_loop\n"
    " AT_H 56, 40, 80, 72, 8, 88, 100, 116, 124, 0, 0, 0, 16\n"
    " AT_C 86, 68\n AT_C 87, 70\n"
    " v_mfma_f32_32x32x16_bf16 v[24:39], v[92:95], v[80:83], v[24:39]\n s_nop 0\n"
    " v_mfma_f32_32x32x16_bf16 v[24:39], v[96:99], v[84:87], v[24:39]\n"
    " s_nop 12\n"
    : "=&{v[8:23]}"(oa), "=&{v[24:39]}"(ob), "+{v131}"(ctl), "+{v[88:91]}"(k1), "+{v[100:101]}"(k2)
    : "{v[116:119]}"(qa1), "{v[120:123]}"(qb1), "{v[124:125]}"(qa2), "{v[126:127]}"(qb2),
      "{v128}"(lds0 + lane * 16), "{v129}"(lds0 + k2off), "{v130}"(lds0 + voff), "{v134}"(lds0 + (voff ^ 16))
    : "memory", "vcc", "scc", "s40", "s41", "v40","v41","v42","v43","v44","v45","v46","v47","v48","v49","v50","v51","v52","v53","v54","v55","v56","v57","v58","v59","v60","v61","v62","v63",
      "v64","v65","v66","v67","v68","v69","v70","v71","v72","v73","v74","v75","v76","v77","v78","v79","v80","v81","v82","v83","v84","v85","v86","v87","v92","v93","v94","v95","v96","v97","v98","v99",
      "v102","v103","v104","v105","v106","v107","v108","v109","v110","v111","v112","v113","v114","v115","v132","v133");
  STAMPT(1, 2);
  const bool fa = attn_unsafe(oa, hh), fb = attn_unsafe(ob, hh);
  if (__builtin_expect(fa, 0)) oa = attn_online(kv, qa1, qa2, lane, k2off, voff);
  if (__builtin_expect(fb, 0)) ob = attn_online(kv, qb1, qb2, lane, k2off, voff);
  const rsrc_t ro = mkrsrc(ws + WS_O, S * D * 2);
#pragma unroll
  for (int u = 0; u < 2; ++u) {
    const f32x16& o = u ? ob : oa;
    const float il = __builtin_amdgcn_rcpf(hh ? o[8] : o[12]);
    st16(ro, ooff0 + u * 40960, u32x4{pk2(o[0] * il, o[1] * il), pk2(o[2] * il, o[3] * il), pk2(o[4] * il, o[5] * il), pk2(o[6] * il, o[7] * il)});
    st8s(ro, ooff1, u * 40960, u32x2{pk2(o[8] * il, o[9] * il), pk2(o[10] * il, o[11] * il)});
  }
  STAMPT(1, 3);
  WAITV(0);
  if (lane < 2) arrive(ws, CT_O + (qc * 4 + wid) * 2 + lane);
  STAMPT(1, 4);
}

template <int NC> struct GS { f32x4 acc[2][NC]; };
template <int NC, int NKS, int NFULL, int PD, int G, int SSTRIDE, int RING, bool BARS, class Mid> DI void gemm_run(GS<NC>& g, const LAS char* wl, rsrc_t ra, unsigned va0, unsigned va1, unsigned astep, Mid mid) {
  bf16x8 af[PD][2], bf[2][NC];
  const unsigned l8 = (threadIdx.x & 63) * 8;
  auto lda = [&](int s, int i) { const unsigned v = i ? va1 : va0; return s < NFULL ? ldfrag(ra, v, s * astep) : cat8(ldhalf(ra, v - l8, s * astep), ldhalf(ra, v - l8 + 512, s * astep)); };
#pragma unroll
  for (int s = 0; s < G; ++s) { af[s][0] = lda(s, 0); af[s][1] = lda(s, 1); SBAR(); }
  const LAS char* wb[3] = {wl, wl + 61440, wl + 122880};
  asm volatile("" : "+v"(wb[0]), "+v"(wb[1]), "+v"(wb[2]));
  auto rdw = [&](int off) { return *(const LAS bf16x8*)(wb[off / 61440] + off % 61440); };
  mid(-1);
#pragma unroll
  for (int j = 0; j < NC; ++j) bf[0][j] = rdw(j * 1024);
  int nl = G;
#pragma unroll
  for (int s = 0; s < NKS; ++s) {
    LGKM0();
    if (BARS && (s + 1) % 4 == 0 && s + 1 <= RING) BAR();
    mid(s);
    SBAR();
    const int lim = s + PD < NKS ? s + PD : NKS;
    const int l0 = nl < lim ? nl : -1, l1 = (nl + 1 < lim) ? nl + 1 : -1;
    nl = l1 >= 0 ? nl + 2 : (l0 >= 0 ? nl + 1 : nl);
#pragma unroll
    for (int m = 0; m < 2 * NC; ++m) {
      mfma16_acc(g.acc[m & 1][m >> 1], bf[s & 1][m >> 1], af[s % PD][m & 1]); SBAR();
      if (s + 1 < NKS && m < NC) { bf[(s + 1) & 1][m] = rdw(((s + 1) % RING) * SSTRIDE + m * 1024); SBAR(); }
      if (l0 >= 0 && (m == NC || m == NC + 1)) { af[l0 % PD][m - NC] = lda(l0, m - NC); SBAR(); }
      if (l1 >= 0 && (m == NC + 2 || m == NC + 3)) { af[l1 % PD][m - NC - 2] = lda(l1, m - NC - 2); SBAR(); }
    }
  }
  asm volatile("s_nop 7" ::: "memory"); SBAR();
}
template <int NC> DI void pair_reduce(GS<NC>& g, char* smem, int wid) {
  const int lane = threadIdx.x & 63;
  LAS f32x4* red = (LAS f32x4*)(smem + 143360) + lane;
#pragma unroll
  for (int j = 0; j < NC; ++j) red[(wid * NC + j) * 64] = g.acc[1][j];
  LGKM0(); BAR();
#pragma unroll
  for (int j = 0; j < NC; ++j) g.acc[0][j] += red[((wid ^ 2) * NC + j) * 64];
}
DI f32x4 unpack4(u32x2 v) { return f32x4{bflo(v[0]), bfhi(v[0]), bflo(v[1]), bfhi(v[1])}; }
DI u32x2 pack4(f32x4 v) { return u32x2{pk2(v[0], v[1]), pk2(v[2], v[3])}; }
template <int NC> DI void store_tiles(rsrc_t r, unsigned o16, unsigned o8, const u32x2 (&v)[NC]) {
#pragma unroll
  for (int j = 0; j + 1 < NC; j += 2) st16(r, o16 + j * 512, u32x4{v[j][0], v[j][1], v[j + 1][0], v[j + 1][1]});
  st8(r, o8, v[NC - 1]);
}

#define P4_PD 8
#define P5_PD 8
#define GATE 3
#define CBAR 11
#define PACE0 2
#define W2EARLY 1
constexpr int LDS_WO = 61440, LDS_CB = 162816, LDS_RS = 161792, LDS_BYTES = 163840;

struct Pre { u32x2 x[5], b2[5]; };
DI void post_prefetch(Pre& p, const Args& a, int wid) {
  const int lane = threadIdx.x & 63, b = blockIdx.x, c = b >> 5;
  const int tok = (((b & 7) * 4 + ((b >> 3) & 3)) * 4 + (wid & 1) * 2 + (wid >> 1)) * 16 + (lane & 15);
#pragma unroll
  for (int j = 0; j < 5; ++j) { p.x[j] = *(const u32x2*)(a.x + (size_t)tok * D + c * 80 + j * 16 + (lane >> 4) * 4); p.b2[j] = *(const u32x2*)(a.b2 + c * 80 + j * 16 + (lane >> 4) * 4); }
}
DI void post_compute(const Args& a, char* smem, int wid, const Pre& pre, u64 tag, u64 t_start) {
  char* ws = a.ws;
  const int lane = threadIdx.x & 63, b = blockIdx.x;
  const int grp = (b & 7) * 4 + ((b >> 3) & 3), c = b >> 5, rh = wid & 1, kh = wid >> 1;
  const int rt = grp * 4 + rh * 2 + kh, tok = rt * 16 + (lane & 15);
  u32x2 hv[5];
  {
    GS<5> g;
#pragma unroll
    for (int j = 0; j < 5; ++j) { g.acc[0][j] = unpack4(pre.x[j]); g.acc[1][j] = f32x4{}; }
#pragma unroll
    for (int j = 0; j < 5; ++j) asm volatile("" : "+v"(g.acc[0][j]), "+v"(g.acc[1][j]));
    const rsrc_t ra = mkrsrc(ws + WS_O, S * D * 2);
    const unsigned va0 = (rt * 20 + kh) * 1024 + lane * 16, va1 = ((rt ^ 1) * 20 + kh) * 1024 + lane * 16;
    unsigned ho16 = (rt * 20 + 2 * c) * 1024 + lane * 16, ho8 = (rt * 20 + 16) * 1024 + c * 512 + lane * 8, so8 = lane < 16 ? (rt * 8 + c) * 128 + lane * 8 : S * 64;
    asm volatile("" : "+v"(ho16), "+v"(ho8), "+v"(so8));
    await(ws, CT_O + grp * 2 + rh, 1, tag + 32);
    STAMPT(2, 0);
    gemm_run<5, 10, 8, 10, GATE, 10240, 10, false>(g, (const LAS char*)smem + LDS_WO + kh * 5120 + lane * 16, ra, va0, va1, 2048,
                                                [&](int s) { if (s == 9) BAR(); });
    STAMPT(2, 1);
    pair_reduce<5>(g, smem, wid);
#pragma unroll
    for (int j = 0; j < 5; ++j) hv[j] = pack4(g.acc[0][j]);
    store_tiles<5>(mkrsrc(ws + WS_H, S * D * 2), ho16, ho8, hv);
    f32x4 s1 = g.acc[0][0], s2 = g.acc[0][0] * g.acc[0][0];
#pragma unroll
    for (int j = 1; j < 5; ++j) { s1 += g.acc[0][j]; s2 += g.acc[0][j] * g.acc[0][j]; }
    const float t1 = xq_sum((s1[0] + s1[1]) + (s1[2] + s1[3])), t2 = xq_sum((s2[0] + s2[1]) + (s2[2] + s2[3]));
    st8(mkrsrc(ws + WS_ST, S * 64), so8, u32x2{__float_as_uint(t1), __float_as_uint(t2)});
    STAMPT(2, 2);
    WAITV(0);
    if (lane == 0) arrive(ws, CT_H + (rt >> 1));
    STAMPT(2, 3);
  }
  {
    const int g4 = (b & 7) * 2 + ((b >> 4) & 1), c4 = ((b >> 5) << 1) | ((b >> 3) & 1);
    GS<7> g;
#pragma unroll
    for (int j = 0; j < 7; ++j) { g.acc[0][j] = f32x4{}; g.acc[1][j] = f32x4{}; asm volatile("" : "+v"(g.acc[0][j]), "+v"(g.acc[1][j])); }
    const rsrc_t ra = mkrsrc(ws + WS_H, S * D * 2);
    const int rt0 = g4 * 8 + wid * 2;
    const unsigned va0 = rt0 * 20480 + lane * 16;
    PADT(4, 70);
    f32x4 cs[7], bv[7]; f32x2 rs[2];
    const LAS f32x4* cb = (const LAS f32x4*)(smem + LDS_CB) + (lane >> 4);
    unsigned go16 = (rt0 * 54 + 3 * c4) * 1024 + lane * 16, go8 = (c4 & 3) ? (rt0 * 54 + 48) * 1024 + (c4 - 1 - (c4 >> 2)) * 512 + lane * 8 : S * F * 2;
    asm volatile("" : "+v"(go16), "+v"(go8));
    await(ws, CT_H + (rt0 >> 1), 1, tag + 16);
    STAMPT(3, 0);
    gemm_run<7, 20, 16, P4_PD, GATE, 7168, 20, false>(g, (const LAS char*)smem + lane * 16, ra, va0, va0 + 20480, 1024, [&](int s) {
      if (s == -1) BAR();
      if (s == CBAR) BAR();
      if (s == 18) {
#pragma unroll
        for (int j = 0; j < 7; ++j) { cs[j] = cb[j * 4]; bv[j] = cb[28 + j * 4]; }
#pragma unroll
        for (int i = 0; i < 2; ++i) rs[i] = *(const LAS f32x2*)(smem + LDS_RS + (wid * 32 + i * 16 + (lane & 15)) * 8);
      }
      if (s == 19) BAR();
    });
    STAMPT(3, 1);
    const rsrc_t ro = mkrsrc(ws + WS_H1, S * F * 2);
#pragma unroll
    for (int i = 0; i < 2; ++i) {
      const float mr = rs[i][0], rstd = rs[i][1];
      f32x4 v[7], t[7];
#pragma unroll
      for (int j = 0; j < 7; ++j) t[j] = cs[j] * mr + bv[j];
      SBAR();
#pragma unroll
      for (int j = 0; j < 7; ++j) v[j] = g.acc[i][j] * rstd + t[j];
      SBAR();
#pragma unroll
      for (int j = 0; j < 7; ++j) t[j] = v[j] * v[j];
      SBAR();
#pragma unroll
      for (int j = 0; j < 7; ++j) t[j] = t[j] * -0.1029432397f + -2.302208198f;
      SBAR();
#pragma unroll
      for (int j = 0; j < 7; ++j) t[j] = t[j] * v[j];
      SBAR();
#pragma unroll
      for (int j = 0; j < 7; ++j) t[j] = f32x4{EX(t[j][0]), EX(t[j][1]), EX(t[j][2]), EX(t[j][3])};
      SBAR();
#pragma unroll
      for (int j = 0; j < 7; ++j) t[j] = t[j] + 1.f;
      SBAR();
#pragma unroll
      for (int j = 0; j < 7; ++j) t[j] = f32x4{__builtin_amdgcn_rcpf(t[j][0]), __builtin_amdgcn_rcpf(t[j][1]), __builtin_amdgcn_rcpf(t[j][2]), __builtin_amdgcn_rcpf(t[j][3])};
      SBAR();
#pragma unroll
      for (int j = 0; j < 7; ++j) t[j] = t[j] * v[j];
      SBAR();
      u32x2 h1[7];
#pragma unroll
      for (int j = 0; j < 7; ++j) h1[j] = pack4(t[j]);
      store_tiles<7>(ro, go16 + i * 55296, go8 + i * 55296, h1);
      SBAR();
    }
    STAMPT(3, 2);
    WAITV(0);
    if (lane == 0) arrive(ws, CT_H1 + (rt0 >> 1));
    STAMPT(3, 3);
  }
  {
    GS<5> g;
#pragma unroll
    for (int j = 0; j < 5; ++j) { g.acc[0][j] = unpack4(pre.b2[j]) + unpack4(hv[j]); g.acc[1][j] = f32x4{}; }
#pragma unroll
    for (int j = 0; j < 5; ++j) asm volatile("" : "+v"(g.acc[0][j]), "+v"(g.acc[1][j]));
    const rsrc_t ra = mkrsrc(ws + WS_H1, S * F * 2);
    const unsigned va0 = (rt * 54 + kh) * 1024 + lane * 16, va1 = ((rt ^ 1) * 54 + kh) * 1024 + lane * 16;
    PADT(5, 80);
    await(ws, CT_H1 + (rt >> 1), 1, tag + 16);
    STAMPT(4, 0);
    gemm_run<5, 27, 24, P5_PD, GATE, 10240, 16, true>(g, (const LAS char*)smem + kh * 5120 + lane * 16, ra, va0, va1, 2048, [&](int s) { if (s == -1) BAR(); });
    STAMPT(4, 1);
    pair_reduce<5>(g, smem, wid);
#pragma unroll
    for (int j = 0; j < 5; ++j) *(u32x2*)(a.out + (size_t)tok * D + c * 80 + j * 16 + (lane >> 4) * 4) = pack4(g.acc[0][j]);
    STAMPT(4, 2);
  }
}

DI void main_helper(const Args& a, char* smem, int head, int hw, u64 tag) {
  char* ws = a.ws;
  const int lane = threadIdx.x & 63, b = blockIdx.x;
  const char* kv = ws + WS_KV + (size_t)head * 64 * KVT;
  const unsigned lds0 = (unsigned)(uintptr_t)smem, v16 = lane * 16;
  auto issue = [&](int c) {
#pragma unroll
    for (int i = 0; i < 6; ++i) { const int pc = hw + 4 * i; if (pc < CH_PIECES) glds16x(v16, kv + (size_t)c * CH_BYTES + pc * 1024, lds0 + (c & 1) * CH_BYTES + pc * 1024); }
  };
  const unsigned ctl = lds0 + CTL2;
  auto refill = [&](int c) { lds_wait<NAP2>((const LAS char*)smem + CTL2 + 64 + (c - 2) * 4); issue(c); WAITV(0); lds_post(ctl + c * 4, 1); STAMPH(6, c); };
  __builtin_amdgcn_s_setprio(3);
  issue(0); issue(1);
  WAITV(5); lds_post(ctl, 1);
  retile_dma(a, lds0 + LDS_RT, hw);
  WAITV(5); lds_post(ctl + 4, 1);
  WAITV(0); lds_post(ctl + 56, 1);
  refill(2);
  lds_wait<2>((const LAS char*)smem + CTL2 + 56);
  const int c = b >> 5, c4 = ((b >> 5) << 1) | ((b >> 3) & 1), gct0 = (c4 * 27) >> 2;
  retile_store(ws, (const LAS char*)smem + LDS_RT, hw);
  if (hw == 0 && lane == 0 && b < 192) __hip_atomic_store(ctrp(ws, CT_O + b), tag, RLX_AGENT);
  WAITV(0);
  if (lane == 0 && __hip_atomic_fetch_add((LAS unsigned*)(smem + CTL2 + 60), 1u, __ATOMIC_RELAXED, __HIP_MEMORY_SCOPE_WORKGROUP) == 3u) signal(ws, FL_W + b, tag);
  refill(3);
  refill(4);
  {
    gu64* p = flagp(ws, FL_W + lane);
#pragma unroll 1
    for (unsigned sp = 0; sp < SPIN_CAP; ++sp) {
      const u64 f0 = __hip_atomic_load(p, RLX_AGENT), f1 = __hip_atomic_load(p + 64, RLX_AGENT), f2 = __hip_atomic_load(p + 128, RLX_AGENT), f3 = __hip_atomic_load(p + 192, RLX_AGENT);
      if (__all(f0 == tag && f1 == tag && f2 == tag && f3 == tag)) break;
      __builtin_amdgcn_s_sleep(4);
    }
    asm volatile("" ::: "memory");
  }
  if (b == 0 && hw == 0 && lane == 0) signal(ws, FL_EPOCH, tag);
  f32x4 cst;
  {
    const int n = gct0 * 16 + (lane < 28 ? lane : (lane < 56 ? lane - 28 : 0)) * 4;
    const rsrc_t rc = mkrsrc(ws + WS_CS1, 4 * 1792 * 4);
    f32x4 t[4];
#pragma unroll
    for (int k = 0; k < 4; ++k) t[k] = __builtin_bit_cast(f32x4, __builtin_amdgcn_raw_buffer_load_b128(rc, n * 4, k * 7168, XAUX));
    const f32x4 bv = unpack4(*(const u32x2*)(a.b1 + n));
    cst = lane < 28 ? (t[0] + t[1]) + (t[2] + t[3]) : bv;
    asm volatile("" : "+v"(cst));
  }
  const char* wo = ws + WS_WO + (size_t)c * 102400;
  auto wo_dma = [&](int i0, int i1) {
#pragma unroll 1
    for (int i = i0; i < i1; ++i) { const int p = i * 4 + hw; glds16x(v16, wo + p * 1024, lds0 + LDS_WO + p * 1024); }
  };
  bf16x8 wa[35], wb[20];
  const rsrc_t r1 = mkrsrc(ws + WS_W1 + (size_t)gct0 * 20480, 7 * 20480), r2 = mkrsrc(ws + WS_W2 + (size_t)c * 276480, 276480);
  auto ld1 = [&](int i) { return ldfrag(r1, v16 + hw * 1024, ((i % 7) * 20 + (i / 7) * 4) * 1024); };
  auto ld2 = [&](int i) { return ldfrag(r2, v16 + hw * 1024, i * 4096); };
  wo_dma(0, 8);
  refill(5);
  wo_dma(8, 25);
  refill(6);
  lds_post(ctl + 32, 1);
#pragma unroll
  for (int i = 0; i < 20; ++i) wa[i] = ld1(i);
  SBAR();
  refill(7);
#pragma unroll
  for (int i = 20; i < 35; ++i) wa[i] = ld1(i);
#pragma unroll
  for (int i = 0; i < 20; ++i) wb[i] = ld2(i);
  SBAR();
  lds_wait<NAP2>((const LAS char*)smem + CTL2 + 64 + 28);
  __builtin_amdgcn_s_setprio(0);
  LAS bf16x8* lw = (LAS bf16x8*)smem + hw * 7 * 64 + lane;
#pragma unroll
  for (int i = 0; i < 14; ++i) lw[((i / 7) * 28 + i % 7) * 64] = wa[i];
  LGKM0(); SBAR();
#pragma unroll
  for (int i = 0; i < 14; ++i) { wa[i] = ld2(20 + i); SBAR(); __builtin_amdgcn_s_sleep(PACE0); SBAR(); }
  BAR();
#pragma unroll
  for (int i = 14; i < 21; ++i) lw[((i / 7) * 28 + i % 7) * 64] = wa[i];
  BAR();
#pragma unroll
  for (int i = 21; i < 35; ++i) lw[((i / 7) * 28 + i % 7) * 64] = wa[i];
  LGKM0();
  BAR();
  if (hw == 0 && lane < 56) *((LAS f32x4*)(smem + LDS_CB) + lane) = cst;
  {
    const rsrc_t rst = mkrsrc(ws + WS_ST, S * 64);
    const unsigned so = (((((b & 7) * 2 + ((b >> 4) & 1)) * 4 + hw) * 2 + ((lane >> 4) & 1)) * 8 + (lane >> 5) * 4) * 128 + (lane & 15) * 8;
    f32x2 p[4];
#pragma unroll
    for (int k = 0; k < 4; ++k) p[k] = __builtin_bit_cast(f32x2, __builtin_amdgcn_raw_buffer_load_b64(rst, so + k * 128, 0, XAUX));
    const float m = xhalf_sum((p[0][0] + p[1][0]) + (p[2][0] + p[3][0])) * (1.f / D);
    const float var = fmaxf(xhalf_sum((p[0][1] + p[1][1]) + (p[2][1] + p[3][1])) * (1.f / D) - m * m, 0.f);
    const float r1 = __builtin_amdgcn_rsqf(var + 1e-5f);
    const float rstd = r1 * __builtin_amdgcn_rsqf(var * r1 * r1 + 1e-5f);
    if (lane < 32) *(LAS f32x2*)(smem + LDS_RS + (hw * 32 + lane) * 8) = f32x2{-m * rstd, rstd};
  }
  LGKM0(); BAR();
#pragma unroll
  for (int i = W2EARLY ? 14 : 0; i < 35; ++i) wa[i] = ld2(20 + i);
  SBAR();
  BAR();
  LAS bf16x8* l2 = (LAS bf16x8*)smem + hw * 64 + lane;
#pragma unroll
  for (int i = 0; i < 20; ++i) l2[i * 256] = wb[i];
  LGKM0(); SBAR();
#pragma unroll
  for (int i = 0; i < 13; ++i) wb[i] = ld2(55 + i);
  SBAR();
#pragma unroll
  for (int i = 0; i < 20; ++i) l2[(20 + i) * 256] = wa[i];
  LGKM0(); BAR();
  BAR();
#pragma unroll
  for (int i = 0; i < 10; ++i) l2[i * 256] = wa[20 + i];
  BAR();
#pragma unroll
  for (int i = 0; i < 10; ++i) l2[(10 + i) * 256] = i < 5 ? wa[30 + i] : wb[i - 5];
  BAR();
#pragma unroll
  for (int i = 0; i < 8; ++i) l2[(20 + i) * 256] = wb[5 + i];
  LGKM0(); BAR();
  BAR();
}

__global__ __launch_bounds__(512, 2) void fused_block(Args a) {
  extern __shared__ __attribute__((aligned(16))) char smem[];
  typedef __attribute__((ext_vector_type(2))) u64 u64x2;
  u64x2 p01;
  asm volatile("s_load_dwordx4 %0, %1, 0x0" : "=s"(p01) : "s"(__builtin_amdgcn_kernarg_segment_ptr()));
  const char* code = (const char*)(__builtin_amdgcn_s_getpc() & ~127ll);
  char* ws = a.ws;
  const int wid = __builtin_amdgcn_readfirstlane(threadIdx.x >> 6), lane = threadIdx.x & 63;
  const int b = blockIdx.x, xg = b & 7, j = b >> 3;
  STAMPT(0, 0);
  const u64 t_start = PADAT >= 0 ? __builtin_amdgcn_s_memrealtime() : 0;
  const int bm = (xg >> 1) * 4 + (j & 3), bn = (xg & 1) * 8 + (j >> 2);
  const int head = (b & 7) * 4 + ((b >> 3) & 3), qc = b >> 5;
  const bool isA = wid < 4;
  unsigned v0 = isA ? (lane >> 3) * 1280 + (((lane & 7) ^ (lane >> 4)) << 4) : (lane >> 4) * 3840 + (((((lane & 15) >> 2) ^ (lane >> 4)) * 4 + (lane & 3)) << 4);
  unsigned v1 = isA ? v0 ^ 64 : v0;
  const unsigned s0 = isA ? (bm * 128 + wid * 32) * 1280 + (K1_ROT) * 128 : ((K1_ROT) * 64 + (wid - 4) * 16) * 3840 + bn * 256;
  unsigned xo = (b * 8 + (wid & 3) * 2 + (lane >> 5)) * 1280 + (lane & 31) * 16;
  v0 += s0; v1 += s0;
  asm volatile("" : "+v"(v0), "+v"(v1), "+v"(xo));
  asm volatile("s_waitcnt lgkmcnt(0)" : "+s"(p01));
  typedef const __attribute__((address_space(1))) char* gptr;
  XRow xr;
  if (isA) { gptr p = (gptr)p01[0]; xr.a = *(const __attribute__((address_space(1))) u32x4*)(p + xo); xr.b = *(const __attribute__((address_space(1))) u32x4*)(p + xo + 512);
             xr.c = *(const __attribute__((address_space(1))) u32x2*)(p + (xo + 1024 - (lane & 31) * 8)); }
  if (bn < 15) {
    const char* src = (const char*)(isA ? p01[0] : p01[1]);
#pragma unroll
    for (int i = 0; i < 4; ++i) glds16((i & 1) ? v1 : v0, src + i * (isA ? 10240 : 15360), (unsigned)(uintptr_t)smem + wid * 4096 + i * 1024);
  }
  u64 tag = 0;
  if (bn >= 15) tag = k1_colsum(a.wqkv, ws, smem, bm, wid, xr);
  if (wid >= 4) {
    if (bn < 15) k1_helper(a, a.x, a.wqkv, ws, smem, bm, bn, K1_ROT, wid - 4, tag);
    BAR(); BAR();
    main_helper(a, smem, head, wid - 4, tag);
  } else {
    if (bn < 15) {
      tag = get_tag(ws);
      if (bn < 10) k1_compute<false>(ws, smem, bm, bn, wid, xr, tag, code, t_start); else k1_compute<true>(ws, smem, bm, bn, wid, xr, tag, code, t_start);
    }
    STAMPT(0, 3);
    if (wid == 3 && lane < 32) { ((LAS unsigned*)(smem + CTL2))[lane] = 0; LGKM0(); }
    WAITV(0); BAR();
    if (wid == 0 && bn < 15 && (bn < 5 ? lane == (bm >> 1) : lane < 8)) arrive(ws, CT_QKV + bn * 8 + lane);
    STAMPT(0, 4);
    Pre pre; post_prefetch(pre, a, wid);
    const int t0 = (head * 20) >> 7, t1 = (head * 20 + 19) >> 7;
    if (wid == 1) await(ws, CT_QKV + (min(lane >> 1, 2) * 5 + ((lane & 1) ? t1 : t0)) * 8 + qc, 6, tag + (lane < 2 ? 2 : 16));
    PADT(1, 30);
    BAR();
    STAMPT(1, 0);
    attn_compute(ws, smem, head, qc, wid); PADT(2, 60); post_compute(a, smem, wid, pre, tag, t_start); PADT(0, 100);
  }
}

extern "C" void kernel_launch(void* const* d_in, const int* in_sizes, int n_in, void* d_out, int out_size, void* d_ws, size_t ws_size, hipStream_t stream) {
  Args a;
  a.x = (const u16*)d_in[0]; a.wqkv = (const u16*)d_in[1]; a.wo = (const u16*)d_in[2];
  a.w1 = (const u16*)d_in[4]; a.b1 = (const u16*)d_in[5]; a.w2 = (const u16*)d_in[6]; a.b2 = (const u16*)d_in[7];
  a.out = (u16*)d_out; a.ws = (char*)d_ws;
  (void)hipFuncSetAttribute((const void*)fused_block, hipFuncAttributeMaxDynamicSharedMemorySize, LDS_BYTES);
  fused_block<<<256, 512, LDS_BYTES, stream>>>(a);
}
```

```cpp
#include <hip/hip_runtime.h>
#include <hip/hip_bf16.h>
typedef unsigned short u16;
typedef __attribute__((ext_vector_type(8))) short bf16x8;
typedef __attribute__((ext_vector_type(8))) __bf16 bf8;
typedef __attribute__((ext_vector_type(4))) short s16x4;
typedef __attribute__((ext_vector_type(16))) float f32x16;
typedef __attribute__((ext_vector_type(4))) float f32x4;
typedef __attribute__((ext_vector_type(2))) float f32x2;
typedef __attribute__((ext_vector_type(4))) unsigned u32x4;
typedef __attribute__((ext_vector_type(2))) unsigned u32x2;
typedef __attribute__((ext_vector_type(2))) __bf16 bf2;
#define STR2(x) #x
#define STR(x) STR2(x)
#define LAS __attribute__((address_space(3)))
#define DI __device__ __forceinline__
#define SBAR() __builtin_amdgcn_sched_barrier(0)
#define LGKM0() __builtin_amdgcn_s_waitcnt(0xC07F)
#define BAR() asm volatile("s_barrier" ::: "memory")
#define WAITV(n) asm volatile("s_waitcnt vmcnt(%0)" :: "i"(n) : "memory")

constexpr int S = 2048, D = 640, H = 32, DH = 20, F = 1728;
constexpr int QT = 1280, KVT = 2688;
constexpr size_t WS_Q = 0, WS_KV = WS_Q + (size_t)H * 64 * QT, WS_O = WS_KV + (size_t)H * 64 * KVT,
                 WS_H = WS_O + (size_t)S * D * 2, WS_H1 = WS_H + (size_t)S * D * 2,
                 WS_ST = WS_H1 + (size_t)S * F * 2, WS_WO = WS_ST + (size_t)S * 128, WS_W1 = WS_WO + (size_t)D * D * 2, WS_W2 = WS_W1 + (size_t)D * F * 2 + 20480,
                 WS_CS1 = WS_W2 + (size_t)D * F * 2, WS_XST = WS_CS1 + 4 * 1792 * 4, WS_CSQ = WS_XST + (size_t)S * 16, WS_FL = WS_CSQ + 16384, WS_END = WS_FL + 81920;
#define STAMPT(k, s) do {} while (0)
#define STAMPH(k, s) do {} while (0)
#define PADAT -1
#define PADT(k, us) do { if (PADAT == (k)) { while (__builtin_amdgcn_s_memrealtime() - t_start < (us) * 100ull) __builtin_amdgcn_s_sleep(2); } } while (0)
constexpr int KSKEW = 1;
constexpr float QSCALE = 0.22360679774997896f * 1.4426950408889634f;

DI float bf2f(u16 v) { return __uint_as_float((unsigned)v << 16); }
DI float bflo(unsigned u) { return __uint_as_float(u << 16); }
DI float bfhi(unsigned u) { return __uint_as_float(u & 0xffff0000u); }
DI unsigned pk2(float lo, float hi) { f32x2 v = {lo, hi}; return __builtin_bit_cast(unsigned, __builtin_convertvector(v, bf2)); }
DI f32x4 unpack4(u32x2 v) { return f32x4{bflo(v[0]), bfhi(v[0]), bflo(v[1]), bfhi(v[1])}; }
DI u32x2 pack4(f32x4 v) { return u32x2{pk2(v[0], v[1]), pk2(v[2], v[3])}; }
DI float rbf(float v) { return bflo(pk2(v, 0.f)); }
DI void glds16(unsigned voff, const void* sbase, unsigned lds) {
  asm volatile("s_mov_b32 m0, %2\n\ts_nop 0\n\tglobal_load_lds_dwordx4 %0, %1" :: "v"(voff), "s"(sbase), "s"(__builtin_amdgcn_readfirstlane(lds)) : "memory"); }
DI void glds16x(unsigned voff, const void* sbase, unsigned lds) {
  asm volatile("s_mov_b32 m0, %2\n\ts_nop 0\n\tglobal_load_lds_dwordx4 %0, %1 sc1" :: "v"(voff), "s"(sbase), "s"(__builtin_amdgcn_readfirstlane(lds)) : "memory"); }
DI s16x4 trread(const LAS char* p) { return __builtin_amdgcn_ds_read_tr16_b64_v4i16((LAS s16x4*)p); }
DI bf16x8 cat8(s16x4 a, s16x4 b) { return __builtin_shufflevector(a, b, 0, 1, 2, 3, 4, 5, 6, 7); }
#define MFMA32(a, b, c) __builtin_amdgcn_mfma_f32_32x32x16_bf16(__builtin_bit_cast(bf8, a), __builtin_bit_cast(bf8, b), c, 0, 0, 0)
#define MFMA32K8(a, b, c) __builtin_amdgcn_mfma_f32_32x32x8bf16_1k(a, b, c, 0, 0, 0)
#define MFMA16(a, b, c) __builtin_amdgcn_mfma_f32_16x16x32_bf16(__builtin_bit_cast(bf8, a), __builtin_bit_cast(bf8, b), c, 0, 0, 0)
DI void mfma16_acc(f32x4& c, bf16x8 a, bf16x8 b) { asm volatile("v_mfma_f32_16x16x32_bf16 %0, %1, %2, %0" : "+v"(c) : "v"(a), "v"(b)); }
DI float xhalf_sum(float v) { auto r = __builtin_amdgcn_permlane32_swap(__float_as_uint(v), __float_as_uint(v), false, false); return __uint_as_float(r[0]) + __uint_as_float(r[1]); }
DI float xhalf_max(float v) { auto r = __builtin_amdgcn_permlane32_swap(__float_as_uint(v), __float_as_uint(v), false, false); return fmaxf(__uint_as_float(r[0]), __uint_as_float(r[1])); }
DI float xq_sum(float v) { auto r = __builtin_amdgcn_permlane16_swap(__float_as_uint(v), __float_as_uint(v), false, false); return xhalf_sum(__uint_as_float(r[0]) + __uint_as_float(r[1])); }
typedef __amdgpu_buffer_rsrc_t rsrc_t;
DI rsrc_t mkrsrc(const void* p, unsigned bytes) { return __builtin_amdgcn_make_buffer_rsrc((void*)p, 0, bytes, 0x00020000); }
constexpr int XAUX = 16;
DI bf16x8 ldfrag(rsrc_t r, unsigned voff, unsigned soff) { return __builtin_bit_cast(bf16x8, __builtin_amdgcn_raw_buffer_load_b128(r, voff, soff, XAUX)); }
DI void st16(rsrc_t r, unsigned voff, u32x4 v) { __builtin_amdgcn_raw_buffer_store_b128(v, r, voff, 0, XAUX); }
DI void st8(rsrc_t r, unsigned voff, u32x2 v) { __builtin_amdgcn_raw_buffer_store_b64(v, r, voff, 0, XAUX); }

DI s16x4 ldhalf(rsrc_t r, unsigned voff, unsigned soff) { return __builtin_bit_cast(s16x4, __builtin_amdgcn_raw_buffer_load_b64(r, voff, soff, XAUX)); }
DI void st16s(rsrc_t r, unsigned voff, unsigned soff, u32x4 v) { __builtin_amdgcn_raw_buffer_store_b128(v, r, voff + soff, 0, XAUX); }
DI void st8s(rsrc_t r, unsigned voff, unsigned soff, u32x2 v) { __builtin_amdgcn_raw_buffer_store_b64(v, r, voff, soff, XAUX); }
typedef unsigned long long u64;
typedef __attribute__((address_space(1))) u64 gu64;
#define RLX_AGENT __ATOMIC_RELAXED, __HIP_MEMORY_SCOPE_AGENT
constexpr int FL_EPOCH = 0, FL_W = 64  ;
constexpr int CT_BASE = 512, CT_QKV = 0  , CT_O = 128  , CT_H = 192  , CT_H1 = 256  ,
              CT_KV2 = 448  ;
constexpr unsigned SPIN_CAP = 1u << 17;
DI u64 tag_of(const u64& e) {
  u64 t;
  asm volatile("s_waitcnt lgkmcnt(0)\n\ts_mov_b64 %0, %1" : "=s"(t) : "s"(e) : "memory");
  return t * 6364136223846793005ull + 1442695040888963407ull;
}
DI gu64* flagp(char* ws, int i) { return (gu64*)(ws + WS_FL) + i; }
DI void signal(char* ws, int i, u64 tag) { __hip_atomic_store(flagp(ws, i), tag, RLX_AGENT); }
DI gu64* ctrp(char* ws, int i) { return (gu64*)(ws + WS_FL) + CT_BASE + i * 16; }
DI void arrive(char* ws, int i) { __hip_atomic_fetch_add(ctrp(ws, i), 1ull, RLX_AGENT); }
DI void await(char* ws, int i, int n, u64 want) {
  gu64* p = ctrp(ws, i);
  const bool on = (int)(threadIdx.x & 63) < n;
#pragma unroll 1
  for (unsigned sp = 0; sp < SPIN_CAP; ++sp) {
    u64 v = want;
    if (on) asm volatile("global_atomic_add_x2 %0, %1, %2, off sc0\n\ts_waitcnt vmcnt(0)" : "=v"(v) : "v"(p), "v"(0ull) : "memory");
    if (__all(v == want)) break;
  }
  asm volatile("" ::: "memory");
}

#define POLL_ASM "\n1:\n\tglobal_atomic_add_x2 %0, %2, %3, %4 sc0\n\ts_sub_u32 %1, %1, 1\n\ts_waitcnt vmcnt(0)\n\tv_cmp_ne_u64 vcc, %5, %0\n\ts_cbranch_scc1 2f\n\ts_nop 1\n\ts_cbranch_vccnz 1b\n2:\n\ts_mov_b64 exec, -1"
DI void poll(char* ws, unsigned vw, u64 want) {
  u64 v; unsigned n;
  asm volatile("s_mov_b32 %1, 0x20000\n\ts_mov_b64 exec, 1" POLL_ASM
               : "=&v"(v), "=&s"(n) : "v"(vw), "v"(0ull), "s"(ws + WS_FL + CT_BASE * 8), "s"(want) : "memory", "vcc", "scc");
}
template <int NV, int AMASK> DI void handoff(char* ws, unsigned va, unsigned vw, u64 want) {
  u64 v; unsigned n;
  asm volatile("s_mov_b32 %1, 0x20000\n\ts_waitcnt vmcnt(%8)\n\ts_mov_b64 exec, %9\n\tglobal_atomic_add_x2 %6, %7, %4\n\ts_mov_b64 exec, 1" POLL_ASM
               : "=&v"(v), "=&s"(n) : "v"(vw), "v"(0ull), "s"(ws + WS_FL + CT_BASE * 8), "s"(want), "v"(va), "v"(1ull), "i"(NV), "i"(AMASK) : "memory", "vcc", "scc");
}
template <int NV> DI void drain_arrive(char* ws, unsigned va) {
  asm volatile("s_waitcnt vmcnt(%3)\n\ts_mov_b64 exec, 1\n\tglobal_atomic_add_x2 %0, %1, %2\n\ts_mov_b64 exec, -1" :: "v"(va), "v"(1ull), "s"(ws + WS_FL + CT_BASE * 8), "i"(NV) : "memory");
}

DI void lds_post_(unsigned ctl, unsigned one, int OFF) { asm volatile("s_mov_b64 exec, 1\n\tds_add_u32 %0, %1 offset:%2\n\ts_mov_b64 exec, -1" :: "v"(ctl), "v"(one), "i"(OFF) : "memory"); }
template <int OFF> DI void lds_post(unsigned ctl, unsigned one) { lds_post_(ctl, one, OFF); }
template <int OFF, int NAP> DI void lds_wait(unsigned ctl) {
  unsigned v, t, n;
  asm volatile("s_mov_b32 %2, 0x100000\n\ts_branch 2f\n1:\n\ts_sleep %5\n2:\n\tds_read_b32 %0, %3 offset:%4\n\ts_sub_u32 %2, %2, 1\n\ts_waitcnt lgkmcnt(0)\n\tv_readfirstlane_b32 %1, %0\n\t"
               "s_cbranch_scc1 3f\n\ts_cmp_lt_u32 %1, 4\n\ts_cbranch_scc1 1b\n3:" : "=&v"(v), "=&s"(t), "=&s"(n) : "v"(ctl), "i"(OFF), "i"(NAP) : "memory", "scc");
}
#define NAP2 6
DI void lds_spin(unsigned v, unsigned ctl, int OFF) {
  unsigned n;
  asm volatile("v_cmp_gt_u32 vcc, 4, %1\n\ts_mov_b32 %0, 0x400000\n\ts_nop 0\n\ts_cbranch_vccz 2f\n"
               "1:\n\tds_read_b32 %1, %2 offset:%3\n\ts_sub_u32 %0, %0, 1\n\ts_waitcnt lgkmcnt(0)\n\tv_cmp_gt_u32 vcc, 4, %1\n\ts_cbranch_scc1 2f\n\ts_nop 1\n\ts_cbranch_vccnz 1b\n2:"
               : "=&s"(n), "+v"(v) : "v"(ctl), "i"(OFF) : "memory", "scc", "vcc");
}
constexpr int CTL1 = 133120, CTL2 = 57344;

#define CODEB 56320
constexpr int K1_NSLOT = 4, K1_STAGE = 32768, K1_STAT = K1_NSLOT * K1_STAGE, CODE_BYTES = CODEB;
#define NAP1 0
#define STAG 12
#define XS -1
#define DM 2
#define FLM 9
#define K1W 3
#define MKS 0
#define K1_PRE 2
DI float dot2(unsigned a, unsigned b, float c) { return __builtin_amdgcn_fdot2_f32_bf16(__builtin_bit_cast(bf2, a), __builtin_bit_cast(bf2, b), c, false); }
template <int CTRL> DI float dpp_add(float v) { return v + __builtin_bit_cast(float, __builtin_amdgcn_update_dpp(0, __builtin_bit_cast(int, v), CTRL, 0xf, 0xf, true)); }
DI float sum32(float v) {
  v = dpp_add<0x128>(v); v = dpp_add<0x124>(v); v = dpp_add<0x122>(v); v = dpp_add<0x121>(v);
  auto r = __builtin_amdgcn_permlane16_swap(__float_as_uint(v), __float_as_uint(v), false, false); return __uint_as_float(r[0]) + __uint_as_float(r[1]);
}
struct XRow { u32x4 a, b; u32x2 c; };
DI void xrow_load(XRow& v, const u16* x, int row0) {
  const int lane = threadIdx.x & 63;
  const rsrc_t rx = mkrsrc(x, S * D * 2);
  const unsigned o = (row0 + (lane >> 5)) * 1280 + (lane & 31) * 16;
  v.a = __builtin_amdgcn_raw_buffer_load_b128(rx, o, 0, 0); v.b = __builtin_amdgcn_raw_buffer_load_b128(rx, o + 512, 0, 0); v.c = __builtin_amdgcn_raw_buffer_load_b64(rx, o + 1024 - (lane & 31) * 8, 0, 0);
}
DI void xrow_publish(const XRow& v, char* ws, int row0, u64 tag) {
  const int lane = threadIdx.x & 63;
  const unsigned e[10] = {v.a[0], v.a[1], v.a[2], v.a[3], v.b[0], v.b[1], v.b[2], v.b[3], v.c[0], v.c[1]};
  float s = 0.f, q = 0.f;
#pragma unroll
  for (int i = 0; i < 10; ++i) { s = dot2(e[i], 0x3F803F80u, s); q = dot2(e[i], e[i], q); }
  const float m = sum32(s) * (1.f / D), var = fmaxf(sum32(q) * (1.f / D) - m * m, 0.f);
  const float rstd = __builtin_amdgcn_rcpf(rbf(__builtin_amdgcn_sqrtf(rbf(rbf(var) + 1e-5f))));
  if ((lane & 31) == 0) st16(mkrsrc(ws + WS_XST, S * 16), (row0 + (lane >> 5)) * 16, u32x4{__float_as_uint(rbf(m)), (unsigned)tag, __float_as_uint(rstd), (unsigned)(tag >> 32)});
}
struct K1Src { rsrc_t ra, rb; unsigned a0, a1, b0, b1; };
DI void k1_stage(const K1Src& k, int ks, unsigned dst) {
  ks = __builtin_amdgcn_readfirstlane(ks); dst = __builtin_amdgcn_readfirstlane(dst);
  unsigned sa = ks * 128, sb = ks * (64 * 3840);
#define K1P(v, r, s, inc) "buffer_load_dwordx4 " v ", " r ", " s " offen lds\n\ts_add_u32 m0, m0, 0x400\n\ts_add_u32 " s ", " s ", " inc "\n\t"
  asm volatile("s_mov_b32 m0, %8\n\ts_nop 4\n\t"
               ".rept 4\n\t" K1P("%2", "%6", "%0", "0x2800") K1P("%3", "%6", "%0", "0x2800") K1P("%3", "%6", "%0", "0x2800") K1P("%2", "%6", "%0", "0x2800") ".endr\n\t"
               ".rept 4\n\t" K1P("%4", "%7", "%1", "0x3c00") K1P("%4", "%7", "%1", "0x3c00") K1P("%5", "%7", "%1", "0x3c00") K1P("%5", "%7", "%1", "0x3c00") ".endr"
               : "+s"(sa), "+s"(sb) : "v"(k.a0), "v"(k.a1), "v"(k.b0), "v"(k.b1), "s"(k.ra), "s"(k.rb), "s"(dst) : "memory", "scc");
}
template <bool isV, class Pub> DI void k1_compute(Pub publish, char* ws, char* smem, int bm, int bn, int wid, const XRow& xr, const u64& ep, u64& tag, u64 t_start) {
  const int lane = threadIdx.x & 63, r = lane & 15, g = lane >> 4, q = r >> 2;
  const int wm = wid >> 1, wn = wid & 1;
  f32x4 c[4][4];
  const LAS char* sm = (const LAS char*)smem;
  const int arow = isV ? 16 * (q & 1) + 4 * (q >> 1) + (r & 3) : r;
  const LAS char* pa[2][2]; const LAS char* pb[2][4];
#pragma unroll
  for (int w = 0; w < 2; ++w) {
#pragma unroll
    for (int s = 0; s < 2; ++s) { pa[w][s] = sm + w * 65536 + (wm * 64 + arow) * 128 + (((4 * s + g) ^ ((arow >> 1) & 3) ^ ((((arow >> 3) ^ (arow >> 4)) & 1) << 2)) << 4); asm volatile("" : "+v"(pa[w][s])); }
#pragma unroll
    for (int ct = 0; ct < 4; ++ct) { pb[w][ct] = sm + w * 65536 + 16384 + (8 * g + q) * 256 + (((4 * wn + ct) ^ (2 * q + (g & 1))) << 5) + 8 * (r & 3); asm volatile("" : "+v"(pb[w][ct])); }
  }
  auto lda = [&](int i, int t) { const int sl = (i >> 1) % K1_NSLOT; return *(const LAS bf16x8*)(pa[sl >> 1][(i ^ t) & 1] + (sl & 1) * K1_STAGE + (isV ? (t >> 1) * 4096 + (t & 1) * 1024 : t * 2048)); };
  auto ldb = [&](int i, int ct, int h) { const int sl = (i >> 1) % K1_NSLOT; return trread(pb[sl >> 1][ct] + (sl & 1) * K1_STAGE + (i & 1) * 8192 + h * 1024); };
  const bool isQ = bn < 5; const int tstride = isQ ? QT : KVT;
  const rsrc_t rimg = mkrsrc(ws + (isQ ? WS_Q : WS_KV), H * 64 * KVT);
  unsigned soff[8];
  auto mkoff = [&](int u) {
    if (!isV) {
      const int G = (bn % 5) * 16 + wn * 8 + 2 * u + (g >> 1), pr = G / 5, g5 = G - pr * 5;
      soff[u] = g5 == 2 ? (2 * pr + (g & 1)) * 64 * tstride + 1024 + r * 8 : (2 * pr + (g5 > 2)) * 64 * tstride + (g5 > 2 ? g5 - 3 : g5) * 512 + r * 16 + (g & 1) * 8;
      soff[4 + u] = soff[u] + (g5 == 2 ? 128 : 256);
      asm volatile("" : "+v"(soff[u]), "+v"(soff[4 + u]));
    } else {
      const int n = (bn - 10) * 128 + wn * 64 + u * 16 + r, head = n / 20, d = n - head * 20;
      soff[u] = head * 64 * KVT + 1280 + d * 64 + ((g ^ (d >> 2)) & 3) * 16;
      asm volatile("" : "+v"(soff[u]));
    }
  };
  SBAR();
  tag = tag_of(ep); xrow_publish(xr, ws, blockIdx.x * 8 + wid * 2, tag);
  unsigned ctl = (unsigned)(uintptr_t)smem + CTL1, one = 1, fl = 0;
  asm volatile("" : "+v"(ctl), "+v"(one));
  lds_wait<0, 0>(ctl); SBAR();
  PADT(3, 15);
  STAMPT(0, 1);
  bf16x8 fa[2][4]; s16x4 fb[2][4][2]; f32x4 sa[4], sb[4]; float sc[8];
  const LAS float* sMean = (const LAS float*)(smem + K1_STAT); const LAS float* sRstd = sMean + 128; const LAS float* sCs = sMean + 256;
  auto fetch = [&](int i, int n) { if (n == 0 || n > 8) fa[i & 1][n ? n - 8 : 0] = lda(i, n ? n - 8 : 0); else fb[i & 1][(n - 1) >> 1][(n - 1) & 1] = ldb(i, (n - 1) >> 1, (n - 1) & 1); };
#pragma unroll
  for (int n = 0; n < 12; ++n) fetch(0, n);
  SBAR();
#pragma unroll
  for (int i = 0; i < 20; ++i) {
    __builtin_amdgcn_s_waitcnt(0xC07F | (K1W << 8));
    SBAR();
#pragma unroll
    for (int m = 0; m < 16; ++m) {
      const bf16x8 a = fa[i & 1][m >> 2], b = cat8(fb[i & 1][m & 3][0], fb[i & 1][m & 3][1]);
      if (i == 0) asm volatile("v_mfma_f32_16x16x32_bf16 %0, %1, %2, 0" : "=&v"(c[m >> 2][m & 3]) : "v"(isV ? a : b), "v"(isV ? b : a));
      else mfma16_acc(c[m >> 2][m & 3], isV ? a : b, isV ? b : a);
      SBAR();
      if ((i & 1) && m == 0) { lds_spin(fl, ctl, 2 * i + 2); SBAR(); }
      if ((i & 1) && m == DM && i < 12) { __builtin_amdgcn_s_waitcnt(0xC07F | ((DM - 1) << 8)); lds_post_(ctl, one, 64 + 2 * (i - 1)); SBAR(); }
      if (!(i & 1) && m == FLM) { fl = *(const volatile LAS unsigned*)(uintptr_t)(ctl + 2 * i + 4); SBAR(); }
      if (i < 19 && m >= 1 && m < 13) { fetch(i + 1, m - 1); SBAR(); }
      if (i == 19 && m >= 1 && m < 5) {
        const int u = m - 1;
        if (!isV) { sa[u] = *(const LAS f32x4*)(sCs + wn * 64 + 16 * u + 4 * g); sc[u] = sMean[wm * 64 + 16 * u + r]; sc[4 + u] = sRstd[wm * 64 + 16 * u + r]; }
        else { const int t0 = wm * 64 + 32 * (u >> 1) + 8 * (u & 1) + 16 * (g & 1) + 4 * (g >> 1);
               sa[u] = *(const LAS f32x4*)(sMean + t0); sb[u] = *(const LAS f32x4*)(sRstd + t0); sc[u] = sCs[wn * 64 + 16 * u + r]; }
        SBAR();
      }
    }
    if (i == XS) { tag = tag_of(ep); xrow_publish(xr, ws, blockIdx.x * 8 + wid * 2, tag); SBAR(); }
    if (i >= MKS && i < MKS + 4) { mkoff(i - MKS); SBAR(); }
  }
  asm volatile("s_nop 7" ::: "memory"); SBAR();
  PADT(6, 22);
  STAMPT(0, 2);
  const int pos0 = bm * 2 + wm;
  if (!isV) {
    u32x2 late[8];
#pragma unroll
    for (int t = 0; t < 4; ++t) {
      const float rstd = sc[4 + t] * (isQ ? QSCALE : 1.f), mr = -sc[t] * rstd;
#pragma unroll
      for (int u = 0; u < 4; ++u) {
        const u32x2 v = pack4(c[t][u] * rstd + sa[u] * mr);
        if (t < 2) st8s(rimg, soff[4 * t + u], (isQ ? 2 * pos0 : ((pos0 + 64 - KSKEW) & 63)) * tstride, v); else late[4 * (t - 2) + u] = v;
      }
    }
#pragma unroll
    for (int u = 0; u < 8; ++u) asm volatile("" : "+v"(late[u]));
    if (!isQ) publish();
#pragma unroll
    for (int u = 0; u < 8; ++u) st8s(rimg, soff[u], (isQ ? 2 * pos0 + 1 : pos0 + 32 - KSKEW) * tstride, late[u]);
    if (isQ) publish();
  } else {
    u32x4 late[4];
#pragma unroll
    for (int i = 0; i < 2; ++i) {
#pragma unroll
      for (int u = 0; u < 4; ++u) {
        const u32x2 lo = pack4((c[2 * i][u] - sa[2 * i] * sc[u]) * sb[2 * i]), hi = pack4((c[2 * i + 1][u] - sa[2 * i + 1] * sc[u]) * sb[2 * i + 1]);
        const u32x4 v = {lo[0], lo[1], hi[0], hi[1]};
        if (i == 0) st16s(rimg, soff[u], pos0 * KVT, v); else late[u] = v;
      }
    }
#pragma unroll
    for (int u = 0; u < 4; ++u) asm volatile("" : "+v"(late[u]));
    publish();
#pragma unroll
    for (int u = 0; u < 4; ++u) st16s(rimg, soff[u], (pos0 + 32) * KVT, late[u]);
  }
}
struct Args { const u16 *x, *wqkv; char* ws; const u16 *wo, *w1, *b1, *w2, *b2; u16* out; };
constexpr int LDS_RT = 143360;
DI int g0of(int c4) { return (c4 * 27) >> 2; }
struct Retile { unsigned rv[5], so0, so4, cso; const LAS char* tp[10]; };
DI void retile_plan(Retile& t, const LAS char* img, int hw) {
  const int lane = threadIdx.x & 63, b = blockIdx.x;
  const bool is1 = b >= 40 && b < 148;
  const int jb = b < 40 ? b : (is1 ? b - 40 : b - 148);
  if (is1) {
#pragma unroll
    for (int i = 0; i < 5; ++i) {
      const int p = hw * 5 + i, ks = (jb & 3) * 5 + (p >> 2), u = (p >> 1) & 1;
      const int tile = ks < 16 ? 5 * (ks >> 1) + 2 * (ks & 1) + u : 5 * (2 * (ks - 16) + u) + 4;
      t.rv[i] = (tile * 16 + (p & 1) * 8 + (lane >> 3)) * (F * 2) + (jb >> 2) * 128 + (lane & 7) * 16;
    }
  } else {
    const int ks = jb >> 1, u = hw >> 1;
    int row0, skip = 0;
    if (b < 40) { if (ks < 16) row0 = (2 * ks + (hw & 1)) * 20 + 8 * u; else { row0 = ((ks - 16) * 8 + 2 * hw) * 20 + 16; skip = 16; } }
    else { const int sg = 2 * (ks - 48) + u; row0 = (ks < 48 ? g0of(ks & 15) + 2 * (ks >> 4) + u : g0of(sg + sg / 3 + 1) + 6) * 16 + (hw & 1) * 8; }
#pragma unroll
    for (int i = 0; i < 5; ++i) { const int idx = i * 64 + lane, row = idx / 40; t.rv[i] = (row0 + row + (row >> 2) * skip) * (D * 2) + (jb & 1) * 640 + (idx - row * 40) * 16; }
  }
  const int rs = is1 ? 128 : 640;
#pragma unroll
  for (int k = 0; k < 10; ++k) t.tp[k] = img + ((lane >> 2) + (k & 1) * 16) * rs + (lane & 3) * 8 + (is1 ? hw * 32 + (k >> 1) * 4096 : hw * 160 + (k >> 1) * 32);
  const int d0 = is1 ? ((jb >> 2) * 4 + hw) * 20 + (jb & 3) * 5 : (((jb & 1) * 4 + hw) * (b < 40 ? 20 : 54) + (jb >> 1)) * 5;
  t.so0 = (unsigned)(b < 40 ? 0 : (is1 ? WS_W1 - WS_WO : WS_W2 - WS_WO)) + d0 * 1024 + lane * 16; t.so4 = t.so0 + 4096;
  t.cso = (is1 && (lane & 15) == 0) ? (unsigned)(WS_CS1 - WS_WO) + ((jb & 3) * 1792 + 16 * ((jb >> 2) * 4 + hw) + (lane >> 4) * 4) * 4 : (unsigned)(WS_XST - WS_WO);
#pragma unroll
  for (int i = 0; i < 5; ++i) asm volatile("" : "+v"(t.rv[i]), "+v"(t.tp[2 * i]), "+v"(t.tp[2 * i + 1]));
  asm volatile("" : "+v"(t.so0), "+v"(t.so4), "+v"(t.cso));
}
DI void retile_store(const Retile& t, char* ws, bf16x8 ones) {
  const rsrc_t rw = mkrsrc(ws + WS_WO, WS_XST - WS_WO);
  f32x4 cs = f32x4{};
#pragma unroll
  for (int k = 0; k < 5; ++k) {
    const bf16x8 v = cat8(trread(t.tp[2 * k]), trread(t.tp[2 * k + 1]));
    st16(rw, k < 4 ? t.so0 + k * 1024 : t.so4, __builtin_bit_cast(u32x4, v));
    cs = MFMA16(v, ones, cs);
  }
  st16(rw, t.cso, __builtin_bit_cast(u32x4, cs));
}
DI void k1_helper(const K1Src& k, char* ws, char* smem, int bm, int bn, int rot, int hw, const u64& ep, u64& tag) {
  const int lane = threadIdx.x & 63;
  const unsigned lds0 = (unsigned)(uintptr_t)smem;
  unsigned ctl = lds0 + CTL1 + hw * 4, four = 4;
  asm volatile("" : "+v"(ctl), "+v"(four));
  tag = tag_of(ep);
  WAITV(0); lds_post<0>(ctl, four);
  lds_wait<64, NAP1>(ctl);
  k1_stage(k, (hw + 4 + rot) % 10, lds0 + hw * K1_STAGE);
  WAITV(0); lds_post<16>(ctl, four);
  if (hw < 2) { lds_wait<80, NAP1>(ctl); k1_stage(k, (hw + 8 + rot) % 10, lds0 + hw * K1_STAGE); }
  else lds_wait<72, NAP1>(ctl);
  {
    const int h0 = ((bn - 10) * 128 + 1) / 20, hd = h0 + (lane >> 3);
    const unsigned c1 = (lane & 4) ? 0u : 0x3F803F80u;
    st16(mkrsrc(ws + WS_KV, H * 64 * KVT), (bn >= 10 && hd * 20 + 18 < (bn - 9) * 128) ? (hd * 64 + (hw & 1) * 32 + bm * 2 + (hw >> 1)) * KVT + 2560 + (lane & 7) * 16 : H * 64 * KVT, u32x4{c1, c1, c1, c1});
  }
  STAMPH(7, 0);
  const rsrc_t rs = mkrsrc(ws + WS_XST, S * 16 + 16384);
  const unsigned go = lane < 32 ? (bm * 128 + hw * 32 + lane) * 16 : S * 16 + (bn * 64 + hw * 16 + (lane & 15)) * 16;
  u32x4 rec = __builtin_amdgcn_raw_buffer_load_b128(rs, go, 0, XAUX);
  SBAR(); WAITV(2);
  if (hw < 2) lds_post<32>(ctl, four);
#pragma unroll 1
  for (unsigned sp = 0; sp < SPIN_CAP; ++sp) {
    if (__all(rec[1] == (unsigned)tag && rec[3] == (unsigned)(tag >> 32))) break;
    __builtin_amdgcn_s_sleep(2);
    asm volatile("" ::: "memory");
    rec = __builtin_amdgcn_raw_buffer_load_b128(rs, go, 0, XAUX);
  }
  STAMPH(7, 1);
  LAS unsigned* st = (LAS unsigned*)(smem + K1_STAT);
  if (lane < 32) { st[hw * 32 + lane] = rec[0]; st[128 + hw * 32 + lane] = rec[2]; }
  else if (lane < 48) { st[256 + hw * 32 + 2 * (lane & 15)] = rec[0]; st[257 + hw * 32 + 2 * (lane & 15)] = rec[2]; }
  LGKM0(); lds_post<40>(lds0 + CTL1, 1);
}
DI u64 k1_colsum(const u16* __restrict__ w, char* ws, char* smem, int u, int wid, const XRow& xr, const u64& ep) {
  const int lane = threadIdx.x & 63;
  const rsrc_t rw = mkrsrc(w, D * 3 * D * 2);
  bf16x8 v[20];
#pragma unroll
  for (int i = 0; i < 20; ++i) v[i] = __builtin_bit_cast(bf16x8, __builtin_amdgcn_raw_buffer_load_b128(rw, (wid * 80 + (lane >> 4)) * 3840 + min(u, 14) * 256 + (lane & 15) * 16, i * 4 * 3840, 0));
  SBAR();
  const u64 tag = tag_of(ep);
  if (wid == 0 && lane < 16 && u < 15) __hip_atomic_store(ctrp(ws, (lane < 8 ? CT_QKV : CT_KV2) + u * 8 + (lane & 7)), tag, RLX_AGENT);
  if (wid < 4) xrow_publish(xr, ws, blockIdx.x * 8 + wid * 2, tag);
  bf16x8 sel = bf16x8{};
#pragma unroll
  for (int j = 0; j < 8; ++j) sel[j] = (lane & 15) == j ? (short)0x3F80 : (short)0;
  f32x4 acc = f32x4{};
#pragma unroll
  for (int i = 0; i < 20; ++i) acc = MFMA16(v[i], sel, acc);
  LAS float* part = (LAS float*)smem;
  if ((lane & 15) < 8) {
#pragma unroll
    for (int i = 0; i < 4; ++i) part[wid * 128 + ((lane >> 4) * 4 + i) * 8 + (lane & 15)] = acc[i];
  }
  LGKM0(); BAR();
  if (wid == 0 && u < 15) {
    float c0 = 0.f, c1 = 0.f;
#pragma unroll
    for (int k = 0; k < 8; ++k) { c0 += part[k * 128 + 2 * lane]; c1 += part[k * 128 + 2 * lane + 1]; }
    WAITV(0);
    st16(mkrsrc(ws + WS_CSQ, 16384), (u * 64 + lane) * 16, u32x4{__float_as_uint(c0), (unsigned)tag, __float_as_uint(c1), (unsigned)(tag >> 32)});
  }
  STAMPT(5, 0);
  return tag;
}

constexpr int CH_TILES = 8, CH_BYTES = CH_TILES * KVT, NCH = 64 / CH_TILES, CH_PIECES = CH_BYTES / 1024;
#define EX(v) __builtin_amdgcn_exp2f(v)
#define PF(p) __builtin_bit_cast(bf16x8, p)
DI float rowmax16(const f32x16& s) {
  float m = s[0];
#pragma unroll
  for (int e = 1; e < 16; ++e) m = fmaxf(m, s[e]);
  return xhalf_max(m);
}
DI s16x4 shift_from(const f32x16& s, s16x4 q2, int hh) {
  float m = s[0];
#pragma unroll
  for (int e = 1; e < 15; e += 2) asm("v_max3_f32 %0, %0, %1, %2" : "+v"(m) : "v"(s[e]), "v"(s[e + 1]));
  m = -fmaxf(m, s[15]);
  const unsigned mh = pk2(m, 0.f) & 0xffffu, ml = pk2(m - bflo(mh), 0.f) & 0xffffu;
  return hh ? s16x4{(short)mh, (short)ml, 0, 0} : q2;
}
DI bool attn_unsafe(const f32x16& o, int hh) {
  const float l = hh ? o[8] : o[12];
  float t = 0.f;
#pragma unroll
  for (int e = 0; e < 12; e += 4) t += (fabsf(o[e]) + fabsf(o[e + 1])) + (fabsf(o[e + 2]) + fabsf(o[e + 3]));
  return __any(!(l >= 1e-20f && l <= 1e30f && t <= 3e38f));
}
DI f32x16 attn_online(rsrc_t kv, bf16x8 q1, s16x4 q2, int lane, int k2off, int voff) {
  if (lane >= 32) q2 = s16x4{0, 0, 0, 0};
  float m = -INFINITY; f32x16 o = f32x16{};
#pragma unroll 1
  for (int t = 0; t < 64; ++t) {
    const int kt = ((t + 64 - KSKEW) & 63) * KVT, vt = t * KVT;
    f32x16 s = MFMA32(ldfrag(kv, lane * 16, kt), q1, f32x16{}); s = MFMA32K8(ldhalf(kv, k2off, kt), q2, s);
    const float mn = fmaxf(m, rowmax16(s)), al = EX(m - mn); m = mn;
#pragma unroll
    for (int e = 0; e < 16; ++e) { o[e] *= al; s[e] = EX(s[e] - mn); }
    const u32x4 p0 = {pk2(s[0], s[1]), pk2(s[2], s[3]), pk2(s[4], s[5]), pk2(s[6], s[7])};
    const u32x4 p1 = {pk2(s[8], s[9]), pk2(s[10], s[11]), pk2(s[12], s[13]), pk2(s[14], s[15])};
    o = MFMA32(ldfrag(kv, voff, vt), PF(p0), o); o = MFMA32(ldfrag(kv, voff ^ 16, vt), PF(p1), o);
  }
  return o;
}
#define AL 1
#define RS0 4
#define RS1 4
#define RS2 4
#define RS3 4
#define ALPAD " .rept " STR(AL) "\n s_nop 0\n .endr\n"
#define WPAD ""
template <class Sync> DI void attn_compute(char* ws, char* smem, int head, int qc, int wid, Sync sync) {
  const int lane = threadIdx.x & 63, r = lane & 31, hh = lane >> 5;
  const rsrc_t kv = mkrsrc(ws + WS_KV + (size_t)head * 64 * KVT, 64 * KVT);
  const int qt = qc * 8 + wid * 2;
  const rsrc_t rq = mkrsrc(ws + WS_Q + (size_t)(head * 64 + qt) * QT, 2 * QT);
  const int k2off = hh ? 2560 : 1024 + r * 8;
  const int vr = r < 21 ? r : (r == 24 ? 20 : 21), voff = 1280 + vr * 64 + (((2 * hh) ^ (vr >> 2)) & 3) * 16;
  unsigned ooff0 = ((qt * 2 + (r >> 4)) * 20 + (head >> 1)) * 1024 + (((head & 1) * 2 + hh) * 16 + (r & 15)) * 16;
  unsigned ooff1 = hh ? S * D * 2 : ((qt * 2 + (r >> 4)) * 20 + 16 + (head >> 3)) * 1024 + ((head & 7) * 16 + (r & 15)) * 8;
  asm volatile("" : "+v"(ooff0), "+v"(ooff1));
  const unsigned lds0 = (unsigned)(uintptr_t)smem;
  unsigned ctl = lds0 + CTL2, ak1 = lds0 + lane * 16, ak2 = lds0 + k2off, av0 = lds0 + voff, av1 = lds0 + (voff ^ 16), q2o = hh ? 2 * QT : 1024 + r * 8;
  f32x16 oa = f32x16{}, ob = f32x16{}; u32x4 z0 = u32x4{}, z1 = u32x4{}, z2 = u32x4{}, z3 = u32x4{}, z4 = u32x4{};
  asm volatile("" : "+{v[8:23]}"(oa), "+{v[24:39]}"(ob), "+{v[68:71]}"(z0), "+{v[80:83]}"(z1), "+{v[84:87]}"(z2), "+{v[92:95]}"(z3), "+{v[96:99]}"(z4),
                    "+{v128}"(ak1), "+{v129}"(ak2), "+{v130}"(av0), "+{v134}"(av1), "+{v131}"(ctl), "+v"(q2o));
  sync();
  bf16x8 k1 = ldfrag(kv, lane * 16, 63 * KVT); s16x4 k2 = ldhalf(kv, k2off, 63 * KVT);
  const bf16x8 qa1 = ldfrag(rq, lane * 16, 0), qb1 = ldfrag(rq, lane * 16, QT);
  s16x4 qa2 = ldhalf(rq, q2o, 0), qb2 = ldhalf(rq, q2o, QT);
  { f32x16 s = MFMA32(k1, qa1, f32x16{}), t = MFMA32(k1, qb1, f32x16{}); s = MFMA32K8(k2, qa2, s); t = MFMA32K8(k2, qb2, t);
    asm volatile("s_nop 15" : "+v"(s), "+v"(t));
    qa2 = shift_from(s, qa2, hh); qb2 = shift_from(t, qb2, hh); }
  STAMPT(1, 1);
  asm volatile(
    ".macro AT_E r\n v_exp_f32 v[\\r], v[\\r]\n v_exp_f32 v[\\r+1], v[\\r+1]\n.endm\n"
    ".macro AT_C d, r\n v_cvt_pk_bf16_f32 v[\\d], v[\\r], v[\\r+1]\n.endm\n"
    ".macro AT_R y, yk2, roff, slot\n"
    " .if (\\slot) == " STR(RS0) "\n ds_read_b128 v[\\y+4:\\y+7], v130 offset:\\roff\n .endif\n"
    " .if (\\slot) == " STR(RS1) "\n ds_read_b128 v[\\y:\\y+3], v128 offset:\\roff\n .endif\n"
    " .if (\\slot) == " STR(RS2) "\n ds_read_b64 v[\\yk2:\\yk2+1], v129 offset:\\roff\n .endif\n"
    " .if (\\slot) == " STR(RS3) "\n ds_read_b128 v[\\y+8:\\y+11], v134 offset:\\roff\n .endif\n"
    ".endm\n"
    ".macro AT_H sc, sn, pc, pp, o, x, xk2, q1, q2, y, yk2, roff, hb, off, mode\n"
    " v_mfma_f32_32x32x16_bf16 v[\\o:\\o+15], v[\\x+4:\\x+7], v[\\pp:\\pp+3], v[\\o:\\o+15]\n"
    " AT_R \\y, \\yk2, \\roff, \\hb*4+1\n"
    " AT_C \\pp+6, \\sn+12\n AT_C \\pp+7, \\sn+14\n AT_E \\sc\n AT_E \\sc+2\n"
    " .if (\\mode & 16) == 0\n v_mfma_f32_32x32x16_bf16 v[\\sn:\\sn+15], v[\\x:\\x+3], v[\\q1:\\q1+3], 0\n .endif\n"
    " AT_R \\y, \\yk2, \\roff, \\hb*4+2\n"
    " .if \\mode & 4\n ds_read_b32 v133, v131 offset:\\off\n .endif\n"
    " AT_E \\sc+4\n AT_C \\pc, \\sc\n AT_E \\sc+6\n AT_C \\pc+1, \\sc+2\n"
    " .if (\\mode & 16) == 0\n v_mfma_f32_32x32x8_bf16 v[\\sn:\\sn+15], v[\\xk2:\\xk2+1], v[\\q2:\\q2+1], v[\\sn:\\sn+15]\n .endif\n"
    " AT_R \\y, \\yk2, \\roff, \\hb*4+3\n"
    " AT_E \\sc+8\n AT_C \\pc+2, \\sc+4\n AT_E \\sc+10\n AT_C \\pc+3, \\sc+6\n"
    " v_mfma_f32_32x32x16_bf16 v[\\o:\\o+15], v[\\x+8:\\x+11], v[\\pp+4:\\pp+7], v[\\o:\\o+15]\n"
    " AT_R \\y, \\yk2, \\roff, \\hb*4+4\n"
    " .if \\mode & 2\n s_waitcnt lgkmcnt(0)\n" WPAD " .endif\n"
    " .if \\mode & 4\n s_mov_b64 exec, 1\n ds_add_u32 v131, v132 offset:\\off+60\n s_mov_b64 exec, -1\n v_cmp_gt_u32 vcc, 4, v133\n .endif\n"
    " AT_E \\sc+12\n AT_C \\pc+4, \\sc+8\n AT_E \\sc+14\n AT_C \\pc+5, \\sc+10\n"
    ".endm\n"
    ".macro AT_A x, xk2, y, yk2, t\n AT_H 56, 40, 80, 72, 8, \\x, \\xk2, 116, 124, \\y, \\yk2, (\\t)*2688, 0, 0, 0\n.endm\n"
    ".macro AT_B x, xk2, t, off, mode\n AT_H 40, 56, 72, 80, 24, \\x, \\xk2, 120, 126, 192-\\x, 202-\\xk2, (\\t)*2688, 1, \\off, \\mode\n.endm\n"
    ".macro AT_2 t\n AT_A 104, 102, 88, 100, \\t\n AT_B 104, 102, \\t, 0, 2\n AT_A 88, 100, 104, 102, \\t+1\n AT_B 88, 100, \\t+1, 0, 2\n.endm\n"
    ".macro AT_SPIN off, back\n"
    "1: s_cmp_eq_u32 s41, 0\n s_cbranch_scc1 \\back\n s_sub_u32 s41, s41, 1\n s_sleep 1\n ds_read_b32 v133, v131 offset:\\off\n s_waitcnt lgkmcnt(0)\n v_cmp_gt_u32 vcc, 4, v133\n s_nop 4\n s_cbranch_vccnz 1b\n s_branch \\back\n"
    ".endm\n"
    " s_nop 1\n"
    " v_mfma_f32_32x32x16_bf16 v[40:55], v[88:91], v[116:119], 0\n"
    " v_mfma_f32_32x32x8_bf16 v[40:55], v[100:101], v[124:125], v[40:55]\n"
    " v_mov_b32 v132, 1\n"
    " s_mov_b32 s40, 4\n s_mov_b32 s41, 0x400000\n"
    ".Lat_w0: s_sub_u32 s41, s41, 1\n s_cbranch_scc1 .Lat_w1\n ds_read_b32 v133, v131\n s_waitcnt lgkmcnt(0)\n v_cmp_gt_u32 vcc, 4, v133\n s_nop 4\n s_cbranch_vccnz .Lat_w0\n"
    ".Lat_w1: s_mov_b32 s41, 0x400000\n"
    " ds_read_b128 v[108:111], v130\n ds_read_b128 v[104:107], v128\n ds_read_b64 v[102:103], v129\n ds_read_b128 v[112:115], v134\n"
    " s_nop 7\n s_branch .Lat_mid\n"
    ".Lat_s1: AT_SPIN 4, .Lat_b1\n"
    ".Lat_s2: AT_SPIN 8, .Lat_b2\n"
    " .p2align 6\n" ALPAD
    ".Lat_loop:\n"
    " AT_A 88, 100, 104, 102, 0\n"
    ".Lat_mid:\n"
    " AT_B 88, 100, 0, 0, 2\n"
    " AT_2 1\n AT_2 3\n AT_2 5\n"
    " AT_A 104, 102, 88, 100, 7\n AT_B 104, 102, 7, 4, 6\n"
    " s_cbranch_vccnz .Lat_s1\n"
    ".Lat_b1:\n"
    " AT_A 88, 100, 104, 102, 8\n AT_B 88, 100, 8, 0, 2\n"
    " AT_2 9\n AT_2 11\n AT_2 13\n"
    " AT_A 104, 102, 88, 100, 15\n AT_B 104, 102, 15, 8, 6\n"
    " s_cbranch_vccnz .Lat_s2\n"
    ".Lat_b2:\n"
    " v_add_u32 v131, 8, v131\n"
    " s_sub_u32 s40, s40, 1\n s_cmp_lg_u32 s40, 0\n s_cbranch_scc1 .Lat_loop\n"
    " AT_H 56, 40, 80, 72, 8, 88, 100, 116, 124, 0, 0, 0, 9, 0, 16\n"
    " AT_C 86, 68\n AT_C 87, 70\n"
    " v_mfma_f32_32x32x16_bf16 v[24:39], v[92:95], v[80:83], v[24:39]\n s_nop 0\n"
    " v_mfma_f32_32x32x16_bf16 v[24:39], v[96:99], v[84:87], v[24:39]\n"
    " s_nop 12\n"
    : "+{v[8:23]}"(oa), "+{v[24:39]}"(ob), "+{v131}"(ctl), "+{v[88:91]}"(k1), "+{v[100:101]}"(k2),
      "+{v[68:71]}"(z0), "+{v[80:83]}"(z1), "+{v[84:87]}"(z2), "+{v[92:95]}"(z3), "+{v[96:99]}"(z4)
    : "{v[116:119]}"(qa1), "{v[120:123]}"(qb1), "{v[124:125]}"(qa2), "{v[126:127]}"(qb2), "{v128}"(ak1), "{v129}"(ak2), "{v130}"(av0), "{v134}"(av1)
    : "memory", "vcc", "scc", "s40", "s41", "v40","v41","v42","v43","v44","v45","v46","v47","v48","v49","v50","v51","v52","v53","v54","v55","v56","v57","v58","v59","v60","v61","v62","v63",
      "v64","v65","v66","v67","v72","v73","v74","v75","v76","v77","v78","v79",
      "v102","v103","v104","v105","v106","v107","v108","v109","v110","v111","v112","v113","v114","v115","v132","v133");
  STAMPT(1, 2);
  const rsrc_t ro = mkrsrc(ws + WS_O, S * D * 2);
  auto put = [&](const f32x16& o, int u) {
    const float il = __builtin_amdgcn_rcpf(hh ? o[8] : o[12]);
    st16(ro, ooff0 + u * 40960, u32x4{pk2(o[0] * il, o[1] * il), pk2(o[2] * il, o[3] * il), pk2(o[4] * il, o[5] * il), pk2(o[6] * il, o[7] * il)});
    st8s(ro, ooff1, u * 40960, u32x2{pk2(o[8] * il, o[9] * il), pk2(o[10] * il, o[11] * il)});
  };
  put(oa, 0); put(ob, 1);
  SBAR();
  const bool fa = attn_unsafe(oa, hh), fb = attn_unsafe(ob, hh);
  if (__builtin_expect(fa, 0)) { WAITV(0); put(attn_online(kv, qa1, qa2, lane, k2off, voff), 0); }
  if (__builtin_expect(fb, 0)) { WAITV(0); put(attn_online(kv, qb1, qb2, lane, k2off, voff), 1); }
  STAMPT(1, 3);
}

#define P5_SX 3
#define P5_SY 8
template <int NC> struct GS { f32x4 acc[2][NC]; };
template <int NC, int NKS, int NFULL, int PD, int G, int SSTRIDE, int RING, bool BARS, class Mid> DI void gemm_run(GS<NC>& g, const LAS char* wl, rsrc_t ra, unsigned va0, unsigned va1, unsigned astep, Mid mid) {
  bf16x8 af[PD][2], bf[2][NC];
  const unsigned l8 = (threadIdx.x & 63) * 8;
  auto lda = [&](int s, int i) { const unsigned v = i ? va1 : va0; return s < NFULL ? ldfrag(ra, v, s * astep) : cat8(ldhalf(ra, v - l8, s * astep), ldhalf(ra, v - l8 + 512, s * astep)); };
  const LAS char* wb[3] = {wl, wl + 61440, wl + 122880};
  asm volatile("" : "+v"(wb[0]), "+v"(wb[1]), "+v"(wb[2]), "+v"(va0), "+v"(va1));
  auto rdw = [&](int off) { return *(const LAS bf16x8*)(wb[off / 61440] + off % 61440); };
  mid(-2);
  SBAR();
#pragma unroll
  for (int s = 0; s < G; ++s) { af[s][0] = lda(s, 0); af[s][1] = lda(s, 1); SBAR(); }
  mid(-1);
#pragma unroll
  for (int j = 0; j < NC; ++j) bf[0][j] = rdw(j * 1024);
  int nl = G;
#pragma unroll
  for (int s = 0; s < NKS; ++s) {
    LGKM0();
    mid(s);
    SBAR();
    int lim = s + PD < NKS ? s + PD : NKS;
    if (BARS) { const int rdy = s < P5_SX ? 8 : (s < P5_SY ? 16 : NKS); lim = lim < rdy ? lim : rdy; }
    const int l0 = nl < lim ? nl : -1, l1 = (nl + 1 < lim) ? nl + 1 : -1;
    nl = l1 >= 0 ? nl + 2 : (l0 >= 0 ? nl + 1 : nl);
#pragma unroll
    for (int m = 0; m < 2 * NC; ++m) {
      mfma16_acc(g.acc[m & 1][m >> 1], bf[s & 1][m >> 1], af[s % PD][m & 1]); SBAR();
      if (s + 1 < NKS && m < NC) { bf[(s + 1) & 1][m] = rdw(((s + 1) % RING) * SSTRIDE + m * 1024); SBAR(); }
      if (l0 >= 0 && (m == NC || m == 2 * NC - 1)) { af[l0 % PD][m != NC] = lda(l0, m != NC); SBAR(); }
      if (l1 >= 0 && (m == 1 || m == NC + 2)) { af[l1 % PD][m != 1] = lda(l1, m != 1); SBAR(); }
    }
  }
  asm volatile("s_nop 7" ::: "memory"); SBAR();
}
template <int NC> DI void pair_reduce(GS<NC>& g, char* smem, int wid) {
  const int lane = threadIdx.x & 63;
  LAS f32x4* red = (LAS f32x4*)(smem + 143360) + lane;
#pragma unroll
  for (int j = 0; j < NC; ++j) red[(wid * NC + j) * 64] = g.acc[1][j];
  LGKM0(); BAR();
#pragma unroll
  for (int j = 0; j < NC; ++j) g.acc[0][j] += red[((wid ^ 2) * NC + j) * 64];
}
DI void store_tiles5(rsrc_t r, unsigned o16, unsigned o8, const u32x2 (&v)[5]) {
  st16(r, o16, u32x4{v[0][0], v[0][1], v[1][0], v[1][1]}); st16(r, o16 + 1024, u32x4{v[2][0], v[2][1], v[3][0], v[3][1]});
  st8(r, o8, v[4]);
}

#define P4_PD 7
#define P5_PD 8
#define STN 1
#define NVA 2
#define NVB 5
#define STSL 3
#define NPRE 14
#define NW4 12
#define NB4 32
#define G3 8
#define GATE5 4
#define NVC (2 * GATE5 + 1)
#define GATE 4
#define CBAR 13
#define PACE0 2
#define W2EARLY 1
constexpr int LDS_WO = 61440, LDS_CB = 162816, LDS_RS = 161792, LDS_BYTES = 163840;

struct Pre { u32x2 x[5], b2[5]; };
DI void post_prefetch(Pre& p, const Args& a, int wid) {
  const int lane = threadIdx.x & 63, b = blockIdx.x, c = b >> 5;
  const int tok = (((b & 7) * 4 + ((b >> 3) & 3)) * 4 + (wid & 1) * 2 + (wid >> 1)) * 16 + (lane & 15);
#pragma unroll
  for (int j = 0; j < 5; ++j) { p.x[j] = *(const u32x2*)(a.x + (size_t)tok * D + c * 80 + j * 16 + (lane >> 4) * 4); p.b2[j] = *(const u32x2*)(a.b2 + c * 80 + j * 16 + (lane >> 4) * 4); }
}
DI void post_compute(const Args& a, char* ws, char* smem, int wid, const Pre& pre, u64 tag, u64 t_start) {
  const int lane = threadIdx.x & 63, b = blockIdx.x;
  const int grp = (b & 7) * 4 + ((b >> 3) & 3), c = b >> 5, rh = wid & 1, kh = wid >> 1;
  const int rt = grp * 4 + rh * 2 + kh, tok = rt * 16 + (lane & 15);
  u32x2 hv[5];
  {
    GS<5> g;
#pragma unroll
    for (int j = 0; j < 5; ++j) { g.acc[0][j] = unpack4(pre.x[j]); asm volatile("" : "+v"(g.acc[0][j])); }
    const rsrc_t ra = mkrsrc(ws + WS_O, S * D * 2);
    unsigned va = rt * 20480 + lane * 16, vh = rt * 20480 + lane * 8;
    unsigned ho16 = (rt * 20 + 2 * c) * 1024 + lane * 16, ho8 = (rt * 20 + 16) * 1024 + c * 512 + lane * 8, so8 = lane < 16 ? (rt * 8 + c) * 256 + lane * 16 : S * 128;
    const LAS char* wb[2] = {(const LAS char*)smem + LDS_WO + lane * 16, (const LAS char*)smem + LDS_WO + 51200 + lane * 16};
    asm volatile("" : "+v"(ho16), "+v"(ho8), "+v"(so8), "+v"(va), "+v"(vh), "+v"(wb[0]), "+v"(wb[1]));
    bf16x8 af[20], bf[3][5];
    auto rdw = [&](int s, int j) { return *(const LAS bf16x8*)(wb[s / 10] + (s % 10) * 5120 + j * 1024); };
    auto lda = [&](int s) { return s < 16 ? ldfrag(ra, va, s * 1024) : cat8(ldhalf(ra, vh, s * 1024), ldhalf(ra, vh + 512, s * 1024)); };
#pragma unroll
    for (int j = 0; j < 10; ++j) bf[j / 5][j % 5] = rdw(j / 5, j % 5);
    SBAR();
    handoff<0, 3>(ws, (CT_O + (c * 4 + wid) * 2 + lane) * 128, (CT_O + grp * 2 + rh) * 128, tag + 32);
    STAMPT(2, 0);
    SBAR();
#pragma unroll
    for (int s = 0; s < G3; ++s) { af[s] = lda(s); SBAR(); }
#pragma unroll
    for (int s = 0; s < 20; ++s) {
#pragma unroll
      for (int m = 0; m < 5; ++m) {
        mfma16_acc(g.acc[0][m], bf[s % 3][m], af[s]); SBAR();
        if (s + 2 < 20) { bf[(s + 2) % 3][m] = rdw(s + 2, m); SBAR(); }
        if (m < 2 && G3 + 2 * s + m < 20) { af[G3 + 2 * s + m] = lda(G3 + 2 * s + m); SBAR(); }
      }
    }
    asm volatile("s_nop 7" ::: "memory"); SBAR();
    STAMPT(2, 1);
#pragma unroll
    for (int j = 0; j < 5; ++j) hv[j] = pack4(g.acc[0][j]);
    store_tiles5(mkrsrc(ws + WS_H, S * D * 2), ho16, ho8, hv);
    BAR(); SBAR();
    f32x4 s1 = g.acc[0][0], s2 = g.acc[0][0] * g.acc[0][0];
#pragma unroll
    for (int j = 1; j < 5; ++j) { s1 += g.acc[0][j]; s2 += g.acc[0][j] * g.acc[0][j]; }
    const float t1 = xq_sum((s1[0] + s1[1]) + (s1[2] + s1[3])), t2 = xq_sum((s2[0] + s2[1]) + (s2[2] + s2[3]));
    st16(mkrsrc(ws + WS_ST, S * 128), so8, u32x4{__float_as_uint(t1), (unsigned)tag, __float_as_uint(t2), (unsigned)(tag >> 32)});
    STAMPT(2, 2);
  }
  {
    const int g4 = (b & 7) * 2 + ((b >> 4) & 1), c4 = ((b >> 5) << 1) | ((b >> 3) & 1);
    GS<7> g;
#pragma unroll
    for (int j = 0; j < 7; ++j) { g.acc[0][j] = f32x4{}; g.acc[1][j] = f32x4{}; asm volatile("" : "+v"(g.acc[0][j]), "+v"(g.acc[1][j])); }
    const rsrc_t ra = mkrsrc(ws + WS_H, S * D * 2);
    const int rt0 = g4 * 8 + wid * 2;
    const unsigned va0 = rt0 * 20480 + lane * 16;
    PADT(4, 70);
    f32x4 cs[7], bv[7]; f32x2 rs[2];
    const LAS f32x4* cb = (const LAS f32x4*)(smem + LDS_CB) + (lane >> 4);
    unsigned go16 = (rt0 * 54 + c4) * 1024 + lane * 16, go8 = (c4 & 3) ? (rt0 * 54 + 48) * 1024 + (c4 - 1 - (c4 >> 2)) * 512 + lane * 8 : S * F * 2;
    asm volatile("" : "+v"(go16), "+v"(go8));
    gemm_run<7, 20, 16, P4_PD, GATE, 7168, 20, false>(g, (const LAS char*)smem + lane * 16, ra, va0, va0 + 20480, 1024, [&](int s) {
      if (s == -2) { handoff<STN, 1>(ws, (CT_H + (rt >> 1)) * 128, (CT_H + (rt0 >> 1)) * 128, tag + 16); STAMPT(3, 0); }
      if (s == -1) BAR();
      if (s == CBAR) BAR();
      if (s == 18) {
#pragma unroll
        for (int j = 0; j < 7; ++j) { cs[j] = cb[j * 4]; bv[j] = cb[28 + j * 4]; }
#pragma unroll
        for (int i = 0; i < 2; ++i) rs[i] = *(const LAS f32x2*)(smem + LDS_RS + (wid * 32 + i * 16 + (lane & 15)) * 8);
      }
      if (s == 19) BAR();
    });
    STAMPT(3, 1);
    const rsrc_t ro = mkrsrc(ws + WS_H1, S * F * 2);
    unsigned ca = (CT_H1 + (rt0 >> 1)) * 128;
    asm volatile("" : "+v"(ca));
#pragma unroll
    for (int p = 0; p < 4; ++p) {
      const int n = p < 3 ? 4 : 2;
      f32x4 v[4], t[4];
#define GSTAGE(expr) _Pragma("unroll") for (int e = 0; e < 4; ++e) if (e < n) { expr; } SBAR()
      GSTAGE(t[e] = cs[2 * p + (e >> 1)] * rs[e & 1][0] + bv[2 * p + (e >> 1)]);
      GSTAGE(v[e] = g.acc[e & 1][2 * p + (e >> 1)] * rs[e & 1][1] + t[e]);
      GSTAGE(t[e] = v[e] * v[e]);
      GSTAGE(t[e] = t[e] * -0.1029432397f + -2.302208198f);
      GSTAGE(t[e] = t[e] * v[e]);
      GSTAGE(t[e] = (f32x4{EX(t[e][0]), EX(t[e][1]), EX(t[e][2]), EX(t[e][3])}));
      if (p == 2) { drain_arrive<NVA>(ws, ca); SBAR(); }
      GSTAGE(t[e] = t[e] + 1.f);
      GSTAGE(t[e] = (f32x4{__builtin_amdgcn_rcpf(t[e][0]), __builtin_amdgcn_rcpf(t[e][1]), __builtin_amdgcn_rcpf(t[e][2]), __builtin_amdgcn_rcpf(t[e][3])}));
      GSTAGE(t[e] = t[e] * v[e]);
#pragma unroll
      for (int i = 0; i < 2; ++i) {
        if (p < 3) { const u32x2 lo = pack4(t[i]), hi = pack4(t[2 + i]); st16(ro, go16 + p * 16384 + i * 55296, u32x4{lo[0], lo[1], hi[0], hi[1]}); }
        else st8(ro, go8 + i * 55296, pack4(t[i]));
      }
      asm volatile("" ::: "memory"); SBAR();
    }
    drain_arrive<NVB>(ws, ca + 64 * 128);
    STAMPT(3, 2);
  }
  {
    GS<5> g;
#pragma unroll
    for (int j = 0; j < 5; ++j) { g.acc[0][j] = unpack4(pre.b2[j]) + unpack4(hv[j]); g.acc[1][j] = f32x4{}; }
#pragma unroll
    for (int j = 0; j < 5; ++j) asm volatile("" : "+v"(g.acc[0][j]), "+v"(g.acc[1][j]));
    const rsrc_t ra = mkrsrc(ws + WS_H1, S * F * 2);
    const unsigned va0 = (rt * 54 + kh) * 1024 + lane * 16, va1 = ((rt ^ 1) * 54 + kh) * 1024 + lane * 16;
    PADT(5, 80);
    unsigned cl = (CT_H1 + 128 + ((b & 7) * 2 + ((b >> 4) & 1)) * 4 + wid) * 128;
    asm volatile("" : "+v"(cl));
    gemm_run<5, 27, 24, P5_PD, GATE5, 10240, 16, true>(g, (const LAS char*)smem + kh * 5120 + lane * 16, ra, va0, va1, 2048, [&](int s) {
      if (s == -2) { BAR(); STAMPT(4, 0); }
      if (s == -1) drain_arrive<NVC>(ws, cl);
      if (s == P5_SX) BAR();
      if (s == P5_SY) BAR();
      if (s == 15 || s == 21) BAR();
    });
    STAMPT(4, 1);
    pair_reduce<5>(g, smem, wid);
#pragma unroll
    for (int j = 0; j < 5; ++j) *(u32x2*)(a.out + (size_t)tok * D + c * 80 + j * 16 + (lane >> 4) * 4) = pack4(g.acc[0][j]);
    STAMPT(4, 2);
  }
}

template <class Sync> DI void main_helper(const Args& a, char* ws, char* smem, int head, int hw, int bn, u64 tag, Sync sync) {
  hw &= 3;
  const int lane = threadIdx.x & 63, b = blockIdx.x;
  const char* kv = ws + WS_KV + (size_t)head * 64 * KVT;
  const unsigned lds0 = (unsigned)(uintptr_t)smem;
  const int c = b >> 5, c4 = ((b >> 5) << 1) | ((b >> 3) & 1), gct0 = (c4 * 27) >> 2;
  unsigned v16 = lane * 16, v16h = lane * 16 + hw * 1024, ctl = lds0 + CTL2, one = 1, v8 = lane * 8;
  Retile rt; retile_plan(rt, (const LAS char*)smem + LDS_RT, hw);
  u32x4 ones = {0x3F803F80u, 0x3F803F80u, 0x3F803F80u, 0x3F803F80u};
  const int cn = gct0 * 16 + (lane < 28 ? lane : (lane < 56 ? lane - 28 : 0)) * 4;
  unsigned cno = lane < 28 ? cn * 4 : 4 * 1792 * 4;
  f32x4 cst = unpack4(*(const u32x2*)(a.b1 + cn));
  if (lane < 28) cst = f32x4{};
  u64 tagv = tag;
  unsigned c2 = (CT_KV2 + bn * 8 + lane) * 128;
  const int ci2 = CT_KV2 + (5 + ((lane >> 1) & 1) * 5 + ((head * 20 + (lane & 1) * 19) >> 7)) * 8 + c;
  asm volatile("" : "+v"(v16), "+v"(v16h), "+v"(ctl), "+v"(one), "+v"(v8), "+v"(ones), "+v"(cno), "+v"(cst), "+v"(tagv), "+v"(c2));
  const char* rsrc0 = (const char*)(b < 40 ? a.wo : (b < 148 ? a.w1 : a.w2));
  auto issue = [&](int ch) {
#pragma unroll
    for (int i = 0; i < 6; ++i) { const int pc = hw + 4 * i; if (pc < CH_PIECES) glds16x(v16, kv + (size_t)ch * CH_BYTES + pc * 1024, lds0 + (ch & 1) * CH_BYTES + pc * 1024); }
  };
#define REFILL(ch) do { lds_wait<64 + ((ch) - 2) * 4, NAP2>(ctl); issue(ch); WAITV(0); lds_post<(ch) * 4>(ctl, one); STAMPH(6, ch); } while (0)
  sync();
  issue(0);
  __builtin_amdgcn_s_setprio(3);
  WAITV(0); lds_post<0>(ctl, one);
  if (hw == 3 && bn >= 5 && bn < 15) asm volatile("s_mov_b64 exec, 0xff\n\tglobal_atomic_add_x2 %0, %1, %2\n\ts_mov_b64 exec, -1" :: "v"(c2), "v"(1ull), "s"(ws + WS_FL + CT_BASE * 8) : "memory");
  issue(1);
#pragma unroll
  for (int i = 0; i < 5; ++i) glds16(rt.rv[i], rsrc0, lds0 + LDS_RT + (hw * 5 + i) * 1024);
  WAITV(5); lds_post<4>(ctl, one);
  WAITV(0); lds_post<56>(ctl, one);
  REFILL(2);
  lds_wait<56, 2>(ctl);
  retile_store(rt, ws, __builtin_bit_cast(bf16x8, ones));
  if (hw < 2 && b < 160) asm volatile("s_mov_b64 exec, 1\n\tglobal_store_dwordx2 %0, %1, %2 sc1\n\ts_mov_b64 exec, -1" :: "v"(v8), "v"(tagv), "s"(ctrp(ws, CT_O + 2 * b + hw)) : "memory");
  WAITV(0); lds_post<60>(ctl, one);
  await(ws, ci2, 4, tag + 16);
  REFILL(3);
  if (hw == 0) {
    lds_wait<60, 1>(ctl);
    asm volatile("s_mov_b64 exec, 1\n\tglobal_store_dwordx2 %0, %1, %2 sc1\n\ts_mov_b64 exec, -1" :: "v"(v8), "v"(tagv), "s"(flagp(ws, FL_W + b)) : "memory");
  }
  REFILL(4);
  {
    const rsrc_t rf = mkrsrc(ws + WS_FL + FL_W * 8, 2048);
#pragma unroll 1
    for (unsigned sp = 0; sp < SPIN_CAP; ++sp) {
      asm volatile("" ::: "memory");
      const u32x4 f0 = __builtin_amdgcn_raw_buffer_load_b128(rf, v16, 0, XAUX), f1 = __builtin_amdgcn_raw_buffer_load_b128(rf, v16, 1024, XAUX);
      typedef __attribute__((ext_vector_type(2))) u64 u64x2;
      const u64x2 g0 = __builtin_bit_cast(u64x2, f0), g1 = __builtin_bit_cast(u64x2, f1);
      if (__all(g0[0] == tag && g0[1] == tag && g1[0] == tag && g1[1] == tag)) break;
      __builtin_amdgcn_s_sleep(8);
    }
    asm volatile("" ::: "memory");
  }
  if (b == 0 && hw == 0 && lane == 0) signal(ws, FL_EPOCH, tag);
  f32x4 ct[4];
  if (hw == 0) {
    const rsrc_t rc = mkrsrc(ws + WS_CS1, 4 * 1792 * 4);
#pragma unroll
    for (int k = 0; k < 4; ++k) ct[k] = __builtin_bit_cast(f32x4, __builtin_amdgcn_raw_buffer_load_b128(rc, cno, k * 7168, XAUX));
    cst += (ct[0] + ct[1]) + (ct[2] + ct[3]);
    asm volatile("" : "+v"(cst));
  }
  const char* wo = ws + WS_WO + (size_t)c * 102400;
  auto wo_dma = [&](int i0, int i1) {
#pragma unroll 1
    for (int i = i0; i < i1; ++i) { const int p = i * 4 + hw; glds16x(v16, wo + p * 1024, lds0 + LDS_WO + p * 1024); }
  };
  bf16x8 wa[35], wb[20];
  const rsrc_t r1 = mkrsrc(ws + WS_W1 + (size_t)gct0 * 20480, 7 * 20480), r2 = mkrsrc(ws + WS_W2 + (size_t)c * 276480, 276480);
  auto ld1 = [&](int i) { return ldfrag(r1, v16h, ((i % 7) * 20 + (i / 7) * 4) * 1024); };
  auto ld2 = [&](int i) { return ldfrag(r2, v16h, i * 4096); };
  wo_dma(0, 8);
  REFILL(5);
  wo_dma(8, 25);
  REFILL(6);
  lds_post<32>(ctl, one);
#pragma unroll
  for (int i = 0; i < 20; ++i) wa[i] = ld1(i);
  SBAR();
  REFILL(7);
#pragma unroll
  for (int i = 20; i < 35; ++i) wa[i] = ld1(i);
#pragma unroll
  for (int i = 0; i < 20; ++i) wb[i] = ld2(i);
  SBAR();
  lds_wait<64 + 28, NAP2>(ctl);
  __builtin_amdgcn_s_setprio(0);
  LAS bf16x8* lw = (LAS bf16x8*)smem + hw * 7 * 64 + lane;
#pragma unroll
  for (int i = 0; i < 14; ++i) lw[((i / 7) * 28 + i % 7) * 64] = wa[i];
  LGKM0(); SBAR();
#pragma unroll
  for (int i = 0; i < 14; ++i) { wa[i] = ld2(20 + i); SBAR(); __builtin_amdgcn_s_sleep(PACE0); SBAR(); }
  BAR();
#pragma unroll
  for (int i = 14; i < 35; ++i) lw[((i / 7) * 28 + i % 7) * 64] = wa[i];
  LGKM0();
  BAR();
  if (hw == 0 && lane < 56) *((LAS f32x4*)(smem + LDS_CB) + lane) = cst;
  {
    const rsrc_t rst = mkrsrc(ws + WS_ST, S * 128);
    const unsigned so = (((((b & 7) * 2 + ((b >> 4) & 1)) * 4 + hw) * 2 + ((lane >> 4) & 1)) * 8 + (lane >> 5) * 4) * 256 + (lane & 15) * 16;
    u32x4 p[4];
    __builtin_amdgcn_s_sleep(STSL);
#pragma unroll 1
    for (unsigned sp = 0; sp < SPIN_CAP; ++sp) {
      asm volatile("" ::: "memory");
      bool ok = true;
#pragma unroll
      for (int k = 0; k < 4; ++k) p[k] = __builtin_amdgcn_raw_buffer_load_b128(rst, so + k * 256, 0, XAUX);
#pragma unroll
      for (int k = 0; k < 4; ++k) ok &= p[k][1] == (unsigned)tag && p[k][3] == (unsigned)(tag >> 32);
      if (__all(ok)) break;
      __builtin_amdgcn_s_sleep(4);
    }
    const float m = xhalf_sum((__uint_as_float(p[0][0]) + __uint_as_float(p[1][0])) + (__uint_as_float(p[2][0]) + __uint_as_float(p[3][0]))) * (1.f / D);
    const float var = fmaxf(xhalf_sum((__uint_as_float(p[0][2]) + __uint_as_float(p[1][2])) + (__uint_as_float(p[2][2]) + __uint_as_float(p[3][2]))) * (1.f / D) - m * m, 0.f);
    const float r1 = __builtin_amdgcn_rsqf(var + 1e-5f);
    const float rstd = r1 * __builtin_amdgcn_rsqf(var * r1 * r1 + 1e-5f);
    if (lane < 32) *(LAS f32x2*)(smem + LDS_RS + (hw * 32 + lane) * 8) = f32x2{-m * rstd, rstd};
  }
  LGKM0(); BAR();
  LAS bf16x8* l2 = (LAS bf16x8*)smem + hw * 64 + lane;
  static_assert(CBAR * 7168 >= NW4 * 4096, "");
#pragma unroll
  for (int i = W2EARLY ? 14 : 0; i < NB4; ++i) { wa[i] = ld2(20 + i); if (i - 14 < NW4) l2[(i - 14) * 256] = wb[i - 14]; SBAR(); }
  BAR();
#pragma unroll
  for (int i = NB4; i < 35; ++i) wa[i] = ld2(20 + i);
  SBAR();
#pragma unroll
  for (int i = NW4; i < 20; ++i) l2[i * 256] = wb[i];
  LGKM0(); SBAR();
#pragma unroll
  for (int i = 0; i < 13; ++i) wb[i] = ld2(55 + i);
  SBAR();
#pragma unroll
  for (int i = 0; i < NPRE; ++i) l2[(20 + i) * 256] = wa[i];
  SBAR();
  const unsigned cw = (CT_H1 + ((b & 7) * 4 + ((b >> 3) & 3)) * 2 + (hw & 1)) * 128;
  if (hw < 2) poll(ws, cw, tag + 16);
  BAR();
#pragma unroll
  for (int i = NPRE; i < 20; ++i) l2[(20 + i) * 256] = wa[i];
  SBAR();
  if (hw >= 2) poll(ws, cw + 64 * 128, tag + 16);
  LGKM0(); BAR();
  if (hw < 2) poll(ws, cw + 128 * 128, tag + 16);
  BAR();
#pragma unroll
  for (int i = 0; i < 15; ++i) l2[i * 256] = wa[20 + i];
  LGKM0(); BAR();
#pragma unroll
  for (int i = 0; i < 13; ++i) l2[(15 + i) * 256] = wb[i];
  LGKM0(); BAR();
  BAR();
}

__global__ __launch_bounds__(512, 2) void fused_block(Args a) {
  extern __shared__ __attribute__((aligned(16))) char smem[];
  typedef __attribute__((ext_vector_type(4))) u64 u64x4;
  u64x4 p01;
  asm volatile("s_load_dwordx8 %0, %1, 0x0" : "=s"(p01) : "s"(__builtin_amdgcn_kernarg_segment_ptr()));
  {
    u64 pc; asm volatile("s_getpc_b64 %0" : "=s"(pc));
    const unsigned off = min((int)((((blockIdx.x >> 3) & 7) + (threadIdx.x & ~7u)) * 128), CODE_BYTES);
    asm volatile("s_mov_b32 m0, %2\n\ts_nop 0\n\tglobal_load_lds_dword %0, %1" :: "v"(off), "s"(pc & ~127ull), "s"(__builtin_amdgcn_readfirstlane((unsigned)(uintptr_t)smem + 140032)) : "memory");
  }
  const int wid = __builtin_amdgcn_readfirstlane(threadIdx.x >> 6), lane = threadIdx.x & 63;
  const int b = blockIdx.x, xg = b & 7, j = b >> 3;
  const u64 t_start = PADAT >= 0 ? __builtin_amdgcn_s_memrealtime() : 0;
  const int bm = (xg >> 1) * 4 + (j & 3), bn = (xg & 1) * 8 + (j >> 2);
#define K1_ROT ((j & 1) * 5)
  const int head = (b & 7) * 4 + ((b >> 3) & 3), qc = b >> 5;
  K1Src k1;
  k1.a0 = (bm * 128 + (lane >> 3)) * 1280 + (((lane & 7) ^ (lane >> 4)) << 4); k1.a1 = k1.a0 ^ 64;
  k1.b0 = (lane >> 4) * 3840 + (((((lane & 15) >> 2) ^ (lane >> 4)) * 4 + (lane & 3)) << 4) + bn * 256; k1.b1 = k1.b0 ^ 32;
  if (wid == 0 && lane < 32) ((LAS unsigned*)(smem + CTL1))[lane] = 0;
  asm volatile("" : "+v"(k1.a0), "+v"(k1.a1), "+v"(k1.b0), "+v"(k1.b1));
  asm volatile("s_waitcnt lgkmcnt(0)\n\ts_barrier" : "+s"(p01) :: "memory");
  char* ws = (char*)p01[2];
  u64 ep;
  asm volatile("s_load_dwordx2 %0, %1, 0x0" : "=s"(ep) : "s"(ws + WS_FL + FL_EPOCH * 8) : "memory");
  XRow xr;
  if (wid < 4) {
    xrow_load(xr, (const u16*)p01[0], b * 8 + wid * 2);
  }
  k1.ra = mkrsrc((const void*)p01[0], S * D * 2); k1.rb = mkrsrc((const void*)p01[1], 3 * D * D * 2);
  if (bn < 15 && wid >= 4) {
#pragma unroll 1
    for (int i = 4; i < wid; ++i) __builtin_amdgcn_s_sleep(STAG);
    k1_stage(k1, (wid - 4 + (K1_ROT)) % 10, (unsigned)(uintptr_t)smem + (wid - 4) * K1_STAGE);
  }
  u64 tag = 0;
  if (__builtin_expect(bn >= 15, 0)) tag = k1_colsum((const u16*)p01[1], ws, smem, bm, wid, xr, ep);
  if (wid >= 4) {
    if (bn < 15) k1_helper(k1, ws, smem, bm, bn, K1_ROT, wid - 4, ep, tag);
    const int t0 = (head * 20) >> 7, t1 = (head * 20 + 19) >> 7;
    const int ci = CT_QKV + (min(lane >> 1, 2) * 5 + ((lane & 1) ? t1 : t0)) * 8 + qc;
    main_helper(a, ws, smem, head, wid - 4, bn, tag, [&]() {
      WAITV(0); BAR();
      if (wid == 4) await(ws, ci, 6, tag + (lane < 2 ? 2 : 16));
      BAR();
    });
  } else {
    auto publish = [&]() {
      STAMPT(0, 3);
      WAITV(0); BAR();
      if (wid == 0 && bn < 15 && (bn < 5 ? lane == (bm >> 1) : lane < 8)) arrive(ws, CT_QKV + bn * 8 + lane);
      STAMPT(0, 4);
    };
    if (bn < 10) k1_compute<false>(publish, ws, smem, bm, bn, wid, xr, ep, tag, t_start);
    else if (bn < 15) k1_compute<true>(publish, ws, smem, bm, bn, wid, xr, ep, tag, t_start);
    else publish();
    if (wid == 3 && lane < 32) { ((LAS unsigned*)(smem + CTL2))[lane] = 0; LGKM0(); }
    asm volatile("" ::: "memory"); SBAR();
    Pre pre; post_prefetch(pre, a, wid);
    attn_compute(ws, smem, head, qc, wid, [&]() {
      PADT(1, 30);
      WAITV(10); BAR();
      STAMPT(1, 0);
    });
    PADT(2, 60); post_compute(a, ws, smem, wid, pre, tag, t_start); PADT(0, 100);
  }
}

extern "C" void kernel_launch(void* const* d_in, const int* in_sizes, int n_in, void* d_out, int out_size, void* d_ws, size_t ws_size, hipStream_t stream) {
  Args a;
  a.x = (const u16*)d_in[0]; a.wqkv = (const u16*)d_in[1]; a.wo = (const u16*)d_in[2];
  a.w1 = (const u16*)d_in[4]; a.b1 = (const u16*)d_in[5]; a.w2 = (const u16*)d_in[6]; a.b2 = (const u16*)d_in[7];
  a.out = (u16*)d_out; a.ws = (char*)d_ws;
  (void)hipFuncSetAttribute((const void*)fused_block, hipFuncAttributeMaxDynamicSharedMemorySize, LDS_BYTES);
  fused_block<<<256, 512, LDS_BYTES, stream>>>(a);
}
```
